```python
import torch

def _softmax(x, axis=-1):
    return torch.softmax(x, dim=axis)

def _gelu(x):
    return torch.nn.functional.gelu(x, approximate='tanh')

def _silu(x):
    return torch.nn.functional.silu(x)

def _sigmoid(x):
    return torch.sigmoid(x)

def _layernorm(x, axis=-1, eps=1e-05):
    m = x.mean(dim=axis, keepdim=True)
    v = x.var(dim=axis, keepdim=True, unbiased=False)
    return (x - m) / torch.sqrt(v + eps)

def _scatter_add(tgt, idx, src, axis=0):
    return tgt.clone().index_add_(axis, idx, src)

def _segment_sum(src, seg, n):
    out = torch.zeros((n,) + src.shape[1:], dtype=src.dtype)
    return out.index_add_(0, seg, src)

def _pad(x, pw):
    flat = tuple((v for pair in reversed(pw) for v in pair))
    return torch.nn.functional.pad(x, flat)
kernel_name = 'adaLN_cond__memory_aug_recurrent_mem_tr__glu_gated_bilinear__prompt'

def setup_inputs():

    _w = lambda a: (lambda t: t.to(torch.bfloat16) if t.is_floating_point() else t)(torch.as_tensor(a))

    def _A_setup():
        B, S, Dm = (1, 2048, 512)
        H, Dh = (32, 128)
        M, seg = (8, 32)
        seg = min(seg, S)
        S = S // seg * seg
        return {'x': _w((lambda *s: torch.randn(*s, dtype=torch.bfloat16) * 0.05)(B, S, Dm)), 'mem0': _w((lambda *s: torch.randn(*s, dtype=torch.bfloat16) * 0.8)(B, M, Dm)), 'Wq': _w((lambda *s: torch.randn(*s, dtype=torch.bfloat16) * 1.6)(Dm, H, Dh) * Dm ** (-0.5)), 'Wk': _w((lambda *s: torch.randn(*s, dtype=torch.bfloat16) * 1.6)(Dm, H, Dh) * Dm ** (-0.5)), 'Wv': _w((lambda *s: torch.randn(*s, dtype=torch.bfloat16) * 0.8)(Dm, H, Dh) * Dm ** (-0.5)), 'Wo': _w((lambda *s: torch.randn(*s, dtype=torch.bfloat16) * 0.8)(H, Dh, Dm) * (H * Dh) ** (-0.5)), 'seg_len': seg}
    _A_in = _A_setup()

    def _B_setup():
        B, S, D, F = (1, 2048, 512, 1792)
        return {'x': _w((lambda *s: torch.randn(*s, dtype=torch.bfloat16) * 0.05)(B, S, D)), 'w_gate': _w((lambda *s: torch.randn(*s, dtype=torch.bfloat16) * 0.8)(D, F) * D ** (-0.5)), 'w_up': _w((lambda *s: torch.randn(*s, dtype=torch.bfloat16) * 0.8)(D, F) * D ** (-0.5)), 'w_down': _w((lambda *s: torch.randn(*s, dtype=torch.bfloat16) * 0.8)(F, D) * F ** (-0.5))}
    _B_in = _B_setup()
    return {'A_x': _A_in['x'], 'A_mem0': _A_in['mem0'], 'A_Wq': _A_in['Wq'], 'A_Wk': _A_in['Wk'], 'A_Wv': _A_in['Wv'], 'A_Wo': _A_in['Wo'], 'A_seg_len': _A_in['seg_len'], 'B_x': _B_in['x'], 'B_w_gate': _B_in['w_gate'], 'B_w_up': _B_in['w_up'], 'B_w_down': _B_in['w_down'], 'c': _w((lambda *s: torch.randn(*s, dtype=torch.bfloat16) * 0.1)(1, 512) * 8.0), 'w_mod': _w((lambda *s: torch.randn(*s, dtype=torch.bfloat16) * 0.1)(512, 6 * 512) * 512 ** (-0.5) * 8.0)}

def reference(A_x, A_mem0, A_Wq, A_Wk, A_Wv, A_Wo, A_seg_len, B_x, B_w_gate, B_w_up, B_w_down, c, w_mod):

    def _A_ref(x, mem0, Wq, Wk, Wv, Wo, seg_len):
        B, S, Dm = x.shape
        H, Dh = (Wq.shape[1], Wq.shape[2])
        M = mem0.shape[1]
        scale = Dh ** (-0.5)
        n_seg = S // seg_len
        L = M + seg_len
        idx = torch.arange(L)
        mask = (idx[:, None] < M) | (idx[:, None] >= idx[None, :])
        mem = mem0
        outs = []
        for s in range(n_seg):
            xs = x[:, s * seg_len:(s + 1) * seg_len, :].clone()
            h = torch.concatenate([mem, xs], axis=1)
            q = torch.einsum('btd,dhe->bhte', h, Wq)
            k = torch.einsum('btd,dhe->bhte', h, Wk)
            v = torch.einsum('btd,dhe->bhte', h, Wv)
            scores = torch.einsum('bhqe,bhke->bhqk', q, k) * scale
            scores = torch.where(mask[None, None], scores, -1000000000.0)
            p = _softmax(scores, axis=-1)
            o = torch.einsum('bhqk,bhke->bhqe', p, v)
            o = torch.einsum('bhte,hed->btd', o, Wo)
            mem = o[:, :M, :].clone()
            outs.append(o[:, M:, :])
        return torch.concatenate(outs, axis=1)

    def _B_ref(x, w_gate, w_up, w_down):
        g = torch.einsum('bsd,df->bsf', x, w_gate)
        u = torch.einsum('bsd,df->bsf', x, w_up)
        h = g * u
        return torch.einsum('bsf,fd->bsd', h, w_down)
    x = A_x
    mod = (_silu(c) @ w_mod).reshape(c.shape[0], 6, 1, -1)
    sa, ca, ga, sb, cb, gb = (mod[:, 0].clone(), mod[:, 1].clone(), mod[:, 2].clone(), mod[:, 3].clone(), mod[:, 4].clone(), mod[:, 5].clone())
    h = _layernorm(x, axis=-1) * (1 + ca) + sa
    x = x + ga * _A_ref(h, A_mem0, A_Wq, A_Wk, A_Wv, A_Wo, A_seg_len)
    h = _layernorm(x, axis=-1) * (1 + cb) + sb
    x = x + gb * _B_ref(h, B_w_gate, B_w_up, B_w_down)
    return x
```

```cpp
#include <hip/hip_runtime.h>
#include <hip/hip_bf16.h>
typedef unsigned short u16;
__device__ __forceinline__ float b2f(u16 x) { return __uint_as_float((unsigned)x << 16); }
__device__ __forceinline__ u16 f2b(float f) { __bf16 b = (__bf16)f; return __builtin_bit_cast(u16, b); }

__global__ void k_mod(const u16* c, const u16* wm, float* mod) {
  int n = blockIdx.x * 256 + threadIdx.x;
  float acc = 0;
  for (int k = 0; k < 512; ++k) { float v = b2f(c[k]); float s = b2f(f2b(v / (1.f + __expf(-v)))); acc += s * b2f(wm[k * 3072 + n]); }
  mod[n] = b2f(f2b(acc));
}
template<bool F32>
__global__ void k_ln(const void* in, const float* sh, const float* sc, u16* h) {
  int row = blockIdx.x, l = threadIdx.x;
  float v[8]; float s = 0;
  for (int i = 0; i < 8; ++i) { int c = l * 8 + i; v[i] = F32 ? ((const float*)in)[row * 512 + c] : b2f(((const u16*)in)[row * 512 + c]); s += v[i]; }
  for (int o = 32; o; o >>= 1) s += __shfl_xor(s, o);
  float m = s / 512; float q = 0;
  for (int i = 0; i < 8; ++i) q += (v[i] - m) * (v[i] - m);
  for (int o = 32; o; o >>= 1) q += __shfl_xor(q, o);
  float r = rsqrtf(q / 512 + 1e-5f);
  for (int i = 0; i < 8; ++i) { int c = l * 8 + i; h[row * 512 + c] = f2b((v[i] - m) * r * (1.f + sc[c]) + sh[c]); }
}
__global__ void k_gemm(const u16* A, int lda, const u16* B, int ldb, float* C, int ldc, int M, int N, int K) {
  __shared__ float As[16][65], Bs[16][65];
  int tx = threadIdx.x & 15, ty = threadIdx.x >> 4;
  int m0 = blockIdx.y * 64, n0 = blockIdx.x * 64;
  float acc[4][4] = {};
  for (int k0 = 0; k0 < K; k0 += 16) {
    for (int i = threadIdx.x; i < 1024; i += 256) {
      int m = i >> 4, k = i & 15; As[k][m] = (m0 + m < M) ? b2f(A[(size_t)(m0 + m) * lda + k0 + k]) : 0.f;
      int kk = i >> 6, n = i & 63; Bs[kk][n] = b2f(B[(size_t)(k0 + kk) * ldb + n0 + n]);
    }
    __syncthreads();
    for (int k = 0; k < 16; ++k)
      for (int i = 0; i < 4; ++i) for (int j = 0; j < 4; ++j) acc[i][j] += As[k][ty * 4 + i] * Bs[k][tx * 4 + j];
    __syncthreads();
  }
  for (int i = 0; i < 4; ++i) for (int j = 0; j < 4; ++j) if (m0 + ty * 4 + i < M) C[(size_t)(m0 + ty * 4 + i) * ldc + n0 + tx * 4 + j] = acc[i][j];
}
__global__ void k_cvt(const float* in, u16* out, int n) { int i = blockIdx.x * 256 + threadIdx.x; if (i < n) out[i] = f2b(in[i]); }
__global__ void k_att(const float* Qx, const float* Kx, const float* Vx, const float* qm, const float* km, const float* vm, int s, u16* Om, u16* Ox) {
  __shared__ float q[40][129], k[40][129], v[40][129], p[40][41];
  int h = blockIdx.x, t = threadIdx.x;
  for (int i = t; i < 40 * 128; i += 256) { int r = i >> 7, e = i & 127;
    size_t o = r < 8 ? (size_t)r * 4096 + h * 128 + e : (size_t)(s * 32 + r - 8) * 4096 + h * 128 + e;
    q[r][e] = b2f(f2b(r < 8 ? qm[o] : Qx[o])); k[r][e] = b2f(f2b(r < 8 ? km[o] : Kx[o])); v[r][e] = b2f(f2b(r < 8 ? vm[o] : Vx[o])); }
  __syncthreads();
  for (int i = t; i < 1600; i += 256) { int a = i / 40, b = i % 40; float acc = 0; for (int e = 0; e < 128; ++e) acc += q[a][e] * k[b][e];
    p[a][b] = (a < 8 || a >= b) ? acc * 0.08838834764831845f : -1e9f; }
  __syncthreads();
  if (t < 40) { float m = -1e30f; for (int b = 0; b < 40; ++b) m = fmaxf(m, p[t][b]); float sum = 0; for (int b = 0; b < 40; ++b) { p[t][b] = __expf(p[t][b] - m); sum += p[t][b]; }
    for (int b = 0; b < 40; ++b) p[t][b] = b2f(f2b(p[t][b] / sum)); }
  __syncthreads();
  for (int i = t; i < 40 * 128; i += 256) { int r = i >> 7, e = i & 127; float acc = 0; for (int b = 0; b < 40; ++b) acc += p[r][b] * v[b][e];
    if (r < 8) Om[r * 4096 + h * 128 + e] = f2b(acc); else Ox[(size_t)(s * 32 + r - 8) * 4096 + h * 128 + e] = f2b(acc); }
}
__global__ void k_res1(const u16* x, const float* att, const float* ga, float* x1) { int i = blockIdx.x * 256 + threadIdx.x; x1[i] = b2f(x[i]) + ga[i & 511] * att[i]; }
__global__ void k_glu(const float* g, const float* u, u16* hh) { int i = blockIdx.x * 256 + threadIdx.x; hh[i] = f2b(g[i] * u[i]); }
__global__ void k_res2(const float* x1, const float* y, const float* gb, u16* out) { int i = blockIdx.x * 256 + threadIdx.x; out[i] = f2b(x1[i] + gb[i & 511] * y[i]); }

extern "C" void kernel_launch(void* const* d_in, const int* in_sizes, int n_in, void* d_out, int out_size, void* d_ws, size_t ws_size, hipStream_t st) {
  const u16 *x = (const u16*)d_in[0], *mem0 = (const u16*)d_in[1], *Wq = (const u16*)d_in[2], *Wk = (const u16*)d_in[3], *Wv = (const u16*)d_in[4], *Wo = (const u16*)d_in[5];
  const u16 *wg = (const u16*)d_in[8], *wu = (const u16*)d_in[9], *wd = (const u16*)d_in[10], *c = (const u16*)d_in[11], *wm = (const u16*)d_in[12];
  char* ws = (char*)d_ws; size_t o = 0;
  auto al = [&](size_t n) { char* p = ws + o; o += (n + 255) & ~(size_t)255; return p; };
  float* mod = (float*)al(3072 * 4); u16* h = (u16*)al(2048 * 512 * 2);
  float *Q = (float*)al((size_t)2048 * 4096 * 4), *K = (float*)al((size_t)2048 * 4096 * 4), *V = (float*)al((size_t)2048 * 4096 * 4);
  float *qm = (float*)al(8 * 4096 * 4), *km = (float*)al(8 * 4096 * 4), *vm = (float*)al(8 * 4096 * 4);
  u16 *Om = (u16*)al(8 * 4096 * 2), *Ox = (u16*)al((size_t)2048 * 4096 * 2), *M = (u16*)al(8 * 512 * 2);
  float *Mf = (float*)al(8 * 512 * 4), *att = (float*)al(2048 * 512 * 4), *x1 = (float*)al(2048 * 512 * 4);
  float *g = (float*)al((size_t)2048 * 1792 * 4), *u = (float*)al((size_t)2048 * 1792 * 4); u16* hh = (u16*)al((size_t)2048 * 1792 * 2);
  k_mod<<<12, 256, 0, st>>>(c, wm, mod);
  k_ln<false><<<2048, 64, 0, st>>>(x, mod, mod + 512, h);
  k_gemm<<<dim3(64, 32), 256, 0, st>>>(h, 512, Wq, 4096, Q, 4096, 2048, 4096, 512);
  k_gemm<<<dim3(64, 32), 256, 0, st>>>(h, 512, Wk, 4096, K, 4096, 2048, 4096, 512);
  k_gemm<<<dim3(64, 32), 256, 0, st>>>(h, 512, Wv, 4096, V, 4096, 2048, 4096, 512);
  for (int s = 0; s < 64; ++s) {
    const u16* Mc = s == 0 ? mem0 : M;
    k_gemm<<<dim3(64, 1), 256, 0, st>>>(Mc, 512, Wq, 4096, qm, 4096, 8, 4096, 512);
    k_gemm<<<dim3(64, 1), 256, 0, st>>>(Mc, 512, Wk, 4096, km, 4096, 8, 4096, 512);
    k_gemm<<<dim3(64, 1), 256, 0, st>>>(Mc, 512, Wv, 4096, vm, 4096, 8, 4096, 512);
    k_att<<<32, 256, 0, st>>>(Q, K, V, qm, km, vm, s, Om, Ox);
    if (s < 63) { k_gemm<<<dim3(8, 1), 256, 0, st>>>(Om, 4096, Wo, 512, Mf, 512, 8, 512, 4096); k_cvt<<<16, 256, 0, st>>>(Mf, M, 4096); }
  }
  k_gemm<<<dim3(8, 32), 256, 0, st>>>(Ox, 4096, Wo, 512, att, 512, 2048, 512, 4096);
  k_res1<<<4096, 256, 0, st>>>(x, att, mod + 1024, x1);
  k_ln<true><<<2048, 64, 0, st>>>(x1, mod + 1536, mod + 2048, h);
  k_gemm<<<dim3(28, 32), 256, 0, st>>>(h, 512, wg, 1792, g, 1792, 2048, 1792, 512);
  k_gemm<<<dim3(28, 32), 256, 0, st>>>(h, 512, wu, 1792, u, 1792, 2048, 1792, 512);
  k_glu<<<2048 * 1792 / 256, 256, 0, st>>>(g, u, hh);
  k_gemm<<<dim3(8, 32), 256, 0, st>>>(hh, 1792, wd, 512, att, 512, 2048, 512, 1792);
  k_res2<<<4096, 256, 0, st>>>(x1, att, mod + 2560, (u16*)d_out);
}
```

```cpp
#include <hip/hip_runtime.h>
#include <hip/hip_bf16.h>
#define STAMPS 0
#define GAS __attribute__((address_space(1)))
#define LAS __attribute__((address_space(3)))
#define DI __device__ __forceinline__
typedef unsigned short u16;
typedef unsigned long long u64;
typedef unsigned v2u __attribute__((ext_vector_type(2)));
typedef unsigned v4u __attribute__((ext_vector_type(4)));
typedef unsigned v8u __attribute__((ext_vector_type(8)));
typedef unsigned v16u __attribute__((ext_vector_type(16)));
typedef __bf16 bf16x2 __attribute__((ext_vector_type(2)));
typedef __bf16 bf16x8 __attribute__((ext_vector_type(8)));
typedef float f32x2 __attribute__((ext_vector_type(2)));
typedef float f32x4 __attribute__((ext_vector_type(4)));
typedef __amdgpu_buffer_rsrc_t rsrc_t;

#define CLS "s84", "s85", "s86", "s87", "s88", "s89", "s90", "s91", "s92", "s93", "s94", "s95"
#define CLA_0_127 "a0", "a1", "a2", "a3", "a4", "a5", "a6", "a7", "a8", "a9", "a10", "a11", "a12", "a13", "a14", "a15", "a16", "a17", "a18", "a19", "a20", "a21", "a22", "a23", "a24", "a25", "a26", "a27", "a28", "a29", "a30", "a31", "a32", "a33", "a34", "a35", "a36", "a37", "a38", "a39", "a40", "a41", "a42", "a43", "a44", "a45", "a46", "a47", "a48", "a49", "a50", "a51", "a52", "a53", "a54", "a55", "a56", "a57", "a58", "a59", "a60", "a61", "a62", "a63", "a64", "a65", "a66", "a67", "a68", "a69", "a70", "a71", "a72", "a73", "a74", "a75", "a76", "a77", "a78", "a79", "a80", "a81", "a82", "a83", "a84", "a85", "a86", "a87", "a88", "a89", "a90", "a91", "a92", "a93", "a94", "a95", "a96", "a97", "a98", "a99", "a100", "a101", "a102", "a103", "a104", "a105", "a106", "a107", "a108", "a109", "a110", "a111", "a112", "a113", "a114", "a115", "a116", "a117", "a118", "a119", "a120", "a121", "a122", "a123", "a124", "a125", "a126", "a127"
#define CLA_0_131 "a0", "a1", "a2", "a3", "a4", "a5", "a6", "a7", "a8", "a9", "a10", "a11", "a12", "a13", "a14", "a15", "a16", "a17", "a18", "a19", "a20", "a21", "a22", "a23", "a24", "a25", "a26", "a27", "a28", "a29", "a30", "a31", "a32", "a33", "a34", "a35", "a36", "a37", "a38", "a39", "a40", "a41", "a42", "a43", "a44", "a45", "a46", "a47", "a48", "a49", "a50", "a51", "a52", "a53", "a54", "a55", "a56", "a57", "a58", "a59", "a60", "a61", "a62", "a63", "a64", "a65", "a66", "a67", "a68", "a69", "a70", "a71", "a72", "a73", "a74", "a75", "a76", "a77", "a78", "a79", "a80", "a81", "a82", "a83", "a84", "a85", "a86", "a87", "a88", "a89", "a90", "a91", "a92", "a93", "a94", "a95", "a96", "a97", "a98", "a99", "a100", "a101", "a102", "a103", "a104", "a105", "a106", "a107", "a108", "a109", "a110", "a111", "a112", "a113", "a114", "a115", "a116", "a117", "a118", "a119", "a120", "a121", "a122", "a123", "a124", "a125", "a126", "a127", "a128", "a129", "a130", "a131"
#define CLA_0_255 "a0", "a1", "a2", "a3", "a4", "a5", "a6", "a7", "a8", "a9", "a10", "a11", "a12", "a13", "a14", "a15", "a16", "a17", "a18", "a19", "a20", "a21", "a22", "a23", "a24", "a25", "a26", "a27", "a28", "a29", "a30", "a31", "a32", "a33", "a34", "a35", "a36", "a37", "a38", "a39", "a40", "a41", "a42", "a43", "a44", "a45", "a46", "a47", "a48", "a49", "a50", "a51", "a52", "a53", "a54", "a55", "a56", "a57", "a58", "a59", "a60", "a61", "a62", "a63", "a64", "a65", "a66", "a67", "a68", "a69", "a70", "a71", "a72", "a73", "a74", "a75", "a76", "a77", "a78", "a79", "a80", "a81", "a82", "a83", "a84", "a85", "a86", "a87", "a88", "a89", "a90", "a91", "a92", "a93", "a94", "a95", "a96", "a97", "a98", "a99", "a100", "a101", "a102", "a103", "a104", "a105", "a106", "a107", "a108", "a109", "a110", "a111", "a112", "a113", "a114", "a115", "a116", "a117", "a118", "a119", "a120", "a121", "a122", "a123", "a124", "a125", "a126", "a127", "a128", "a129", "a130", "a131", "a132", "a133", "a134", "a135", "a136", "a137", "a138", "a139", "a140", "a141", "a142", "a143", "a144", "a145", "a146", "a147", "a148", "a149", "a150", "a151", "a152", "a153", "a154", "a155", "a156", "a157", "a158", "a159", "a160", "a161", "a162", "a163", "a164", "a165", "a166", "a167", "a168", "a169", "a170", "a171", "a172", "a173", "a174", "a175", "a176", "a177", "a178", "a179", "a180", "a181", "a182", "a183", "a184", "a185", "a186", "a187", "a188", "a189", "a190", "a191", "a192", "a193", "a194", "a195", "a196", "a197", "a198", "a199", "a200", "a201", "a202", "a203", "a204", "a205", "a206", "a207", "a208", "a209", "a210", "a211", "a212", "a213", "a214", "a215", "a216", "a217", "a218", "a219", "a220", "a221", "a222", "a223", "a224", "a225", "a226", "a227", "a228", "a229", "a230", "a231", "a232", "a233", "a234", "a235", "a236", "a237", "a238", "a239", "a240", "a241", "a242", "a243", "a244", "a245", "a246", "a247", "a248", "a249", "a250", "a251", "a252", "a253", "a254", "a255"
#define CLA_128_159 "a128", "a129", "a130", "a131", "a132", "a133", "a134", "a135", "a136", "a137", "a138", "a139", "a140", "a141", "a142", "a143", "a144", "a145", "a146", "a147", "a148", "a149", "a150", "a151", "a152", "a153", "a154", "a155", "a156", "a157", "a158", "a159"
#define CLV_48_99 "v48", "v49", "v50", "v51", "v52", "v53", "v54", "v55", "v56", "v57", "v58", "v59", "v60", "v61", "v62", "v63", "v64", "v65", "v66", "v67", "v68", "v69", "v70", "v71", "v72", "v73", "v74", "v75", "v76", "v77", "v78", "v79", "v80", "v81", "v82", "v83", "v84", "v85", "v86", "v87", "v88", "v89", "v90", "v91", "v92", "v93", "v94", "v95", "v96", "v97", "v98", "v99"
#define CLV_48_175 "v48", "v49", "v50", "v51", "v52", "v53", "v54", "v55", "v56", "v57", "v58", "v59", "v60", "v61", "v62", "v63", "v64", "v65", "v66", "v67", "v68", "v69", "v70", "v71", "v72", "v73", "v74", "v75", "v76", "v77", "v78", "v79", "v80", "v81", "v82", "v83", "v84", "v85", "v86", "v87", "v88", "v89", "v90", "v91", "v92", "v93", "v94", "v95", "v96", "v97", "v98", "v99", "v100", "v101", "v102", "v103", "v104", "v105", "v106", "v107", "v108", "v109", "v110", "v111", "v112", "v113", "v114", "v115", "v116", "v117", "v118", "v119", "v120", "v121", "v122", "v123", "v124", "v125", "v126", "v127", "v128", "v129", "v130", "v131", "v132", "v133", "v134", "v135", "v136", "v137", "v138", "v139", "v140", "v141", "v142", "v143", "v144", "v145", "v146", "v147", "v148", "v149", "v150", "v151", "v152", "v153", "v154", "v155", "v156", "v157", "v158", "v159", "v160", "v161", "v162", "v163", "v164", "v165", "v166", "v167", "v168", "v169", "v170", "v171", "v172", "v173", "v174", "v175"
#define CLV_48_191 "v48", "v49", "v50", "v51", "v52", "v53", "v54", "v55", "v56", "v57", "v58", "v59", "v60", "v61", "v62", "v63", "v64", "v65", "v66", "v67", "v68", "v69", "v70", "v71", "v72", "v73", "v74", "v75", "v76", "v77", "v78", "v79", "v80", "v81", "v82", "v83", "v84", "v85", "v86", "v87", "v88", "v89", "v90", "v91", "v92", "v93", "v94", "v95", "v96", "v97", "v98", "v99", "v100", "v101", "v102", "v103", "v104", "v105", "v106", "v107", "v108", "v109", "v110", "v111", "v112", "v113", "v114", "v115", "v116", "v117", "v118", "v119", "v120", "v121", "v122", "v123", "v124", "v125", "v126", "v127", "v128", "v129", "v130", "v131", "v132", "v133", "v134", "v135", "v136", "v137", "v138", "v139", "v140", "v141", "v142", "v143", "v144", "v145", "v146", "v147", "v148", "v149", "v150", "v151", "v152", "v153", "v154", "v155", "v156", "v157", "v158", "v159", "v160", "v161", "v162", "v163", "v164", "v165", "v166", "v167", "v168", "v169", "v170", "v171", "v172", "v173", "v174", "v175", "v176", "v177", "v178", "v179", "v180", "v181", "v182", "v183", "v184", "v185", "v186", "v187", "v188", "v189", "v190", "v191"
#define CLV_64_71 "v64", "v65", "v66", "v67", "v68", "v69", "v70", "v71"
#define CLV_64_151 "v64", "v65", "v66", "v67", "v68", "v69", "v70", "v71", "v72", "v73", "v74", "v75", "v76", "v77", "v78", "v79", "v80", "v81", "v82", "v83", "v84", "v85", "v86", "v87", "v88", "v89", "v90", "v91", "v92", "v93", "v94", "v95", "v96", "v97", "v98", "v99", "v100", "v101", "v102", "v103", "v104", "v105", "v106", "v107", "v108", "v109", "v110", "v111", "v112", "v113", "v114", "v115", "v116", "v117", "v118", "v119", "v120", "v121", "v122", "v123", "v124", "v125", "v126", "v127", "v128", "v129", "v130", "v131", "v132", "v133", "v134", "v135", "v136", "v137", "v138", "v139", "v140", "v141", "v142", "v143", "v144", "v145", "v146", "v147", "v148", "v149", "v150", "v151"
#define CLV_64_167 "v64", "v65", "v66", "v67", "v68", "v69", "v70", "v71", "v72", "v73", "v74", "v75", "v76", "v77", "v78", "v79", "v80", "v81", "v82", "v83", "v84", "v85", "v86", "v87", "v88", "v89", "v90", "v91", "v92", "v93", "v94", "v95", "v96", "v97", "v98", "v99", "v100", "v101", "v102", "v103", "v104", "v105", "v106", "v107", "v108", "v109", "v110", "v111", "v112", "v113", "v114", "v115", "v116", "v117", "v118", "v119", "v120", "v121", "v122", "v123", "v124", "v125", "v126", "v127", "v128", "v129", "v130", "v131", "v132", "v133", "v134", "v135", "v136", "v137", "v138", "v139", "v140", "v141", "v142", "v143", "v144", "v145", "v146", "v147", "v148", "v149", "v150", "v151", "v152", "v153", "v154", "v155", "v156", "v157", "v158", "v159", "v160", "v161", "v162", "v163", "v164", "v165", "v166", "v167"
#define CLV_64_185 "v64", "v65", "v66", "v67", "v68", "v69", "v70", "v71", "v72", "v73", "v74", "v75", "v76", "v77", "v78", "v79", "v80", "v81", "v82", "v83", "v84", "v85", "v86", "v87", "v88", "v89", "v90", "v91", "v92", "v93", "v94", "v95", "v96", "v97", "v98", "v99", "v100", "v101", "v102", "v103", "v104", "v105", "v106", "v107", "v108", "v109", "v110", "v111", "v112", "v113", "v114", "v115", "v116", "v117", "v118", "v119", "v120", "v121", "v122", "v123", "v124", "v125", "v126", "v127", "v128", "v129", "v130", "v131", "v132", "v133", "v134", "v135", "v136", "v137", "v138", "v139", "v140", "v141", "v142", "v143", "v144", "v145", "v146", "v147", "v148", "v149", "v150", "v151", "v152", "v153", "v154", "v155", "v156", "v157", "v158", "v159", "v160", "v161", "v162", "v163", "v164", "v165", "v166", "v167", "v168", "v169", "v170", "v171", "v172", "v173", "v174", "v175", "v176", "v177", "v178", "v179", "v180", "v181", "v182", "v183", "v184", "v185"
#define CLV_96_203 "v96", "v97", "v98", "v99", "v100", "v101", "v102", "v103", "v104", "v105", "v106", "v107", "v108", "v109", "v110", "v111", "v112", "v113", "v114", "v115", "v116", "v117", "v118", "v119", "v120", "v121", "v122", "v123", "v124", "v125", "v126", "v127", "v128", "v129", "v130", "v131", "v132", "v133", "v134", "v135", "v136", "v137", "v138", "v139", "v140", "v141", "v142", "v143", "v144", "v145", "v146", "v147", "v148", "v149", "v150", "v151", "v152", "v153", "v154", "v155", "v156", "v157", "v158", "v159", "v160", "v161", "v162", "v163", "v164", "v165", "v166", "v167", "v168", "v169", "v170", "v171", "v172", "v173", "v174", "v175", "v176", "v177", "v178", "v179", "v180", "v181", "v182", "v183", "v184", "v185", "v186", "v187", "v188", "v189", "v190", "v191", "v192", "v193", "v194", "v195", "v196", "v197", "v198", "v199", "v200", "v201", "v202", "v203"
#define CLV_96_207 "v96", "v97", "v98", "v99", "v100", "v101", "v102", "v103", "v104", "v105", "v106", "v107", "v108", "v109", "v110", "v111", "v112", "v113", "v114", "v115", "v116", "v117", "v118", "v119", "v120", "v121", "v122", "v123", "v124", "v125", "v126", "v127", "v128", "v129", "v130", "v131", "v132", "v133", "v134", "v135", "v136", "v137", "v138", "v139", "v140", "v141", "v142", "v143", "v144", "v145", "v146", "v147", "v148", "v149", "v150", "v151", "v152", "v153", "v154", "v155", "v156", "v157", "v158", "v159", "v160", "v161", "v162", "v163", "v164", "v165", "v166", "v167", "v168", "v169", "v170", "v171", "v172", "v173", "v174", "v175", "v176", "v177", "v178", "v179", "v180", "v181", "v182", "v183", "v184", "v185", "v186", "v187", "v188", "v189", "v190", "v191", "v192", "v193", "v194", "v195", "v196", "v197", "v198", "v199", "v200", "v201", "v202", "v203", "v204", "v205", "v206", "v207"
#define CLV_96_227 "v96", "v97", "v98", "v99", "v100", "v101", "v102", "v103", "v104", "v105", "v106", "v107", "v108", "v109", "v110", "v111", "v112", "v113", "v114", "v115", "v116", "v117", "v118", "v119", "v120", "v121", "v122", "v123", "v124", "v125", "v126", "v127", "v128", "v129", "v130", "v131", "v132", "v133", "v134", "v135", "v136", "v137", "v138", "v139", "v140", "v141", "v142", "v143", "v144", "v145", "v146", "v147", "v148", "v149", "v150", "v151", "v152", "v153", "v154", "v155", "v156", "v157", "v158", "v159", "v160", "v161", "v162", "v163", "v164", "v165", "v166", "v167", "v168", "v169", "v170", "v171", "v172", "v173", "v174", "v175", "v176", "v177", "v178", "v179", "v180", "v181", "v182", "v183", "v184", "v185", "v186", "v187", "v188", "v189", "v190", "v191", "v192", "v193", "v194", "v195", "v196", "v197", "v198", "v199", "v200", "v201", "v202", "v203", "v204", "v205", "v206", "v207", "v208", "v209", "v210", "v211", "v212", "v213", "v214", "v215", "v216", "v217", "v218", "v219", "v220", "v221", "v222", "v223", "v224", "v225", "v226", "v227"
#define CLV_96_239 "v96", "v97", "v98", "v99", "v100", "v101", "v102", "v103", "v104", "v105", "v106", "v107", "v108", "v109", "v110", "v111", "v112", "v113", "v114", "v115", "v116", "v117", "v118", "v119", "v120", "v121", "v122", "v123", "v124", "v125", "v126", "v127", "v128", "v129", "v130", "v131", "v132", "v133", "v134", "v135", "v136", "v137", "v138", "v139", "v140", "v141", "v142", "v143", "v144", "v145", "v146", "v147", "v148", "v149", "v150", "v151", "v152", "v153", "v154", "v155", "v156", "v157", "v158", "v159", "v160", "v161", "v162", "v163", "v164", "v165", "v166", "v167", "v168", "v169", "v170", "v171", "v172", "v173", "v174", "v175", "v176", "v177", "v178", "v179", "v180", "v181", "v182", "v183", "v184", "v185", "v186", "v187", "v188", "v189", "v190", "v191", "v192", "v193", "v194", "v195", "v196", "v197", "v198", "v199", "v200", "v201", "v202", "v203", "v204", "v205", "v206", "v207", "v208", "v209", "v210", "v211", "v212", "v213", "v214", "v215", "v216", "v217", "v218", "v219", "v220", "v221", "v222", "v223", "v224", "v225", "v226", "v227", "v228", "v229", "v230", "v231", "v232", "v233", "v234", "v235", "v236", "v237", "v238", "v239"
#define CLV_112_215 "v112", "v113", "v114", "v115", "v116", "v117", "v118", "v119", "v120", "v121", "v122", "v123", "v124", "v125", "v126", "v127", "v128", "v129", "v130", "v131", "v132", "v133", "v134", "v135", "v136", "v137", "v138", "v139", "v140", "v141", "v142", "v143", "v144", "v145", "v146", "v147", "v148", "v149", "v150", "v151", "v152", "v153", "v154", "v155", "v156", "v157", "v158", "v159", "v160", "v161", "v162", "v163", "v164", "v165", "v166", "v167", "v168", "v169", "v170", "v171", "v172", "v173", "v174", "v175", "v176", "v177", "v178", "v179", "v180", "v181", "v182", "v183", "v184", "v185", "v186", "v187", "v188", "v189", "v190", "v191", "v192", "v193", "v194", "v195", "v196", "v197", "v198", "v199", "v200", "v201", "v202", "v203", "v204", "v205", "v206", "v207", "v208", "v209", "v210", "v211", "v212", "v213", "v214", "v215"
DI void s1_step0(rsrc_t rc, unsigned sM, int wM, unsigned lkq, unsigned lm, unsigned lnx, unsigned sK, const v16u& cv, int& bud, unsigned& nret, unsigned sFM, int wW) {
  asm volatile(
    "s_nop 4\nv_mov_b64 v[160:161], 0\nv_mov_b64 v[162:163], 0\nv_mov_b64 v[164:165], 0\nv_mov_b64 v[166:167], 0\nv_mov_b64 v[168:169], 0\n"
    "v_mov_b64 v[170:171], 0\nv_mov_b64 v[172:173], 0\nv_mov_b64 v[174:175], 0\nv_mov_b64 v[176:177], 0\nv_mov_b64 v[178:179], 0\nv_mov_b64 v[180:181], 0\n"
    "v_mov_b64 v[182:183], 0\nv_mov_b64 v[184:185], 0\nv_mov_b64 v[186:187], 0\ns_mov_b32 s86, 0x3e0293ee\nv_add_u32 v196, %[lkq], v62\n"
    "s_mov_b32 s94, %[sM]\ns_mov_b32 s91, 0\ns_add_u32 s87, %[sM], 65536\ns_add_u32 s88, %[sM], 98304\ns_add_u32 s89, %[sM], 114688\n"
    "s_cmp_eq_u32 %[sFM], -1\ns_cbranch_scc1 2f\ns_mov_b32 s84, %[wW]\ns_branch 8f\n.p2align 6\n8:\ns_sub_u32 s84, s84, 1\ns_cbranch_scc0 8b\n"
    "s_memrealtime s[92:93]\ns_waitcnt lgkmcnt(0)\ns_lshl_b32 s92, s92, 6\ns_and_b32 s92, s92, 0xf00\ns_add_u32 s92, s92, %[sFM]\n"
    "buffer_load_dword v200, v57, %[rc], s92 offen sc1\ns_nop 2\ns_memrealtime s[92:93]\ns_waitcnt lgkmcnt(0)\ns_lshl_b32 s92, s92, 6\n"
    "s_and_b32 s92, s92, 0xf00\ns_add_u32 s92, s92, %[sFM]\nbuffer_load_dword v201, v57, %[rc], s92 offen sc1\ns_nop 2\ns_memrealtime s[92:93]\n"
    "s_waitcnt lgkmcnt(0)\ns_lshl_b32 s92, s92, 6\ns_and_b32 s92, s92, 0xf00\ns_add_u32 s92, s92, %[sFM]\n"
    "buffer_load_dword v202, v57, %[rc], s92 offen sc1\ns_nop 2\ns_memrealtime s[92:93]\ns_waitcnt lgkmcnt(0)\ns_lshl_b32 s92, s92, 6\n"
    "s_and_b32 s92, s92, 0xf00\ns_add_u32 s92, s92, %[sFM]\nbuffer_load_dword v203, v57, %[rc], s92 offen sc1\ns_nop 2\ns_memrealtime s[92:93]\n"
    "s_waitcnt lgkmcnt(0)\ns_lshl_b32 s92, s92, 6\ns_and_b32 s92, s92, 0xf00\ns_add_u32 s92, s92, %[sFM]\n"
    "buffer_load_dword v204, v57, %[rc], s92 offen sc1\n1:\ns_waitcnt vmcnt(4)\ns_memrealtime s[92:93]\ns_waitcnt vmcnt(4)\nv_cmp_eq_u32 vcc, -1, v200\n"
    "s_cbranch_vccz 2f\ns_waitcnt lgkmcnt(0)\ns_lshl_b32 s92, s92, 6\ns_and_b32 s92, s92, 0xf00\ns_add_u32 s92, s92, %[sFM]\n"
    "buffer_load_dword v200, v57, %[rc], s92 offen sc1\ns_waitcnt vmcnt(4)\ns_memrealtime s[92:93]\ns_waitcnt vmcnt(4)\nv_cmp_eq_u32 vcc, -1, v201\n"
    "s_cbranch_vccz 2f\ns_waitcnt lgkmcnt(0)\ns_lshl_b32 s92, s92, 6\ns_and_b32 s92, s92, 0xf00\ns_add_u32 s92, s92, %[sFM]\n"
    "buffer_load_dword v201, v57, %[rc], s92 offen sc1\ns_waitcnt vmcnt(4)\ns_memrealtime s[92:93]\ns_waitcnt vmcnt(4)\nv_cmp_eq_u32 vcc, -1, v202\n"
    "s_cbranch_vccz 2f\ns_waitcnt lgkmcnt(0)\ns_lshl_b32 s92, s92, 6\ns_and_b32 s92, s92, 0xf00\ns_add_u32 s92, s92, %[sFM]\n"
    "buffer_load_dword v202, v57, %[rc], s92 offen sc1\ns_waitcnt vmcnt(4)\ns_memrealtime s[92:93]\ns_waitcnt vmcnt(4)\nv_cmp_eq_u32 vcc, -1, v203\n"
    "s_cbranch_vccz 2f\ns_waitcnt lgkmcnt(0)\ns_lshl_b32 s92, s92, 6\ns_and_b32 s92, s92, 0xf00\ns_add_u32 s92, s92, %[sFM]\n"
    "buffer_load_dword v203, v57, %[rc], s92 offen sc1\ns_waitcnt vmcnt(4)\ns_memrealtime s[92:93]\ns_waitcnt vmcnt(4)\nv_cmp_eq_u32 vcc, -1, v204\n"
    "s_cbranch_vccz 2f\ns_waitcnt lgkmcnt(0)\ns_lshl_b32 s92, s92, 6\ns_and_b32 s92, s92, 0xf00\ns_add_u32 s92, s92, %[sFM]\n"
    "buffer_load_dword v204, v57, %[rc], s92 offen sc1\ns_sub_u32 %[bud], %[bud], 1\ns_cmp_gt_i32 %[bud], 0\ns_cbranch_scc1 1b\n2:\ns_barrier\n"
    "s_waitcnt lgkmcnt(0)\ns_mov_b32 s84, %[wM]\ns_branch 3f\n.p2align 6\n3:\ns_sub_u32 s84, s84, 1\ns_cbranch_scc0 3b\n4:\ns_mov_b32 m0, %[lm]\ns_nop 0\n"
    "buffer_load_dwordx4 v48, %[rc], s94 offen sc1 lds\ns_add_u32 m0, %[lm], 4096\ns_nop 0\nbuffer_load_dwordx4 v58, %[rc], s94 offen sc1 lds\n"
    "s_nop 9\ns_mov_b32 m0, %[lnx]\ns_mov_b32 s85, %[sK]\nbuffer_load_dwordx4 v52, %[rc], s85 offen sc1 lds\ns_add_u32 m0, m0, 1024\n"
    "s_add_u32 s85, s85, 1024\nbuffer_load_dwordx4 v52, %[rc], s85 offen sc1 lds\ns_add_u32 m0, m0, 1024\ns_add_u32 s85, s85, 1024\n"
    "buffer_load_dwordx4 v52, %[rc], s85 offen sc1 lds\ns_add_u32 m0, m0, 1024\ns_add_u32 s85, s85, 1024\n"
    "buffer_load_dwordx4 v52, %[rc], s85 offen sc1 lds\ns_waitcnt vmcnt(5)\ns_barrier\nds_read_b128 v[128:131], v49 offset:0\n"
    "ds_read_b128 v[132:135], v49 offset:1024\nds_read_b128 v[136:139], v49 offset:2048\nds_read_b128 v[140:143], v49 offset:3072\n"
    "s_waitcnt vmcnt(4)\ns_barrier\nds_read_b128 v[144:147], v49 offset:4096\nds_read_b128 v[148:151], v49 offset:5120\n"
    "ds_read_b128 v[152:155], v49 offset:6144\nds_read_b128 v[156:159], v49 offset:7168\n5:\ns_waitcnt lgkmcnt(7)\n"
    "v_smfmac_f32_16x16x64_bf16 v[160:163], v[128:131], a[0:7], v50\nv_max3_u32 v198, 0, v128, v129\n"
    "v_smfmac_f32_16x16x64_bf16 v[164:167], v[128:131], a[8:15], v50\nv_max3_u32 v198, v198, v130, v131\n"
    "v_smfmac_f32_16x16x64_bf16 v[168:171], v[128:131], a[16:23], v50\nv_smfmac_f32_16x16x64_bf16 v[172:175], v[128:131], a[24:31], v50\n"
    "s_waitcnt lgkmcnt(6)\nv_smfmac_f32_16x16x64_bf16 v[160:163], v[132:135], a[32:39], v50\nv_max3_u32 v198, v198, v132, v133\n"
    "v_smfmac_f32_16x16x64_bf16 v[164:167], v[132:135], a[40:47], v50\nv_max3_u32 v198, v198, v134, v135\n"
    "v_smfmac_f32_16x16x64_bf16 v[168:171], v[132:135], a[48:55], v50\nv_smfmac_f32_16x16x64_bf16 v[172:175], v[132:135], a[56:63], v50\n"
    "s_waitcnt lgkmcnt(0)\nv_smfmac_f32_16x16x64_bf16 v[160:163], v[136:139], a[64:71], v50\nv_max3_u32 v198, v198, v136, v137\n"
    "v_smfmac_f32_16x16x64_bf16 v[164:167], v[136:139], a[72:79], v50\nv_max3_u32 v198, v198, v138, v139\n"
    "v_smfmac_f32_16x16x64_bf16 v[168:171], v[136:139], a[80:87], v50\nv_max3_u32 v198, v198, v140, v141\n"
    "v_smfmac_f32_16x16x64_bf16 v[172:175], v[136:139], a[88:95], v50\nv_max3_u32 v198, v198, v142, v143\n"
    "v_smfmac_f32_16x16x64_bf16 v[160:163], v[140:143], a[96:103], v50\nv_max3_u32 v198, v198, v144, v145\n"
    "v_smfmac_f32_16x16x64_bf16 v[164:167], v[140:143], a[104:111], v50\nv_max3_u32 v198, v198, v146, v147\n"
    "v_smfmac_f32_16x16x64_bf16 v[168:171], v[140:143], a[112:119], v50\nv_max3_u32 v198, v198, v148, v149\n"
    "v_smfmac_f32_16x16x64_bf16 v[172:175], v[140:143], a[120:127], v50\nv_max3_u32 v198, v198, v150, v151\n"
    "v_smfmac_f32_16x16x64_bf16 v[160:163], v[144:147], a[128:135], v50\nv_max3_u32 v198, v198, v152, v153\n"
    "v_smfmac_f32_16x16x64_bf16 v[164:167], v[144:147], a[136:143], v50\nv_max3_u32 v198, v198, v154, v155\n"
    "v_smfmac_f32_16x16x64_bf16 v[168:171], v[144:147], a[144:151], v50\nv_max3_u32 v198, v198, v156, v157\n"
    "v_smfmac_f32_16x16x64_bf16 v[172:175], v[144:147], a[152:159], v50\nv_max3_u32 v198, v198, v158, v159\n"
    "v_smfmac_f32_16x16x64_bf16 v[160:163], v[148:151], a[160:167], v50\nv_cmp_eq_u32 vcc, -1, v198\n"
    "v_smfmac_f32_16x16x64_bf16 v[164:167], v[148:151], a[168:175], v50\nds_read_b128 v[96:99], v196 offset:0\n"
    "v_smfmac_f32_16x16x64_bf16 v[168:171], v[148:151], a[176:183], v50\nds_read_b128 v[100:103], v196 offset:1024\n"
    "v_smfmac_f32_16x16x64_bf16 v[172:175], v[148:151], a[184:191], v50\ns_cbranch_vccnz 7f\n6:\n"
    "v_smfmac_f32_16x16x64_bf16 v[160:163], v[152:155], a[192:199], v50\nds_read_b128 v[104:107], v196 offset:2048\n"
    "v_smfmac_f32_16x16x64_bf16 v[164:167], v[152:155], a[200:207], v50\nds_read_b128 v[108:111], v196 offset:3072\n"
    "v_smfmac_f32_16x16x64_bf16 v[168:171], v[152:155], a[208:215], v50\nds_read_b128 v[112:115], v196 offset:4096\n"
    "v_smfmac_f32_16x16x64_bf16 v[172:175], v[152:155], a[216:223], v50\nds_read_b128 v[116:119], v196 offset:5120\n"
    "v_smfmac_f32_16x16x64_bf16 v[160:163], v[156:159], a[224:231], v50\nds_read_b128 v[120:123], v196 offset:6144\n"
    "v_smfmac_f32_16x16x64_bf16 v[164:167], v[156:159], a[232:239], v50\nds_read_b128 v[124:127], v196 offset:7168\n"
    "v_smfmac_f32_16x16x64_bf16 v[168:171], v[156:159], a[240:247], v50\nv_smfmac_f32_16x16x64_bf16 v[172:175], v[156:159], a[248:255], v50\ns_nop 4\n"
    "v_add_f32 v160, v160, v162\nv_add_f32 v161, v161, v163\nv_add_f32 v164, v164, v166\nv_add_f32 v165, v165, v167\nv_cvt_pk_bf16_f32 v192, v160, v164\n"
    "v_cvt_pk_bf16_f32 v194, v161, v165\nds_write2_b32 v51, v192, v194 offset1:4\nv_add_f32 v168, v168, v170\nv_add_f32 v169, v169, v171\n"
    "v_add_f32 v172, v172, v174\nv_add_f32 v173, v173, v175\nv_cvt_pk_bf16_f32 v193, v168, v172\nv_cvt_pk_bf16_f32 v195, v169, v173\n"
    "ds_write2_b32 v51, v193, v195 offset0:8 offset1:12\ns_waitcnt lgkmcnt(0)\ns_barrier\nds_read_b128 v[128:131], v53\n"
    "ds_read_b128 v[132:135], v53 offset:1088\nds_read_b128 v[136:139], v54\nds_read_b128 v[140:143], v54 offset:16\n"
    "ds_read_b128 v[144:147], v54 offset:128\nds_read_b128 v[148:151], v54 offset:144\ns_waitcnt lgkmcnt(5)\n"
    "v_smfmac_f32_16x16x64_bf16 v[176:179], v[128:131], v[96:103], v50\nv_smfmac_f32_16x16x64_bf16 v[180:183], v[128:131], v[112:119], v50\n"
    "s_waitcnt lgkmcnt(4)\nv_smfmac_f32_16x16x64_bf16 v[176:179], v[132:135], v[104:111], v50\n"
    "v_smfmac_f32_16x16x64_bf16 v[180:183], v[132:135], v[120:127], v50\ns_waitcnt lgkmcnt(2)\n"
    "v_smfmac_f32_16x16x64_bf16 v[184:187], v[128:131], v[136:143], v50\ns_waitcnt lgkmcnt(0)\n"
    "v_smfmac_f32_16x16x64_bf16 v[184:187], v[132:135], v[144:151], v50\ns_nop 3\nv_add_f32 v188, v176, v178\nv_add_f32 v189, v180, v182\n"
    "v_max_f32 v192, v188, v189\ns_nop 0\nv_add_f32 v190, v184, v186\nv_max_f32 v192, v192, v190\nv_mul_f32 v188, s86, v188\nv_mul_f32 v189, s86, v189\n"
    "v_max_f32_dpp v192, v192, v192 row_ror:8 row_mask:0xf bank_mask:0xf\nv_mul_f32 v190, s86, v190\ns_nop 0\n"
    "v_max_f32_dpp v192, v192, v192 row_ror:4 row_mask:0xf bank_mask:0xf\ns_nop 1\n"
    "v_max_f32_dpp v192, v192, v192 quad_perm:[2,3,0,1] row_mask:0xf bank_mask:0xf\ns_nop 1\n"
    "v_max_f32_dpp v192, v192, v192 quad_perm:[1,0,3,2] row_mask:0xf bank_mask:0xf\nv_fma_f32 v188, -v192, s86, v188\nv_fma_f32 v189, -v192, s86, v189\n"
    "v_fma_f32 v190, -v192, s86, v190\nv_exp_f32 v188, v188\nv_exp_f32 v189, v189\nv_exp_f32 v190, v190\nv_add_f32 v192, v188, v189\n"
    "v_fmac_f32 v192, 0.5, v190\ns_nop 0\nv_mov_b32_dpp v191, v190 quad_perm:[1,1,3,3] row_mask:0xf bank_mask:0xf\n"
    "v_add_f32_dpp v192, v192, v192 row_ror:8 row_mask:0xf bank_mask:0xf\ns_nop 0\nv_mov_b32_dpp v190, v190 quad_perm:[0,0,2,2] row_mask:0xf bank_mask:0xf\n"
    "v_add_f32_dpp v192, v192, v192 row_ror:4 row_mask:0xf bank_mask:0xf\ns_nop 1\n"
    "v_add_f32_dpp v192, v192, v192 quad_perm:[2,3,0,1] row_mask:0xf bank_mask:0xf\ns_nop 1\n"
    "v_add_f32_dpp v192, v192, v192 quad_perm:[1,0,3,2] row_mask:0xf bank_mask:0xf\nv_rcp_f32 v192, v192\ns_nop 0\n"
    "v_pk_mul_f32 v[188:189], v[188:189], v[192:193] op_sel_hi:[1,0]\nv_cvt_pk_bf16_f32 v188, v188, v189\n"
    "buffer_store_dword v188, v55, %[rc], %[sM] offen sc1\nv_pk_mul_f32 v[190:191], v[190:191], v[192:193] op_sel_hi:[1,0]\n"
    "v_cvt_pk_bf16_f32 v190, v190, v191\nv_mov_b32 v191, v190\ns_mov_b32 vcc_lo, 0xaaaaaaaa\ns_mov_b32 vcc_hi, 0xaaaaaaaa\nv_permlane16_swap_b32 v190, v191\n"
    "v_cndmask_b32 v190, v190, v191, vcc\nv_mov_b32 v191, v190\ns_mov_b32 vcc_lo, 0xff00ff00\ns_mov_b32 vcc_hi, 0xff00ff00\n"
    "v_permlane32_swap_b32 v190, v191\nv_cndmask_b32 v190, v190, v191, vcc\nbuffer_store_dword v190, v56, %[rc], %[sM] offen sc1\ns_waitcnt vmcnt(2)\n"
    "s_branch 9f\n7:\ns_cmp_eq_u32 s91, 0\ns_cbranch_scc0 6b\ns_mov_b32 s91, 1\nbuffer_load_dwordx4 v[216:219], v48, %[rc], s87 offen sc1\n"
    "buffer_load_dwordx4 v[220:223], v58, %[rc], s87 offen sc1\ns_waitcnt lgkmcnt(0)\ns_barrier\ns_nop 15\ns_nop 15\n"
    "buffer_load_dwordx4 v[224:227], v48, %[rc], s88 offen sc1\nbuffer_load_dwordx4 v[228:231], v58, %[rc], s88 offen sc1\ns_nop 15\ns_nop 15\n"
    "buffer_load_dwordx4 v[208:211], v48, %[rc], s89 offen sc1\nbuffer_load_dwordx4 v[212:215], v58, %[rc], s89 offen sc1\nv_mov_b64 v[160:161], 0\n"
    "v_mov_b64 v[162:163], 0\nv_mov_b64 v[164:165], 0\nv_mov_b64 v[166:167], 0\nv_mov_b64 v[168:169], 0\nv_mov_b64 v[170:171], 0\nv_mov_b64 v[172:173], 0\n"
    "v_mov_b64 v[174:175], 0\ns_add_u32 %[nret], %[nret], 1\ns_waitcnt vmcnt(4)\nds_write_b128 v61, v[216:219]\n"
    "ds_write_b128 v61, v[220:223] offset:4096\nv_max3_u32 v188, v216, v217, v218\nv_max3_u32 v189, v220, v221, v222\nv_max3_u32 v188, v188, v219, v223\n"
    "v_max_u32 v188, v188, v189\nv_cmp_eq_u32 vcc, -1, v188\ns_waitcnt lgkmcnt(0)\ns_cbranch_vccz 42f\n"
    "buffer_load_dwordx4 v[216:219], v48, %[rc], s94 offen sc1\nbuffer_load_dwordx4 v[220:223], v58, %[rc], s94 offen sc1\n"
    "s_add_u32 %[nret], %[nret], 1\ns_waitcnt vmcnt(4)\nds_write_b128 v61, v[224:227]\nds_write_b128 v61, v[228:231] offset:4096\n"
    "v_max3_u32 v188, v224, v225, v226\nv_max3_u32 v189, v228, v229, v230\nv_max3_u32 v188, v188, v227, v231\nv_max_u32 v188, v188, v189\n"
    "v_cmp_eq_u32 vcc, -1, v188\ns_waitcnt lgkmcnt(0)\ns_cbranch_vccz 42f\nbuffer_load_dwordx4 v[224:227], v48, %[rc], s87 offen sc1\n"
    "buffer_load_dwordx4 v[228:231], v58, %[rc], s87 offen sc1\n41:\ns_add_u32 %[nret], %[nret], 1\ns_waitcnt vmcnt(4)\nds_write_b128 v61, v[208:211]\n"
    "ds_write_b128 v61, v[212:215] offset:4096\nv_max3_u32 v188, v208, v209, v210\nv_max3_u32 v189, v212, v213, v214\nv_max3_u32 v188, v188, v211, v215\n"
    "v_max_u32 v188, v188, v189\nv_cmp_eq_u32 vcc, -1, v188\ns_waitcnt lgkmcnt(0)\ns_cbranch_vccz 42f\n"
    "buffer_load_dwordx4 v[208:211], v48, %[rc], s88 offen sc1\nbuffer_load_dwordx4 v[212:215], v58, %[rc], s88 offen sc1\n"
    "s_add_u32 %[nret], %[nret], 1\ns_waitcnt vmcnt(4)\nds_write_b128 v61, v[216:219]\nds_write_b128 v61, v[220:223] offset:4096\n"
    "v_max3_u32 v188, v216, v217, v218\nv_max3_u32 v189, v220, v221, v222\nv_max3_u32 v188, v188, v219, v223\nv_max_u32 v188, v188, v189\n"
    "v_cmp_eq_u32 vcc, -1, v188\ns_waitcnt lgkmcnt(0)\ns_cbranch_vccz 42f\nbuffer_load_dwordx4 v[216:219], v48, %[rc], s89 offen sc1\n"
    "buffer_load_dwordx4 v[220:223], v58, %[rc], s89 offen sc1\ns_add_u32 %[nret], %[nret], 1\ns_waitcnt vmcnt(4)\nds_write_b128 v61, v[224:227]\n"
    "ds_write_b128 v61, v[228:231] offset:4096\nv_max3_u32 v188, v224, v225, v226\nv_max3_u32 v189, v228, v229, v230\nv_max3_u32 v188, v188, v227, v231\n"
    "v_max_u32 v188, v188, v189\nv_cmp_eq_u32 vcc, -1, v188\ns_waitcnt lgkmcnt(0)\ns_cbranch_vccz 42f\n"
    "buffer_load_dwordx4 v[224:227], v48, %[rc], s94 offen sc1\nbuffer_load_dwordx4 v[228:231], v58, %[rc], s94 offen sc1\n"
    "s_add_u32 %[nret], %[nret], 1\ns_waitcnt vmcnt(4)\nds_write_b128 v61, v[208:211]\nds_write_b128 v61, v[212:215] offset:4096\n"
    "v_max3_u32 v188, v208, v209, v210\nv_max3_u32 v189, v212, v213, v214\nv_max3_u32 v188, v188, v211, v215\nv_max_u32 v188, v188, v189\n"
    "v_cmp_eq_u32 vcc, -1, v188\ns_waitcnt lgkmcnt(0)\ns_cbranch_vccz 42f\nbuffer_load_dwordx4 v[208:211], v48, %[rc], s87 offen sc1\n"
    "buffer_load_dwordx4 v[212:215], v58, %[rc], s87 offen sc1\ns_add_u32 %[nret], %[nret], 1\ns_waitcnt vmcnt(4)\nds_write_b128 v61, v[216:219]\n"
    "ds_write_b128 v61, v[220:223] offset:4096\nv_max3_u32 v188, v216, v217, v218\nv_max3_u32 v189, v220, v221, v222\nv_max3_u32 v188, v188, v219, v223\n"
    "v_max_u32 v188, v188, v189\nv_cmp_eq_u32 vcc, -1, v188\ns_waitcnt lgkmcnt(0)\ns_cbranch_vccz 42f\n"
    "buffer_load_dwordx4 v[216:219], v48, %[rc], s88 offen sc1\nbuffer_load_dwordx4 v[220:223], v58, %[rc], s88 offen sc1\n"
    "s_add_u32 %[nret], %[nret], 1\ns_waitcnt vmcnt(4)\nds_write_b128 v61, v[224:227]\nds_write_b128 v61, v[228:231] offset:4096\n"
    "v_max3_u32 v188, v224, v225, v226\nv_max3_u32 v189, v228, v229, v230\nv_max3_u32 v188, v188, v227, v231\nv_max_u32 v188, v188, v189\n"
    "v_cmp_eq_u32 vcc, -1, v188\ns_waitcnt lgkmcnt(0)\ns_cbranch_vccz 42f\nbuffer_load_dwordx4 v[224:227], v48, %[rc], s89 offen sc1\n"
    "buffer_load_dwordx4 v[228:231], v58, %[rc], s89 offen sc1\ns_add_u32 %[nret], %[nret], 1\ns_waitcnt vmcnt(4)\nds_write_b128 v61, v[208:211]\n"
    "ds_write_b128 v61, v[212:215] offset:4096\nv_max3_u32 v188, v208, v209, v210\nv_max3_u32 v189, v212, v213, v214\nv_max3_u32 v188, v188, v211, v215\n"
    "v_max_u32 v188, v188, v189\nv_cmp_eq_u32 vcc, -1, v188\ns_waitcnt lgkmcnt(0)\ns_cbranch_vccz 42f\n"
    "buffer_load_dwordx4 v[208:211], v48, %[rc], s94 offen sc1\nbuffer_load_dwordx4 v[212:215], v58, %[rc], s94 offen sc1\n"
    "s_add_u32 %[nret], %[nret], 1\ns_waitcnt vmcnt(4)\nds_write_b128 v61, v[216:219]\nds_write_b128 v61, v[220:223] offset:4096\n"
    "v_max3_u32 v188, v216, v217, v218\nv_max3_u32 v189, v220, v221, v222\nv_max3_u32 v188, v188, v219, v223\nv_max_u32 v188, v188, v189\n"
    "v_cmp_eq_u32 vcc, -1, v188\ns_waitcnt lgkmcnt(0)\ns_cbranch_vccz 42f\nbuffer_load_dwordx4 v[216:219], v48, %[rc], s87 offen sc1\n"
    "buffer_load_dwordx4 v[220:223], v58, %[rc], s87 offen sc1\ns_add_u32 %[nret], %[nret], 1\ns_waitcnt vmcnt(4)\nds_write_b128 v61, v[224:227]\n"
    "ds_write_b128 v61, v[228:231] offset:4096\nv_max3_u32 v188, v224, v225, v226\nv_max3_u32 v189, v228, v229, v230\nv_max3_u32 v188, v188, v227, v231\n"
    "v_max_u32 v188, v188, v189\nv_cmp_eq_u32 vcc, -1, v188\ns_waitcnt lgkmcnt(0)\ns_cbranch_vccz 42f\n"
    "buffer_load_dwordx4 v[224:227], v48, %[rc], s88 offen sc1\nbuffer_load_dwordx4 v[228:231], v58, %[rc], s88 offen sc1\n"
    "s_add_u32 %[nret], %[nret], 1\ns_waitcnt vmcnt(4)\nds_write_b128 v61, v[208:211]\nds_write_b128 v61, v[212:215] offset:4096\n"
    "v_max3_u32 v188, v208, v209, v210\nv_max3_u32 v189, v212, v213, v214\nv_max3_u32 v188, v188, v211, v215\nv_max_u32 v188, v188, v189\n"
    "v_cmp_eq_u32 vcc, -1, v188\ns_waitcnt lgkmcnt(0)\ns_cbranch_vccz 42f\nbuffer_load_dwordx4 v[208:211], v48, %[rc], s89 offen sc1\n"
    "buffer_load_dwordx4 v[212:215], v58, %[rc], s89 offen sc1\ns_add_u32 %[nret], %[nret], 1\ns_waitcnt vmcnt(4)\nds_write_b128 v61, v[216:219]\n"
    "ds_write_b128 v61, v[220:223] offset:4096\nv_max3_u32 v188, v216, v217, v218\nv_max3_u32 v189, v220, v221, v222\nv_max3_u32 v188, v188, v219, v223\n"
    "v_max_u32 v188, v188, v189\nv_cmp_eq_u32 vcc, -1, v188\ns_waitcnt lgkmcnt(0)\ns_cbranch_vccz 42f\n"
    "buffer_load_dwordx4 v[216:219], v48, %[rc], s94 offen sc1\nbuffer_load_dwordx4 v[220:223], v58, %[rc], s94 offen sc1\n"
    "s_add_u32 %[nret], %[nret], 1\ns_waitcnt vmcnt(4)\nds_write_b128 v61, v[224:227]\nds_write_b128 v61, v[228:231] offset:4096\n"
    "v_max3_u32 v188, v224, v225, v226\nv_max3_u32 v189, v228, v229, v230\nv_max3_u32 v188, v188, v227, v231\nv_max_u32 v188, v188, v189\n"
    "v_cmp_eq_u32 vcc, -1, v188\ns_waitcnt lgkmcnt(0)\ns_cbranch_vccz 42f\nbuffer_load_dwordx4 v[224:227], v48, %[rc], s87 offen sc1\n"
    "buffer_load_dwordx4 v[228:231], v58, %[rc], s87 offen sc1\ns_sub_u32 %[bud], %[bud], 4\ns_cmp_gt_i32 %[bud], 0\ns_cbranch_scc1 41b\n42:\ns_barrier\n"
    "ds_read_b128 v[128:131], v49 offset:0\nds_read_b128 v[132:135], v49 offset:1024\nds_read_b128 v[136:139], v49 offset:2048\n"
    "ds_read_b128 v[140:143], v49 offset:3072\nds_read_b128 v[144:147], v49 offset:4096\nds_read_b128 v[148:151], v49 offset:5120\n"
    "ds_read_b128 v[152:155], v49 offset:6144\nds_read_b128 v[156:159], v49 offset:7168\ns_branch 5b\n9:\n"
    : [bud] "+s"(bud), [nret] "+s"(nret)
    : [rc] "s"(rc), [sM] "s"(sM), [wM] "s"(wM), [lkq] "s"(lkq), [lm] "s"(lm), [lnx] "s"(lnx), [sK] "s"(sK), [sFM] "s"(sFM), [wW] "s"(wW), "{v[48:63]}"(cv)
    : "memory", "vcc", "scc", CLS, CLV_96_239);
}
DI void s1_step1(rsrc_t rc, unsigned sM, int wM, unsigned lkq, unsigned lm, unsigned lnx, unsigned sK, const v16u& cv, int& bud, unsigned& nret, unsigned sv, unsigned sG) {
  asm volatile(
    "s_nop 4\nv_mov_b64 v[160:161], 0\nv_mov_b64 v[162:163], 0\nv_mov_b64 v[164:165], 0\nv_mov_b64 v[166:167], 0\nv_mov_b64 v[168:169], 0\n"
    "v_mov_b64 v[170:171], 0\nv_mov_b64 v[172:173], 0\nv_mov_b64 v[174:175], 0\nv_mov_b64 v[176:177], 0\nv_mov_b64 v[178:179], 0\nv_mov_b64 v[180:181], 0\n"
    "v_mov_b64 v[182:183], 0\nv_mov_b64 v[184:185], 0\nv_mov_b64 v[186:187], 0\ns_mov_b32 s86, 0x3e0293ee\nv_add_u32 v196, %[lkq], v62\n"
    "s_mov_b32 s94, %[sM]\ns_mov_b32 s91, 0\ns_add_u32 s87, %[sM], 65536\ns_add_u32 s88, %[sM], 98304\ns_add_u32 s89, %[sM], 114688\nv_mov_b32 v197, %[sv]\n"
    "s_barrier\ns_waitcnt lgkmcnt(0)\ns_mov_b32 s84, %[wM]\ns_branch 3f\n.p2align 6\n3:\ns_sub_u32 s84, s84, 1\ns_cbranch_scc0 3b\n4:\ns_mov_b32 m0, %[lm]\n"
    "s_nop 0\nbuffer_load_dwordx4 v48, %[rc], s94 offen sc1 lds\ns_add_u32 m0, %[lm], 4096\ns_nop 0\n"
    "buffer_load_dwordx4 v58, %[rc], s94 offen sc1 lds\ns_nop 9\ns_mov_b32 m0, %[lnx]\ns_mov_b32 s85, %[sK]\n"
    "buffer_load_dwordx4 v52, %[rc], s85 offen sc1 lds\ns_add_u32 m0, m0, 1024\ns_add_u32 s85, s85, 1024\n"
    "buffer_load_dwordx4 v52, %[rc], s85 offen sc1 lds\ns_add_u32 m0, m0, 1024\ns_add_u32 s85, s85, 1024\n"
    "buffer_load_dwordx4 v52, %[rc], s85 offen sc1 lds\ns_add_u32 m0, m0, 1024\ns_add_u32 s85, s85, 1024\n"
    "buffer_load_dwordx4 v52, %[rc], s85 offen sc1 lds\ns_waitcnt vmcnt(5)\ns_barrier\nds_read_b128 v[128:131], v49 offset:0\n"
    "ds_read_b128 v[132:135], v49 offset:1024\nds_read_b128 v[136:139], v49 offset:2048\nds_read_b128 v[140:143], v49 offset:3072\n"
    "s_waitcnt vmcnt(4)\ns_barrier\nds_read_b128 v[144:147], v49 offset:4096\nds_read_b128 v[148:151], v49 offset:5120\n"
    "ds_read_b128 v[152:155], v49 offset:6144\nds_read_b128 v[156:159], v49 offset:7168\nbuffer_store_dword v197, v60, %[rc], %[sG] offen sc1\n5:\n"
    "s_waitcnt lgkmcnt(7)\nv_smfmac_f32_16x16x64_bf16 v[160:163], v[128:131], a[0:7], v50\nv_max3_u32 v198, 0, v128, v129\n"
    "v_smfmac_f32_16x16x64_bf16 v[164:167], v[128:131], a[8:15], v50\nv_max3_u32 v198, v198, v130, v131\n"
    "v_smfmac_f32_16x16x64_bf16 v[168:171], v[128:131], a[16:23], v50\nv_smfmac_f32_16x16x64_bf16 v[172:175], v[128:131], a[24:31], v50\n"
    "s_waitcnt lgkmcnt(6)\nv_smfmac_f32_16x16x64_bf16 v[160:163], v[132:135], a[32:39], v50\nv_max3_u32 v198, v198, v132, v133\n"
    "v_smfmac_f32_16x16x64_bf16 v[164:167], v[132:135], a[40:47], v50\nv_max3_u32 v198, v198, v134, v135\n"
    "v_smfmac_f32_16x16x64_bf16 v[168:171], v[132:135], a[48:55], v50\nv_smfmac_f32_16x16x64_bf16 v[172:175], v[132:135], a[56:63], v50\n"
    "s_waitcnt lgkmcnt(0)\nv_smfmac_f32_16x16x64_bf16 v[160:163], v[136:139], a[64:71], v50\nv_max3_u32 v198, v198, v136, v137\n"
    "v_smfmac_f32_16x16x64_bf16 v[164:167], v[136:139], a[72:79], v50\nv_max3_u32 v198, v198, v138, v139\n"
    "v_smfmac_f32_16x16x64_bf16 v[168:171], v[136:139], a[80:87], v50\nv_max3_u32 v198, v198, v140, v141\n"
    "v_smfmac_f32_16x16x64_bf16 v[172:175], v[136:139], a[88:95], v50\nv_max3_u32 v198, v198, v142, v143\n"
    "v_smfmac_f32_16x16x64_bf16 v[160:163], v[140:143], a[96:103], v50\nv_max3_u32 v198, v198, v144, v145\n"
    "v_smfmac_f32_16x16x64_bf16 v[164:167], v[140:143], a[104:111], v50\nv_max3_u32 v198, v198, v146, v147\n"
    "v_smfmac_f32_16x16x64_bf16 v[168:171], v[140:143], a[112:119], v50\nv_max3_u32 v198, v198, v148, v149\n"
    "v_smfmac_f32_16x16x64_bf16 v[172:175], v[140:143], a[120:127], v50\nv_max3_u32 v198, v198, v150, v151\n"
    "v_smfmac_f32_16x16x64_bf16 v[160:163], v[144:147], a[128:135], v50\nv_max3_u32 v198, v198, v152, v153\n"
    "v_smfmac_f32_16x16x64_bf16 v[164:167], v[144:147], a[136:143], v50\nv_max3_u32 v198, v198, v154, v155\n"
    "v_smfmac_f32_16x16x64_bf16 v[168:171], v[144:147], a[144:151], v50\nv_max3_u32 v198, v198, v156, v157\n"
    "v_smfmac_f32_16x16x64_bf16 v[172:175], v[144:147], a[152:159], v50\nv_max3_u32 v198, v198, v158, v159\n"
    "v_smfmac_f32_16x16x64_bf16 v[160:163], v[148:151], a[160:167], v50\nv_cmp_eq_u32 vcc, -1, v198\n"
    "v_smfmac_f32_16x16x64_bf16 v[164:167], v[148:151], a[168:175], v50\nds_read_b128 v[96:99], v196 offset:0\n"
    "v_smfmac_f32_16x16x64_bf16 v[168:171], v[148:151], a[176:183], v50\nds_read_b128 v[100:103], v196 offset:1024\n"
    "v_smfmac_f32_16x16x64_bf16 v[172:175], v[148:151], a[184:191], v50\ns_cbranch_vccnz 7f\n6:\n"
    "v_smfmac_f32_16x16x64_bf16 v[160:163], v[152:155], a[192:199], v50\nds_read_b128 v[104:107], v196 offset:2048\n"
    "v_smfmac_f32_16x16x64_bf16 v[164:167], v[152:155], a[200:207], v50\nds_read_b128 v[108:111], v196 offset:3072\n"
    "v_smfmac_f32_16x16x64_bf16 v[168:171], v[152:155], a[208:215], v50\nds_read_b128 v[112:115], v196 offset:4096\n"
    "v_smfmac_f32_16x16x64_bf16 v[172:175], v[152:155], a[216:223], v50\nds_read_b128 v[116:119], v196 offset:5120\n"
    "v_smfmac_f32_16x16x64_bf16 v[160:163], v[156:159], a[224:231], v50\nds_read_b128 v[120:123], v196 offset:6144\n"
    "v_smfmac_f32_16x16x64_bf16 v[164:167], v[156:159], a[232:239], v50\nds_read_b128 v[124:127], v196 offset:7168\n"
    "v_smfmac_f32_16x16x64_bf16 v[168:171], v[156:159], a[240:247], v50\nv_smfmac_f32_16x16x64_bf16 v[172:175], v[156:159], a[248:255], v50\ns_nop 4\n"
    "v_add_f32 v160, v160, v162\nv_add_f32 v161, v161, v163\nv_add_f32 v164, v164, v166\nv_add_f32 v165, v165, v167\nv_cvt_pk_bf16_f32 v192, v160, v164\n"
    "v_cvt_pk_bf16_f32 v194, v161, v165\nds_write2_b32 v51, v192, v194 offset1:4\nv_add_f32 v168, v168, v170\nv_add_f32 v169, v169, v171\n"
    "v_add_f32 v172, v172, v174\nv_add_f32 v173, v173, v175\nv_cvt_pk_bf16_f32 v193, v168, v172\nv_cvt_pk_bf16_f32 v195, v169, v173\n"
    "ds_write2_b32 v51, v193, v195 offset0:8 offset1:12\ns_waitcnt lgkmcnt(0)\ns_barrier\nds_read_b128 v[128:131], v53\n"
    "ds_read_b128 v[132:135], v53 offset:1088\nds_read_b128 v[136:139], v54\nds_read_b128 v[140:143], v54 offset:16\n"
    "ds_read_b128 v[144:147], v54 offset:128\nds_read_b128 v[148:151], v54 offset:144\ns_waitcnt lgkmcnt(5)\n"
    "v_smfmac_f32_16x16x64_bf16 v[176:179], v[128:131], v[96:103], v50\nv_smfmac_f32_16x16x64_bf16 v[180:183], v[128:131], v[112:119], v50\n"
    "s_waitcnt lgkmcnt(4)\nv_smfmac_f32_16x16x64_bf16 v[176:179], v[132:135], v[104:111], v50\n"
    "v_smfmac_f32_16x16x64_bf16 v[180:183], v[132:135], v[120:127], v50\ns_waitcnt lgkmcnt(2)\n"
    "v_smfmac_f32_16x16x64_bf16 v[184:187], v[128:131], v[136:143], v50\ns_waitcnt lgkmcnt(0)\n"
    "v_smfmac_f32_16x16x64_bf16 v[184:187], v[132:135], v[144:151], v50\ns_nop 3\nv_add_f32 v188, v177, v179\nv_add_f32 v189, v181, v183\n"
    "v_max_f32 v192, v188, v189\ns_nop 0\nv_add_f32 v190, v185, v187\nv_max_f32 v192, v192, v190\nv_mul_f32 v188, s86, v188\nv_mul_f32 v189, s86, v189\n"
    "v_max_f32_dpp v192, v192, v192 row_ror:8 row_mask:0xf bank_mask:0xf\nv_mul_f32 v190, s86, v190\ns_nop 0\n"
    "v_max_f32_dpp v192, v192, v192 row_ror:4 row_mask:0xf bank_mask:0xf\ns_nop 1\n"
    "v_max_f32_dpp v192, v192, v192 quad_perm:[2,3,0,1] row_mask:0xf bank_mask:0xf\ns_nop 1\n"
    "v_max_f32_dpp v192, v192, v192 quad_perm:[1,0,3,2] row_mask:0xf bank_mask:0xf\nv_fma_f32 v188, -v192, s86, v188\nv_fma_f32 v189, -v192, s86, v189\n"
    "v_fma_f32 v190, -v192, s86, v190\nv_exp_f32 v188, v188\nv_exp_f32 v189, v189\nv_exp_f32 v190, v190\nv_add_f32 v192, v188, v189\n"
    "v_fmac_f32 v192, 0.5, v190\ns_nop 0\nv_mov_b32_dpp v191, v190 quad_perm:[1,1,3,3] row_mask:0xf bank_mask:0xf\n"
    "v_add_f32_dpp v192, v192, v192 row_ror:8 row_mask:0xf bank_mask:0xf\ns_nop 0\nv_mov_b32_dpp v190, v190 quad_perm:[0,0,2,2] row_mask:0xf bank_mask:0xf\n"
    "v_add_f32_dpp v192, v192, v192 row_ror:4 row_mask:0xf bank_mask:0xf\ns_nop 1\n"
    "v_add_f32_dpp v192, v192, v192 quad_perm:[2,3,0,1] row_mask:0xf bank_mask:0xf\ns_nop 1\n"
    "v_add_f32_dpp v192, v192, v192 quad_perm:[1,0,3,2] row_mask:0xf bank_mask:0xf\nv_rcp_f32 v192, v192\ns_nop 0\n"
    "v_pk_mul_f32 v[188:189], v[188:189], v[192:193] op_sel_hi:[1,0]\nv_cvt_pk_bf16_f32 v188, v188, v189\n"
    "buffer_store_dword v188, v55, %[rc], %[sM] offen sc1\nv_pk_mul_f32 v[190:191], v[190:191], v[192:193] op_sel_hi:[1,0]\n"
    "v_cvt_pk_bf16_f32 v190, v190, v191\nv_mov_b32 v191, v190\ns_mov_b32 vcc_lo, 0xaaaaaaaa\ns_mov_b32 vcc_hi, 0xaaaaaaaa\nv_permlane16_swap_b32 v190, v191\n"
    "v_cndmask_b32 v190, v190, v191, vcc\nv_mov_b32 v191, v190\ns_mov_b32 vcc_lo, 0xff00ff00\ns_mov_b32 vcc_hi, 0xff00ff00\n"
    "v_permlane32_swap_b32 v190, v191\nv_cndmask_b32 v190, v190, v191, vcc\nbuffer_store_dword v190, v56, %[rc], %[sM] offen sc1\ns_cmp_eq_u32 s91, 0\n"
    "s_cbranch_scc1 81f\ns_waitcnt vmcnt(2)\n81:\ns_waitcnt vmcnt(3)\ns_branch 9f\n7:\ns_cmp_eq_u32 s91, 0\ns_cbranch_scc0 6b\ns_mov_b32 s91, 1\n"
    "buffer_load_dwordx4 v[216:219], v48, %[rc], s87 offen sc1\nbuffer_load_dwordx4 v[220:223], v58, %[rc], s87 offen sc1\ns_waitcnt lgkmcnt(0)\n"
    "s_barrier\ns_nop 15\ns_nop 15\nbuffer_load_dwordx4 v[224:227], v48, %[rc], s88 offen sc1\n"
    "buffer_load_dwordx4 v[228:231], v58, %[rc], s88 offen sc1\ns_nop 15\ns_nop 15\nbuffer_load_dwordx4 v[208:211], v48, %[rc], s89 offen sc1\n"
    "buffer_load_dwordx4 v[212:215], v58, %[rc], s89 offen sc1\nv_mov_b64 v[160:161], 0\nv_mov_b64 v[162:163], 0\nv_mov_b64 v[164:165], 0\n"
    "v_mov_b64 v[166:167], 0\nv_mov_b64 v[168:169], 0\nv_mov_b64 v[170:171], 0\nv_mov_b64 v[172:173], 0\nv_mov_b64 v[174:175], 0\n"
    "s_add_u32 %[nret], %[nret], 1\ns_waitcnt vmcnt(4)\nds_write_b128 v61, v[216:219]\nds_write_b128 v61, v[220:223] offset:4096\n"
    "v_max3_u32 v188, v216, v217, v218\nv_max3_u32 v189, v220, v221, v222\nv_max3_u32 v188, v188, v219, v223\nv_max_u32 v188, v188, v189\n"
    "v_cmp_eq_u32 vcc, -1, v188\ns_waitcnt lgkmcnt(0)\ns_cbranch_vccz 42f\nbuffer_load_dwordx4 v[216:219], v48, %[rc], s94 offen sc1\n"
    "buffer_load_dwordx4 v[220:223], v58, %[rc], s94 offen sc1\ns_add_u32 %[nret], %[nret], 1\ns_waitcnt vmcnt(4)\nds_write_b128 v61, v[224:227]\n"
    "ds_write_b128 v61, v[228:231] offset:4096\nv_max3_u32 v188, v224, v225, v226\nv_max3_u32 v189, v228, v229, v230\nv_max3_u32 v188, v188, v227, v231\n"
    "v_max_u32 v188, v188, v189\nv_cmp_eq_u32 vcc, -1, v188\ns_waitcnt lgkmcnt(0)\ns_cbranch_vccz 42f\n"
    "buffer_load_dwordx4 v[224:227], v48, %[rc], s87 offen sc1\nbuffer_load_dwordx4 v[228:231], v58, %[rc], s87 offen sc1\n41:\n"
    "s_add_u32 %[nret], %[nret], 1\ns_waitcnt vmcnt(4)\nds_write_b128 v61, v[208:211]\nds_write_b128 v61, v[212:215] offset:4096\n"
    "v_max3_u32 v188, v208, v209, v210\nv_max3_u32 v189, v212, v213, v214\nv_max3_u32 v188, v188, v211, v215\nv_max_u32 v188, v188, v189\n"
    "v_cmp_eq_u32 vcc, -1, v188\ns_waitcnt lgkmcnt(0)\ns_cbranch_vccz 42f\nbuffer_load_dwordx4 v[208:211], v48, %[rc], s88 offen sc1\n"
    "buffer_load_dwordx4 v[212:215], v58, %[rc], s88 offen sc1\ns_add_u32 %[nret], %[nret], 1\ns_waitcnt vmcnt(4)\nds_write_b128 v61, v[216:219]\n"
    "ds_write_b128 v61, v[220:223] offset:4096\nv_max3_u32 v188, v216, v217, v218\nv_max3_u32 v189, v220, v221, v222\nv_max3_u32 v188, v188, v219, v223\n"
    "v_max_u32 v188, v188, v189\nv_cmp_eq_u32 vcc, -1, v188\ns_waitcnt lgkmcnt(0)\ns_cbranch_vccz 42f\n"
    "buffer_load_dwordx4 v[216:219], v48, %[rc], s89 offen sc1\nbuffer_load_dwordx4 v[220:223], v58, %[rc], s89 offen sc1\n"
    "s_add_u32 %[nret], %[nret], 1\ns_waitcnt vmcnt(4)\nds_write_b128 v61, v[224:227]\nds_write_b128 v61, v[228:231] offset:4096\n"
    "v_max3_u32 v188, v224, v225, v226\nv_max3_u32 v189, v228, v229, v230\nv_max3_u32 v188, v188, v227, v231\nv_max_u32 v188, v188, v189\n"
    "v_cmp_eq_u32 vcc, -1, v188\ns_waitcnt lgkmcnt(0)\ns_cbranch_vccz 42f\nbuffer_load_dwordx4 v[224:227], v48, %[rc], s94 offen sc1\n"
    "buffer_load_dwordx4 v[228:231], v58, %[rc], s94 offen sc1\ns_add_u32 %[nret], %[nret], 1\ns_waitcnt vmcnt(4)\nds_write_b128 v61, v[208:211]\n"
    "ds_write_b128 v61, v[212:215] offset:4096\nv_max3_u32 v188, v208, v209, v210\nv_max3_u32 v189, v212, v213, v214\nv_max3_u32 v188, v188, v211, v215\n"
    "v_max_u32 v188, v188, v189\nv_cmp_eq_u32 vcc, -1, v188\ns_waitcnt lgkmcnt(0)\ns_cbranch_vccz 42f\n"
    "buffer_load_dwordx4 v[208:211], v48, %[rc], s87 offen sc1\nbuffer_load_dwordx4 v[212:215], v58, %[rc], s87 offen sc1\n"
    "s_add_u32 %[nret], %[nret], 1\ns_waitcnt vmcnt(4)\nds_write_b128 v61, v[216:219]\nds_write_b128 v61, v[220:223] offset:4096\n"
    "v_max3_u32 v188, v216, v217, v218\nv_max3_u32 v189, v220, v221, v222\nv_max3_u32 v188, v188, v219, v223\nv_max_u32 v188, v188, v189\n"
    "v_cmp_eq_u32 vcc, -1, v188\ns_waitcnt lgkmcnt(0)\ns_cbranch_vccz 42f\nbuffer_load_dwordx4 v[216:219], v48, %[rc], s88 offen sc1\n"
    "buffer_load_dwordx4 v[220:223], v58, %[rc], s88 offen sc1\ns_add_u32 %[nret], %[nret], 1\ns_waitcnt vmcnt(4)\nds_write_b128 v61, v[224:227]\n"
    "ds_write_b128 v61, v[228:231] offset:4096\nv_max3_u32 v188, v224, v225, v226\nv_max3_u32 v189, v228, v229, v230\nv_max3_u32 v188, v188, v227, v231\n"
    "v_max_u32 v188, v188, v189\nv_cmp_eq_u32 vcc, -1, v188\ns_waitcnt lgkmcnt(0)\ns_cbranch_vccz 42f\n"
    "buffer_load_dwordx4 v[224:227], v48, %[rc], s89 offen sc1\nbuffer_load_dwordx4 v[228:231], v58, %[rc], s89 offen sc1\n"
    "s_add_u32 %[nret], %[nret], 1\ns_waitcnt vmcnt(4)\nds_write_b128 v61, v[208:211]\nds_write_b128 v61, v[212:215] offset:4096\n"
    "v_max3_u32 v188, v208, v209, v210\nv_max3_u32 v189, v212, v213, v214\nv_max3_u32 v188, v188, v211, v215\nv_max_u32 v188, v188, v189\n"
    "v_cmp_eq_u32 vcc, -1, v188\ns_waitcnt lgkmcnt(0)\ns_cbranch_vccz 42f\nbuffer_load_dwordx4 v[208:211], v48, %[rc], s94 offen sc1\n"
    "buffer_load_dwordx4 v[212:215], v58, %[rc], s94 offen sc1\ns_add_u32 %[nret], %[nret], 1\ns_waitcnt vmcnt(4)\nds_write_b128 v61, v[216:219]\n"
    "ds_write_b128 v61, v[220:223] offset:4096\nv_max3_u32 v188, v216, v217, v218\nv_max3_u32 v189, v220, v221, v222\nv_max3_u32 v188, v188, v219, v223\n"
    "v_max_u32 v188, v188, v189\nv_cmp_eq_u32 vcc, -1, v188\ns_waitcnt lgkmcnt(0)\ns_cbranch_vccz 42f\n"
    "buffer_load_dwordx4 v[216:219], v48, %[rc], s87 offen sc1\nbuffer_load_dwordx4 v[220:223], v58, %[rc], s87 offen sc1\n"
    "s_add_u32 %[nret], %[nret], 1\ns_waitcnt vmcnt(4)\nds_write_b128 v61, v[224:227]\nds_write_b128 v61, v[228:231] offset:4096\n"
    "v_max3_u32 v188, v224, v225, v226\nv_max3_u32 v189, v228, v229, v230\nv_max3_u32 v188, v188, v227, v231\nv_max_u32 v188, v188, v189\n"
    "v_cmp_eq_u32 vcc, -1, v188\ns_waitcnt lgkmcnt(0)\ns_cbranch_vccz 42f\nbuffer_load_dwordx4 v[224:227], v48, %[rc], s88 offen sc1\n"
    "buffer_load_dwordx4 v[228:231], v58, %[rc], s88 offen sc1\ns_add_u32 %[nret], %[nret], 1\ns_waitcnt vmcnt(4)\nds_write_b128 v61, v[208:211]\n"
    "ds_write_b128 v61, v[212:215] offset:4096\nv_max3_u32 v188, v208, v209, v210\nv_max3_u32 v189, v212, v213, v214\nv_max3_u32 v188, v188, v211, v215\n"
    "v_max_u32 v188, v188, v189\nv_cmp_eq_u32 vcc, -1, v188\ns_waitcnt lgkmcnt(0)\ns_cbranch_vccz 42f\n"
    "buffer_load_dwordx4 v[208:211], v48, %[rc], s89 offen sc1\nbuffer_load_dwordx4 v[212:215], v58, %[rc], s89 offen sc1\n"
    "s_add_u32 %[nret], %[nret], 1\ns_waitcnt vmcnt(4)\nds_write_b128 v61, v[216:219]\nds_write_b128 v61, v[220:223] offset:4096\n"
    "v_max3_u32 v188, v216, v217, v218\nv_max3_u32 v189, v220, v221, v222\nv_max3_u32 v188, v188, v219, v223\nv_max_u32 v188, v188, v189\n"
    "v_cmp_eq_u32 vcc, -1, v188\ns_waitcnt lgkmcnt(0)\ns_cbranch_vccz 42f\nbuffer_load_dwordx4 v[216:219], v48, %[rc], s94 offen sc1\n"
    "buffer_load_dwordx4 v[220:223], v58, %[rc], s94 offen sc1\ns_add_u32 %[nret], %[nret], 1\ns_waitcnt vmcnt(4)\nds_write_b128 v61, v[224:227]\n"
    "ds_write_b128 v61, v[228:231] offset:4096\nv_max3_u32 v188, v224, v225, v226\nv_max3_u32 v189, v228, v229, v230\nv_max3_u32 v188, v188, v227, v231\n"
    "v_max_u32 v188, v188, v189\nv_cmp_eq_u32 vcc, -1, v188\ns_waitcnt lgkmcnt(0)\ns_cbranch_vccz 42f\n"
    "buffer_load_dwordx4 v[224:227], v48, %[rc], s87 offen sc1\nbuffer_load_dwordx4 v[228:231], v58, %[rc], s87 offen sc1\n"
    "s_sub_u32 %[bud], %[bud], 4\ns_cmp_gt_i32 %[bud], 0\ns_cbranch_scc1 41b\n42:\ns_barrier\nds_read_b128 v[128:131], v49 offset:0\n"
    "ds_read_b128 v[132:135], v49 offset:1024\nds_read_b128 v[136:139], v49 offset:2048\nds_read_b128 v[140:143], v49 offset:3072\n"
    "ds_read_b128 v[144:147], v49 offset:4096\nds_read_b128 v[148:151], v49 offset:5120\nds_read_b128 v[152:155], v49 offset:6144\n"
    "ds_read_b128 v[156:159], v49 offset:7168\ns_branch 5b\n9:\n"
    : [bud] "+s"(bud), [nret] "+s"(nret)
    : [rc] "s"(rc), [sM] "s"(sM), [wM] "s"(wM), [lkq] "s"(lkq), [lm] "s"(lm), [lnx] "s"(lnx), [sK] "s"(sK), [sv] "s"(sv), [sG] "s"(sG), "{v[48:63]}"(cv)
    : "memory", "vcc", "scc", CLS, CLV_96_239);
}
DI void s1_step2(rsrc_t rc, unsigned sM, int wM, unsigned lkq, unsigned lm, unsigned lnx, unsigned sK, const v16u& cv, int& bud, unsigned& nret, unsigned lpg, unsigned sX, v8u& pt) {
  asm volatile(
    "s_nop 4\nv_mov_b64 v[160:161], 0\nv_mov_b64 v[162:163], 0\nv_mov_b64 v[164:165], 0\nv_mov_b64 v[166:167], 0\nv_mov_b64 v[168:169], 0\n"
    "v_mov_b64 v[170:171], 0\nv_mov_b64 v[172:173], 0\nv_mov_b64 v[174:175], 0\nv_add_u32 v196, %[lkq], v52\ns_mov_b32 s94, %[sM]\ns_mov_b32 s91, 0\n"
    "s_add_u32 s87, %[sM], 65536\ns_add_u32 s88, %[sM], 98304\ns_add_u32 s89, %[sM], 114688\ns_barrier\ns_waitcnt lgkmcnt(0)\ns_mov_b32 s84, %[wM]\n"
    "s_branch 3f\n.p2align 6\n3:\ns_sub_u32 s84, s84, 1\ns_cbranch_scc0 3b\n4:\ns_mov_b32 m0, %[lm]\ns_nop 0\n"
    "buffer_load_dwordx4 v48, %[rc], s94 offen sc1 lds\ns_add_u32 m0, %[lm], 4096\ns_nop 0\nbuffer_load_dwordx4 v58, %[rc], s94 offen sc1 lds\n"
    "s_nop 9\ns_mov_b32 m0, %[lnx]\ns_mov_b32 s85, %[sK]\nbuffer_load_dwordx4 v52, %[rc], s85 offen sc1 lds\ns_add_u32 m0, m0, 1024\n"
    "s_add_u32 s85, s85, 1024\nbuffer_load_dwordx4 v52, %[rc], s85 offen sc1 lds\ns_add_u32 m0, m0, 1024\ns_add_u32 s85, s85, 1024\n"
    "buffer_load_dwordx4 v52, %[rc], s85 offen sc1 lds\ns_add_u32 m0, m0, 1024\ns_add_u32 s85, s85, 1024\n"
    "buffer_load_dwordx4 v52, %[rc], s85 offen sc1 lds\nbuffer_store_dwordx4 v[64:67], v55, %[rc], %[sX] offen sc1\n"
    "buffer_store_dwordx4 v[68:71], v56, %[rc], %[sX] offen sc1\ns_mov_b32 m0, %[lpg]\ns_nop 0\nbuffer_load_dword v59, %[rc], 0 offen sc1 lds\n"
    "s_waitcnt vmcnt(8)\ns_barrier\nds_read_b128 v[128:131], v49 offset:0\nds_read_b128 v[132:135], v49 offset:1024\n"
    "ds_read_b128 v[136:139], v49 offset:2048\nds_read_b128 v[140:143], v49 offset:3072\ns_waitcnt vmcnt(7)\ns_barrier\n"
    "ds_read_b128 v[144:147], v49 offset:4096\nds_read_b128 v[148:151], v49 offset:5120\nds_read_b128 v[152:155], v49 offset:6144\n"
    "ds_read_b128 v[156:159], v49 offset:7168\n5:\ns_waitcnt lgkmcnt(7)\nv_smfmac_f32_16x16x64_bf16 v[160:163], v[128:131], a[0:7], v50\n"
    "v_max3_u32 v198, 0, v128, v129\nv_smfmac_f32_16x16x64_bf16 v[164:167], v[128:131], a[8:15], v50\nv_max3_u32 v198, v198, v130, v131\n"
    "v_smfmac_f32_16x16x64_bf16 v[168:171], v[128:131], a[16:23], v50\nv_smfmac_f32_16x16x64_bf16 v[172:175], v[128:131], a[24:31], v50\n"
    "s_waitcnt lgkmcnt(6)\nv_smfmac_f32_16x16x64_bf16 v[160:163], v[132:135], a[32:39], v50\nv_max3_u32 v198, v198, v132, v133\n"
    "v_smfmac_f32_16x16x64_bf16 v[164:167], v[132:135], a[40:47], v50\nv_max3_u32 v198, v198, v134, v135\n"
    "v_smfmac_f32_16x16x64_bf16 v[168:171], v[132:135], a[48:55], v50\nv_smfmac_f32_16x16x64_bf16 v[172:175], v[132:135], a[56:63], v50\n"
    "s_waitcnt lgkmcnt(0)\nv_smfmac_f32_16x16x64_bf16 v[160:163], v[136:139], a[64:71], v50\nv_max3_u32 v198, v198, v136, v137\n"
    "v_smfmac_f32_16x16x64_bf16 v[164:167], v[136:139], a[72:79], v50\nv_max3_u32 v198, v198, v138, v139\n"
    "v_smfmac_f32_16x16x64_bf16 v[168:171], v[136:139], a[80:87], v50\nv_max3_u32 v198, v198, v140, v141\n"
    "v_smfmac_f32_16x16x64_bf16 v[172:175], v[136:139], a[88:95], v50\nv_max3_u32 v198, v198, v142, v143\n"
    "v_smfmac_f32_16x16x64_bf16 v[160:163], v[140:143], a[96:103], v50\nv_max3_u32 v198, v198, v144, v145\n"
    "v_smfmac_f32_16x16x64_bf16 v[164:167], v[140:143], a[104:111], v50\nv_max3_u32 v198, v198, v146, v147\n"
    "v_smfmac_f32_16x16x64_bf16 v[168:171], v[140:143], a[112:119], v50\nv_max3_u32 v198, v198, v148, v149\n"
    "v_smfmac_f32_16x16x64_bf16 v[172:175], v[140:143], a[120:127], v50\nv_max3_u32 v198, v198, v150, v151\n"
    "v_smfmac_f32_16x16x64_bf16 v[160:163], v[144:147], a[128:135], v50\nv_max3_u32 v198, v198, v152, v153\n"
    "v_smfmac_f32_16x16x64_bf16 v[164:167], v[144:147], a[136:143], v50\nv_max3_u32 v198, v198, v154, v155\n"
    "v_smfmac_f32_16x16x64_bf16 v[168:171], v[144:147], a[144:151], v50\nv_max3_u32 v198, v198, v156, v157\n"
    "v_smfmac_f32_16x16x64_bf16 v[172:175], v[144:147], a[152:159], v50\nv_max3_u32 v198, v198, v158, v159\n"
    "v_smfmac_f32_16x16x64_bf16 v[160:163], v[148:151], a[160:167], v50\nv_cmp_eq_u32 vcc, -1, v198\n"
    "v_smfmac_f32_16x16x64_bf16 v[164:167], v[148:151], a[168:175], v50\nv_smfmac_f32_16x16x64_bf16 v[168:171], v[148:151], a[176:183], v50\n"
    "v_smfmac_f32_16x16x64_bf16 v[172:175], v[148:151], a[184:191], v50\ns_cbranch_vccnz 7f\n6:\n"
    "v_smfmac_f32_16x16x64_bf16 v[160:163], v[152:155], a[192:199], v50\nv_smfmac_f32_16x16x64_bf16 v[164:167], v[152:155], a[200:207], v50\n"
    "v_smfmac_f32_16x16x64_bf16 v[168:171], v[152:155], a[208:215], v50\nv_smfmac_f32_16x16x64_bf16 v[172:175], v[152:155], a[216:223], v50\n"
    "v_smfmac_f32_16x16x64_bf16 v[160:163], v[156:159], a[224:231], v50\nv_smfmac_f32_16x16x64_bf16 v[164:167], v[156:159], a[232:239], v50\n"
    "v_smfmac_f32_16x16x64_bf16 v[168:171], v[156:159], a[240:247], v50\nv_smfmac_f32_16x16x64_bf16 v[172:175], v[156:159], a[248:255], v50\ns_nop 4\n"
    "v_add_f32 v160, v160, v162\nv_add_f32 v161, v161, v163\nv_add_f32 v164, v164, v166\nv_add_f32 v165, v165, v167\nv_cvt_pk_bf16_f32 v192, v160, v164\n"
    "v_cvt_pk_bf16_f32 v194, v161, v165\nv_add_f32 v168, v168, v170\nv_add_f32 v169, v169, v171\nv_add_f32 v172, v172, v174\nv_add_f32 v173, v173, v175\n"
    "v_cvt_pk_bf16_f32 v193, v168, v172\nv_cvt_pk_bf16_f32 v195, v169, v173\nds_write2_b64 v51, v[192:193], v[194:195] offset1:34\ns_waitcnt lgkmcnt(0)\n"
    "s_barrier\ns_nop 15\nds_read_b128 v[96:99], v196 offset:8192\nds_read_b128 v[112:115], v54 offset:0\nds_read_b128 v[128:131], v196 offset:0\n"
    "ds_read_b128 v[100:103], v196 offset:9216\nds_read_b128 v[116:119], v54 offset:16\nds_read_b128 v[132:135], v196 offset:1024\n"
    "ds_read_b128 v[104:107], v196 offset:10240\nds_read_b128 v[120:123], v54 offset:128\nds_read_b128 v[136:139], v196 offset:2048\n"
    "ds_read_b128 v[108:111], v196 offset:11264\nds_read_b128 v[124:127], v54 offset:144\nds_read_b128 v[140:143], v196 offset:3072\n"
    "s_mov_b32 s86, 0xffff0000\ns_mov_b32 s88, 0x3db504f3\ns_mov_b32 s89, 0x3fb8aa3b\nv_mov_b32 v194, 0xce6e6b28\ns_waitcnt lgkmcnt(0)\n"
    "v_mfma_f32_16x16x32_bf16 v[160:163], v[112:115], v[96:99], 0\nv_mfma_f32_16x16x32_bf16 v[160:163], v[116:119], v[100:103], v[160:163]\n"
    "v_mfma_f32_16x16x32_bf16 v[160:163], v[120:123], v[104:107], v[160:163]\nv_mfma_f32_16x16x32_bf16 v[160:163], v[124:127], v[108:111], v[160:163]\n"
    "v_mfma_f32_16x16x32_bf16 v[164:167], v[128:131], v[96:99], 0\nv_mfma_f32_16x16x32_bf16 v[164:167], v[132:135], v[100:103], v[164:167]\n"
    "v_mfma_f32_16x16x32_bf16 v[164:167], v[136:139], v[104:107], v[164:167]\nv_mfma_f32_16x16x32_bf16 v[164:167], v[140:143], v[108:111], v[164:167]\n"
    "s_nop 3\nv_cvt_pk_bf16_f32 v176, v160, v161\nv_cvt_pk_bf16_f32 v177, v162, v163\nv_lshlrev_b32 v160, 16, v176\nv_and_b32 v161, s86, v176\n"
    "v_lshlrev_b32 v162, 16, v177\nv_and_b32 v163, s86, v177\nv_mul_f32 v160, s88, v160\nv_mul_f32 v161, s88, v161\nv_mul_f32 v162, s88, v162\n"
    "v_mul_f32 v163, s88, v163\nv_cvt_pk_bf16_f32 v176, v160, v161\nv_cvt_pk_bf16_f32 v177, v162, v163\nv_lshlrev_b32 v160, 16, v176\n"
    "v_and_b32 v161, s86, v176\nv_lshlrev_b32 v162, 16, v177\nv_and_b32 v163, s86, v177\nv_cvt_pk_bf16_f32 v176, v164, v165\n"
    "v_cvt_pk_bf16_f32 v177, v166, v167\nv_lshlrev_b32 v164, 16, v176\nv_and_b32 v165, s86, v176\nv_lshlrev_b32 v166, 16, v177\nv_and_b32 v167, s86, v177\n"
    "v_mul_f32 v164, s88, v164\nv_mul_f32 v165, s88, v165\nv_mul_f32 v166, s88, v166\nv_mul_f32 v167, s88, v167\nv_cvt_pk_bf16_f32 v176, v164, v165\n"
    "v_cvt_pk_bf16_f32 v177, v166, v167\nv_lshlrev_b32 v164, 16, v176\nv_and_b32 v165, s86, v176\nv_lshlrev_b32 v166, 16, v177\nv_and_b32 v167, s86, v177\n"
    "s_mov_b32 s95, s91\nv_cmp_le_i32 vcc, 0, v62\nv_cmp_le_i32 s[84:85], 1, v62\nv_cmp_le_i32 s[90:91], 2, v62\nv_cmp_le_i32 s[92:93], 3, v62\n"
    "v_max3_f32 v192, v160, v161, v162\nv_cndmask_b32 v164, v194, v164, vcc\nv_cndmask_b32 v165, v194, v165, s[84:85]\n"
    "v_cndmask_b32 v166, v194, v166, s[90:91]\nv_cndmask_b32 v167, v194, v167, s[92:93]\ns_mov_b32 s91, s95\nv_max3_f32 v192, v192, v163, v164\n"
    "v_max3_f32 v192, v192, v165, v166\nv_max_f32 v192, v192, v167\nv_mov_b32 v193, v192\ns_nop 1\nv_permlane16_swap_b32 v192, v193\n"
    "v_max_f32 v192, v192, v193\nv_mov_b32 v193, v192\ns_nop 1\nv_permlane32_swap_b32 v192, v193\nv_max_f32 v192, v192, v193\nv_sub_f32 v160, v160, v192\n"
    "v_sub_f32 v161, v161, v192\nv_sub_f32 v162, v162, v192\nv_sub_f32 v163, v163, v192\nv_sub_f32 v164, v164, v192\nv_sub_f32 v165, v165, v192\n"
    "v_sub_f32 v166, v166, v192\nv_sub_f32 v167, v167, v192\nv_mul_f32 v160, s89, v160\nv_mul_f32 v161, s89, v161\nv_mul_f32 v162, s89, v162\n"
    "v_mul_f32 v163, s89, v163\nv_mul_f32 v164, s89, v164\nv_mul_f32 v165, s89, v165\nv_mul_f32 v166, s89, v166\nv_mul_f32 v167, s89, v167\n"
    "v_exp_f32 v160, v160\nv_exp_f32 v161, v161\nv_exp_f32 v162, v162\nv_exp_f32 v163, v163\nv_exp_f32 v164, v164\nv_exp_f32 v165, v165\n"
    "v_exp_f32 v166, v166\nv_exp_f32 v167, v167\nv_add_f32 v192, v160, v161\nv_add_f32 v193, v162, v163\nv_add_f32 v192, v192, v193\n"
    "v_fma_f32 v192, v192, 0.5, v164\nv_add_f32 v192, v192, v165\nv_add_f32 v192, v192, v166\nv_add_f32 v192, v192, v167\nv_mov_b32 v193, v192\ns_nop 1\n"
    "v_permlane16_swap_b32 v192, v193\nv_add_f32 v192, v192, v193\nv_mov_b32 v193, v192\ns_nop 1\nv_permlane32_swap_b32 v192, v193\n"
    "v_add_f32 v192, v192, v193\nv_rcp_f32 v192, v192\ns_nop 0\nv_pk_mul_f32 v[160:161], v[160:161], v[192:193] op_sel_hi:[1,0]\n"
    "v_pk_mul_f32 v[162:163], v[162:163], v[192:193] op_sel_hi:[1,0]\nv_pk_mul_f32 v[164:165], v[164:165], v[192:193] op_sel_hi:[1,0]\n"
    "v_pk_mul_f32 v[166:167], v[166:167], v[192:193] op_sel_hi:[1,0]\nv_cvt_pk_bf16_f32 v68, v160, v161\nv_cvt_pk_bf16_f32 v69, v162, v163\n"
    "v_mov_b32 v70, v68\nv_mov_b32 v71, v69\nv_cvt_pk_bf16_f32 v64, v164, 0\nv_cvt_pk_bf16_f32 v65, v165, 0\nv_cvt_pk_bf16_f32 v66, v166, 0\n"
    "v_cvt_pk_bf16_f32 v67, v167, 0\nv_permlane16_swap_b32 v68, v70\nv_permlane16_swap_b32 v69, v71\ns_cmp_eq_u32 s91, 0\ns_cbranch_scc1 81f\n"
    "s_waitcnt vmcnt(0)\n81:\ns_waitcnt vmcnt(3)\ns_branch 9f\n7:\ns_cmp_eq_u32 s91, 0\ns_cbranch_scc0 6b\ns_mov_b32 s91, 1\n"
    "buffer_load_dwordx4 v[216:219], v48, %[rc], s87 offen sc1\nbuffer_load_dwordx4 v[220:223], v58, %[rc], s87 offen sc1\ns_waitcnt lgkmcnt(0)\n"
    "s_barrier\ns_nop 15\ns_nop 15\nbuffer_load_dwordx4 v[224:227], v48, %[rc], s88 offen sc1\n"
    "buffer_load_dwordx4 v[228:231], v58, %[rc], s88 offen sc1\ns_nop 15\ns_nop 15\nbuffer_load_dwordx4 v[208:211], v48, %[rc], s89 offen sc1\n"
    "buffer_load_dwordx4 v[212:215], v58, %[rc], s89 offen sc1\nv_mov_b64 v[160:161], 0\nv_mov_b64 v[162:163], 0\nv_mov_b64 v[164:165], 0\n"
    "v_mov_b64 v[166:167], 0\nv_mov_b64 v[168:169], 0\nv_mov_b64 v[170:171], 0\nv_mov_b64 v[172:173], 0\nv_mov_b64 v[174:175], 0\n"
    "s_add_u32 %[nret], %[nret], 1\ns_waitcnt vmcnt(4)\nds_write_b128 v61, v[216:219]\nds_write_b128 v61, v[220:223] offset:4096\n"
    "v_max3_u32 v188, v216, v217, v218\nv_max3_u32 v189, v220, v221, v222\nv_max3_u32 v188, v188, v219, v223\nv_max_u32 v188, v188, v189\n"
    "v_cmp_eq_u32 vcc, -1, v188\ns_waitcnt lgkmcnt(0)\ns_cbranch_vccz 42f\nbuffer_load_dwordx4 v[216:219], v48, %[rc], s94 offen sc1\n"
    "buffer_load_dwordx4 v[220:223], v58, %[rc], s94 offen sc1\ns_add_u32 %[nret], %[nret], 1\ns_waitcnt vmcnt(4)\nds_write_b128 v61, v[224:227]\n"
    "ds_write_b128 v61, v[228:231] offset:4096\nv_max3_u32 v188, v224, v225, v226\nv_max3_u32 v189, v228, v229, v230\nv_max3_u32 v188, v188, v227, v231\n"
    "v_max_u32 v188, v188, v189\nv_cmp_eq_u32 vcc, -1, v188\ns_waitcnt lgkmcnt(0)\ns_cbranch_vccz 42f\n"
    "buffer_load_dwordx4 v[224:227], v48, %[rc], s87 offen sc1\nbuffer_load_dwordx4 v[228:231], v58, %[rc], s87 offen sc1\n41:\n"
    "s_add_u32 %[nret], %[nret], 1\ns_waitcnt vmcnt(4)\nds_write_b128 v61, v[208:211]\nds_write_b128 v61, v[212:215] offset:4096\n"
    "v_max3_u32 v188, v208, v209, v210\nv_max3_u32 v189, v212, v213, v214\nv_max3_u32 v188, v188, v211, v215\nv_max_u32 v188, v188, v189\n"
    "v_cmp_eq_u32 vcc, -1, v188\ns_waitcnt lgkmcnt(0)\ns_cbranch_vccz 42f\nbuffer_load_dwordx4 v[208:211], v48, %[rc], s88 offen sc1\n"
    "buffer_load_dwordx4 v[212:215], v58, %[rc], s88 offen sc1\ns_add_u32 %[nret], %[nret], 1\ns_waitcnt vmcnt(4)\nds_write_b128 v61, v[216:219]\n"
    "ds_write_b128 v61, v[220:223] offset:4096\nv_max3_u32 v188, v216, v217, v218\nv_max3_u32 v189, v220, v221, v222\nv_max3_u32 v188, v188, v219, v223\n"
    "v_max_u32 v188, v188, v189\nv_cmp_eq_u32 vcc, -1, v188\ns_waitcnt lgkmcnt(0)\ns_cbranch_vccz 42f\n"
    "buffer_load_dwordx4 v[216:219], v48, %[rc], s89 offen sc1\nbuffer_load_dwordx4 v[220:223], v58, %[rc], s89 offen sc1\n"
    "s_add_u32 %[nret], %[nret], 1\ns_waitcnt vmcnt(4)\nds_write_b128 v61, v[224:227]\nds_write_b128 v61, v[228:231] offset:4096\n"
    "v_max3_u32 v188, v224, v225, v226\nv_max3_u32 v189, v228, v229, v230\nv_max3_u32 v188, v188, v227, v231\nv_max_u32 v188, v188, v189\n"
    "v_cmp_eq_u32 vcc, -1, v188\ns_waitcnt lgkmcnt(0)\ns_cbranch_vccz 42f\nbuffer_load_dwordx4 v[224:227], v48, %[rc], s94 offen sc1\n"
    "buffer_load_dwordx4 v[228:231], v58, %[rc], s94 offen sc1\ns_add_u32 %[nret], %[nret], 1\ns_waitcnt vmcnt(4)\nds_write_b128 v61, v[208:211]\n"
    "ds_write_b128 v61, v[212:215] offset:4096\nv_max3_u32 v188, v208, v209, v210\nv_max3_u32 v189, v212, v213, v214\nv_max3_u32 v188, v188, v211, v215\n"
    "v_max_u32 v188, v188, v189\nv_cmp_eq_u32 vcc, -1, v188\ns_waitcnt lgkmcnt(0)\ns_cbranch_vccz 42f\n"
    "buffer_load_dwordx4 v[208:211], v48, %[rc], s87 offen sc1\nbuffer_load_dwordx4 v[212:215], v58, %[rc], s87 offen sc1\n"
    "s_add_u32 %[nret], %[nret], 1\ns_waitcnt vmcnt(4)\nds_write_b128 v61, v[216:219]\nds_write_b128 v61, v[220:223] offset:4096\n"
    "v_max3_u32 v188, v216, v217, v218\nv_max3_u32 v189, v220, v221, v222\nv_max3_u32 v188, v188, v219, v223\nv_max_u32 v188, v188, v189\n"
    "v_cmp_eq_u32 vcc, -1, v188\ns_waitcnt lgkmcnt(0)\ns_cbranch_vccz 42f\nbuffer_load_dwordx4 v[216:219], v48, %[rc], s88 offen sc1\n"
    "buffer_load_dwordx4 v[220:223], v58, %[rc], s88 offen sc1\ns_add_u32 %[nret], %[nret], 1\ns_waitcnt vmcnt(4)\nds_write_b128 v61, v[224:227]\n"
    "ds_write_b128 v61, v[228:231] offset:4096\nv_max3_u32 v188, v224, v225, v226\nv_max3_u32 v189, v228, v229, v230\nv_max3_u32 v188, v188, v227, v231\n"
    "v_max_u32 v188, v188, v189\nv_cmp_eq_u32 vcc, -1, v188\ns_waitcnt lgkmcnt(0)\ns_cbranch_vccz 42f\n"
    "buffer_load_dwordx4 v[224:227], v48, %[rc], s89 offen sc1\nbuffer_load_dwordx4 v[228:231], v58, %[rc], s89 offen sc1\n"
    "s_add_u32 %[nret], %[nret], 1\ns_waitcnt vmcnt(4)\nds_write_b128 v61, v[208:211]\nds_write_b128 v61, v[212:215] offset:4096\n"
    "v_max3_u32 v188, v208, v209, v210\nv_max3_u32 v189, v212, v213, v214\nv_max3_u32 v188, v188, v211, v215\nv_max_u32 v188, v188, v189\n"
    "v_cmp_eq_u32 vcc, -1, v188\ns_waitcnt lgkmcnt(0)\ns_cbranch_vccz 42f\nbuffer_load_dwordx4 v[208:211], v48, %[rc], s94 offen sc1\n"
    "buffer_load_dwordx4 v[212:215], v58, %[rc], s94 offen sc1\ns_add_u32 %[nret], %[nret], 1\ns_waitcnt vmcnt(4)\nds_write_b128 v61, v[216:219]\n"
    "ds_write_b128 v61, v[220:223] offset:4096\nv_max3_u32 v188, v216, v217, v218\nv_max3_u32 v189, v220, v221, v222\nv_max3_u32 v188, v188, v219, v223\n"
    "v_max_u32 v188, v188, v189\nv_cmp_eq_u32 vcc, -1, v188\ns_waitcnt lgkmcnt(0)\ns_cbranch_vccz 42f\n"
    "buffer_load_dwordx4 v[216:219], v48, %[rc], s87 offen sc1\nbuffer_load_dwordx4 v[220:223], v58, %[rc], s87 offen sc1\n"
    "s_add_u32 %[nret], %[nret], 1\ns_waitcnt vmcnt(4)\nds_write_b128 v61, v[224:227]\nds_write_b128 v61, v[228:231] offset:4096\n"
    "v_max3_u32 v188, v224, v225, v226\nv_max3_u32 v189, v228, v229, v230\nv_max3_u32 v188, v188, v227, v231\nv_max_u32 v188, v188, v189\n"
    "v_cmp_eq_u32 vcc, -1, v188\ns_waitcnt lgkmcnt(0)\ns_cbranch_vccz 42f\nbuffer_load_dwordx4 v[224:227], v48, %[rc], s88 offen sc1\n"
    "buffer_load_dwordx4 v[228:231], v58, %[rc], s88 offen sc1\ns_add_u32 %[nret], %[nret], 1\ns_waitcnt vmcnt(4)\nds_write_b128 v61, v[208:211]\n"
    "ds_write_b128 v61, v[212:215] offset:4096\nv_max3_u32 v188, v208, v209, v210\nv_max3_u32 v189, v212, v213, v214\nv_max3_u32 v188, v188, v211, v215\n"
    "v_max_u32 v188, v188, v189\nv_cmp_eq_u32 vcc, -1, v188\ns_waitcnt lgkmcnt(0)\ns_cbranch_vccz 42f\n"
    "buffer_load_dwordx4 v[208:211], v48, %[rc], s89 offen sc1\nbuffer_load_dwordx4 v[212:215], v58, %[rc], s89 offen sc1\n"
    "s_add_u32 %[nret], %[nret], 1\ns_waitcnt vmcnt(4)\nds_write_b128 v61, v[216:219]\nds_write_b128 v61, v[220:223] offset:4096\n"
    "v_max3_u32 v188, v216, v217, v218\nv_max3_u32 v189, v220, v221, v222\nv_max3_u32 v188, v188, v219, v223\nv_max_u32 v188, v188, v189\n"
    "v_cmp_eq_u32 vcc, -1, v188\ns_waitcnt lgkmcnt(0)\ns_cbranch_vccz 42f\nbuffer_load_dwordx4 v[216:219], v48, %[rc], s94 offen sc1\n"
    "buffer_load_dwordx4 v[220:223], v58, %[rc], s94 offen sc1\ns_add_u32 %[nret], %[nret], 1\ns_waitcnt vmcnt(4)\nds_write_b128 v61, v[224:227]\n"
    "ds_write_b128 v61, v[228:231] offset:4096\nv_max3_u32 v188, v224, v225, v226\nv_max3_u32 v189, v228, v229, v230\nv_max3_u32 v188, v188, v227, v231\n"
    "v_max_u32 v188, v188, v189\nv_cmp_eq_u32 vcc, -1, v188\ns_waitcnt lgkmcnt(0)\ns_cbranch_vccz 42f\n"
    "buffer_load_dwordx4 v[224:227], v48, %[rc], s87 offen sc1\nbuffer_load_dwordx4 v[228:231], v58, %[rc], s87 offen sc1\n"
    "s_sub_u32 %[bud], %[bud], 4\ns_cmp_gt_i32 %[bud], 0\ns_cbranch_scc1 41b\n42:\ns_barrier\nds_read_b128 v[128:131], v49 offset:0\n"
    "ds_read_b128 v[132:135], v49 offset:1024\nds_read_b128 v[136:139], v49 offset:2048\nds_read_b128 v[140:143], v49 offset:3072\n"
    "ds_read_b128 v[144:147], v49 offset:4096\nds_read_b128 v[148:151], v49 offset:5120\nds_read_b128 v[152:155], v49 offset:6144\n"
    "ds_read_b128 v[156:159], v49 offset:7168\ns_branch 5b\n9:\n"
    : [bud] "+s"(bud), [nret] "+s"(nret), "+{v[64:71]}"(pt)
    : [rc] "s"(rc), [sM] "s"(sM), [wM] "s"(wM), [lkq] "s"(lkq), [lm] "s"(lm), [lnx] "s"(lnx), [sK] "s"(sK), [lpg] "s"(lpg), [sX] "s"(sX), "{v[48:63]}"(cv)
    : "memory", "vcc", "scc", CLS, CLV_96_239);
}
DI void s1_step3(rsrc_t rc, unsigned sM, int wM, unsigned lkq, unsigned lm, unsigned lnx, unsigned sK, const v16u& cv, int& bud, unsigned& nret, unsigned lpg, unsigned sX, v8u& pt) {
  asm volatile(
    "s_nop 4\nv_mov_b64 v[160:161], 0\nv_mov_b64 v[162:163], 0\nv_mov_b64 v[164:165], 0\nv_mov_b64 v[166:167], 0\nv_mov_b64 v[168:169], 0\n"
    "v_mov_b64 v[170:171], 0\nv_mov_b64 v[172:173], 0\nv_mov_b64 v[174:175], 0\nv_mov_b32 v191, 1\nv_add_u32 v196, %[lkq], v52\ns_mov_b32 s94, %[sM]\n"
    "s_mov_b32 s91, 0\ns_add_u32 s87, %[sM], 65536\ns_add_u32 s88, %[sM], 98304\ns_add_u32 s89, %[sM], 114688\ns_barrier\ns_waitcnt lgkmcnt(0)\n"
    "s_mov_b32 s84, %[wM]\ns_branch 3f\n.p2align 6\n3:\ns_sub_u32 s84, s84, 1\ns_cbranch_scc0 3b\n4:\ns_mov_b32 m0, %[lm]\ns_nop 0\n"
    "buffer_load_dwordx4 v48, %[rc], s94 offen sc1 lds\ns_add_u32 m0, %[lm], 4096\ns_nop 0\nbuffer_load_dwordx4 v58, %[rc], s94 offen sc1 lds\n"
    "s_nop 9\ns_mov_b32 m0, %[lnx]\ns_mov_b32 s85, %[sK]\nbuffer_load_dwordx4 v52, %[rc], s85 offen sc1 lds\ns_add_u32 m0, m0, 1024\n"
    "s_add_u32 s85, s85, 1024\nbuffer_load_dwordx4 v52, %[rc], s85 offen sc1 lds\ns_add_u32 m0, m0, 1024\ns_add_u32 s85, s85, 1024\n"
    "buffer_load_dwordx4 v52, %[rc], s85 offen sc1 lds\ns_add_u32 m0, m0, 1024\ns_add_u32 s85, s85, 1024\n"
    "buffer_load_dwordx4 v52, %[rc], s85 offen sc1 lds\nbuffer_store_dwordx4 v[64:67], v55, %[rc], %[sX] offen sc1\n"
    "buffer_store_dwordx4 v[68:71], v56, %[rc], %[sX] offen sc1\ns_mov_b32 m0, %[lpg]\ns_nop 0\nbuffer_load_dword v59, %[rc], 0 offen sc1 lds\n"
    "s_waitcnt vmcnt(8)\ns_barrier\nds_read_b128 v[128:131], v49 offset:0\nds_read_b128 v[132:135], v49 offset:1024\n"
    "ds_read_b128 v[136:139], v49 offset:2048\nds_read_b128 v[140:143], v49 offset:3072\ns_waitcnt vmcnt(7)\ns_barrier\n"
    "ds_read_b128 v[144:147], v49 offset:4096\nds_read_b128 v[148:151], v49 offset:5120\nds_read_b128 v[152:155], v49 offset:6144\n"
    "ds_read_b128 v[156:159], v49 offset:7168\n5:\ns_waitcnt lgkmcnt(7)\nv_smfmac_f32_16x16x64_bf16 v[160:163], v[128:131], a[0:7], v50\n"
    "v_max3_u32 v198, 0, v128, v129\nv_smfmac_f32_16x16x64_bf16 v[164:167], v[128:131], a[8:15], v50\nv_max3_u32 v198, v198, v130, v131\n"
    "v_smfmac_f32_16x16x64_bf16 v[168:171], v[128:131], a[16:23], v50\nv_smfmac_f32_16x16x64_bf16 v[172:175], v[128:131], a[24:31], v50\n"
    "s_waitcnt lgkmcnt(6)\nv_smfmac_f32_16x16x64_bf16 v[160:163], v[132:135], a[32:39], v50\nv_max3_u32 v198, v198, v132, v133\n"
    "v_smfmac_f32_16x16x64_bf16 v[164:167], v[132:135], a[40:47], v50\nv_max3_u32 v198, v198, v134, v135\n"
    "v_smfmac_f32_16x16x64_bf16 v[168:171], v[132:135], a[48:55], v50\nv_smfmac_f32_16x16x64_bf16 v[172:175], v[132:135], a[56:63], v50\n"
    "s_waitcnt lgkmcnt(0)\nv_smfmac_f32_16x16x64_bf16 v[160:163], v[136:139], a[64:71], v50\nv_max3_u32 v198, v198, v136, v137\n"
    "v_smfmac_f32_16x16x64_bf16 v[164:167], v[136:139], a[72:79], v50\nv_max3_u32 v198, v198, v138, v139\n"
    "v_smfmac_f32_16x16x64_bf16 v[168:171], v[136:139], a[80:87], v50\nv_max3_u32 v198, v198, v140, v141\n"
    "v_smfmac_f32_16x16x64_bf16 v[172:175], v[136:139], a[88:95], v50\nv_max3_u32 v198, v198, v142, v143\n"
    "v_smfmac_f32_16x16x64_bf16 v[160:163], v[140:143], a[96:103], v50\nv_max3_u32 v198, v198, v144, v145\n"
    "v_smfmac_f32_16x16x64_bf16 v[164:167], v[140:143], a[104:111], v50\nv_max3_u32 v198, v198, v146, v147\n"
    "v_smfmac_f32_16x16x64_bf16 v[168:171], v[140:143], a[112:119], v50\nv_max3_u32 v198, v198, v148, v149\n"
    "v_smfmac_f32_16x16x64_bf16 v[172:175], v[140:143], a[120:127], v50\nv_max3_u32 v198, v198, v150, v151\n"
    "v_smfmac_f32_16x16x64_bf16 v[160:163], v[144:147], a[128:135], v50\nv_max3_u32 v198, v198, v152, v153\n"
    "v_smfmac_f32_16x16x64_bf16 v[164:167], v[144:147], a[136:143], v50\nv_max3_u32 v198, v198, v154, v155\n"
    "v_smfmac_f32_16x16x64_bf16 v[168:171], v[144:147], a[144:151], v50\nv_max3_u32 v198, v198, v156, v157\n"
    "v_smfmac_f32_16x16x64_bf16 v[172:175], v[144:147], a[152:159], v50\nv_max3_u32 v198, v198, v158, v159\n"
    "v_smfmac_f32_16x16x64_bf16 v[160:163], v[148:151], a[160:167], v50\nv_cmp_eq_u32 vcc, -1, v198\n"
    "v_smfmac_f32_16x16x64_bf16 v[164:167], v[148:151], a[168:175], v50\nv_smfmac_f32_16x16x64_bf16 v[168:171], v[148:151], a[176:183], v50\n"
    "v_smfmac_f32_16x16x64_bf16 v[172:175], v[148:151], a[184:191], v50\ns_cbranch_vccnz 7f\n6:\n"
    "v_smfmac_f32_16x16x64_bf16 v[160:163], v[152:155], a[192:199], v50\nv_smfmac_f32_16x16x64_bf16 v[164:167], v[152:155], a[200:207], v50\n"
    "v_smfmac_f32_16x16x64_bf16 v[168:171], v[152:155], a[208:215], v50\nv_smfmac_f32_16x16x64_bf16 v[172:175], v[152:155], a[216:223], v50\n"
    "v_smfmac_f32_16x16x64_bf16 v[160:163], v[156:159], a[224:231], v50\nv_smfmac_f32_16x16x64_bf16 v[164:167], v[156:159], a[232:239], v50\n"
    "v_smfmac_f32_16x16x64_bf16 v[168:171], v[156:159], a[240:247], v50\nv_smfmac_f32_16x16x64_bf16 v[172:175], v[156:159], a[248:255], v50\ns_nop 4\n"
    "v_add_f32 v160, v160, v162\nv_add_f32 v161, v161, v163\nv_add_f32 v164, v164, v166\nv_add_f32 v165, v165, v167\nv_cvt_pk_bf16_f32 v192, v160, v164\n"
    "v_cvt_pk_bf16_f32 v194, v161, v165\nv_add_f32 v168, v168, v170\nv_add_f32 v169, v169, v171\nv_add_f32 v172, v172, v174\nv_add_f32 v173, v173, v175\n"
    "v_cvt_pk_bf16_f32 v193, v168, v172\nv_cvt_pk_bf16_f32 v195, v169, v173\nds_write2_b64 v51, v[192:193], v[194:195] offset1:34\n"
    "buffer_store_dword v191, v57, %[rc], %[sM] offen sc1\ns_waitcnt lgkmcnt(0)\ns_barrier\ns_nop 15\nds_read_b128 v[96:99], v196 offset:12288\n"
    "ds_read_b128 v[112:115], v54 offset:0\nds_read_b128 v[128:131], v196 offset:0\nds_read_b128 v[144:147], v196 offset:4096\n"
    "ds_read_b128 v[100:103], v196 offset:13312\nds_read_b128 v[116:119], v54 offset:16\nds_read_b128 v[132:135], v196 offset:1024\n"
    "ds_read_b128 v[104:107], v196 offset:14336\nds_read_b128 v[120:123], v54 offset:128\nds_read_b128 v[136:139], v196 offset:2048\n"
    "ds_read_b128 v[108:111], v196 offset:15360\nds_read_b128 v[124:127], v54 offset:144\nds_read_b128 v[140:143], v196 offset:3072\n"
    "ds_read_b128 v[148:151], v196 offset:5120\nds_read_b128 v[152:155], v196 offset:6144\nds_read_b128 v[156:159], v196 offset:7168\n"
    "s_mov_b32 s86, 0xffff0000\ns_mov_b32 s88, 0x3db504f3\ns_mov_b32 s89, 0x3fb8aa3b\nv_mov_b32 v194, 0xce6e6b28\ns_waitcnt lgkmcnt(3)\n"
    "v_mfma_f32_16x16x32_bf16 v[160:163], v[112:115], v[96:99], 0\nv_mfma_f32_16x16x32_bf16 v[160:163], v[116:119], v[100:103], v[160:163]\n"
    "v_mfma_f32_16x16x32_bf16 v[160:163], v[120:123], v[104:107], v[160:163]\nv_mfma_f32_16x16x32_bf16 v[160:163], v[124:127], v[108:111], v[160:163]\n"
    "v_mfma_f32_16x16x32_bf16 v[164:167], v[128:131], v[96:99], 0\nv_mfma_f32_16x16x32_bf16 v[164:167], v[132:135], v[100:103], v[164:167]\n"
    "v_mfma_f32_16x16x32_bf16 v[164:167], v[136:139], v[104:107], v[164:167]\nv_mfma_f32_16x16x32_bf16 v[164:167], v[140:143], v[108:111], v[164:167]\n"
    "s_waitcnt lgkmcnt(0)\nv_mfma_f32_16x16x32_bf16 v[168:171], v[144:147], v[96:99], 0\n"
    "v_mfma_f32_16x16x32_bf16 v[168:171], v[148:151], v[100:103], v[168:171]\nv_mfma_f32_16x16x32_bf16 v[168:171], v[152:155], v[104:107], v[168:171]\n"
    "v_mfma_f32_16x16x32_bf16 v[168:171], v[156:159], v[108:111], v[168:171]\nv_cvt_pk_bf16_f32 v176, v160, v161\nv_cvt_pk_bf16_f32 v177, v162, v163\n"
    "v_lshlrev_b32 v160, 16, v176\nv_and_b32 v161, s86, v176\nv_lshlrev_b32 v162, 16, v177\nv_and_b32 v163, s86, v177\nv_mul_f32 v160, s88, v160\n"
    "v_mul_f32 v161, s88, v161\nv_mul_f32 v162, s88, v162\nv_mul_f32 v163, s88, v163\nv_cvt_pk_bf16_f32 v176, v160, v161\n"
    "v_cvt_pk_bf16_f32 v177, v162, v163\nv_lshlrev_b32 v160, 16, v176\nv_and_b32 v161, s86, v176\nv_lshlrev_b32 v162, 16, v177\nv_and_b32 v163, s86, v177\n"
    "v_cvt_pk_bf16_f32 v176, v164, v165\nv_cvt_pk_bf16_f32 v177, v166, v167\nv_lshlrev_b32 v164, 16, v176\nv_and_b32 v165, s86, v176\n"
    "v_lshlrev_b32 v166, 16, v177\nv_and_b32 v167, s86, v177\nv_mul_f32 v164, s88, v164\nv_mul_f32 v165, s88, v165\nv_mul_f32 v166, s88, v166\n"
    "v_mul_f32 v167, s88, v167\nv_cvt_pk_bf16_f32 v176, v164, v165\nv_cvt_pk_bf16_f32 v177, v166, v167\nv_lshlrev_b32 v164, 16, v176\n"
    "v_and_b32 v165, s86, v176\nv_lshlrev_b32 v166, 16, v177\nv_and_b32 v167, s86, v177\nv_cvt_pk_bf16_f32 v176, v168, v169\n"
    "v_cvt_pk_bf16_f32 v177, v170, v171\nv_lshlrev_b32 v168, 16, v176\nv_and_b32 v169, s86, v176\nv_lshlrev_b32 v170, 16, v177\nv_and_b32 v171, s86, v177\n"
    "v_mul_f32 v168, s88, v168\nv_mul_f32 v169, s88, v169\nv_mul_f32 v170, s88, v170\nv_mul_f32 v171, s88, v171\nv_cvt_pk_bf16_f32 v176, v168, v169\n"
    "v_cvt_pk_bf16_f32 v177, v170, v171\nv_lshlrev_b32 v168, 16, v176\nv_and_b32 v169, s86, v176\nv_lshlrev_b32 v170, 16, v177\nv_and_b32 v171, s86, v177\n"
    "s_mov_b32 s95, s91\nv_cmp_le_i32 vcc, 0, v62\nv_cmp_le_i32 s[84:85], 1, v62\nv_cmp_le_i32 s[90:91], 2, v62\nv_cmp_le_i32 s[92:93], 3, v62\n"
    "v_max3_f32 v192, v160, v161, v162\nv_cndmask_b32 v168, v194, v168, vcc\nv_cndmask_b32 v169, v194, v169, s[84:85]\n"
    "v_cndmask_b32 v170, v194, v170, s[90:91]\nv_cndmask_b32 v171, v194, v171, s[92:93]\ns_mov_b32 s91, s95\nv_max3_f32 v192, v192, v163, v164\n"
    "v_max3_f32 v192, v192, v165, v166\nv_max3_f32 v192, v192, v167, v168\nv_max3_f32 v192, v192, v169, v170\nv_max_f32 v192, v192, v171\n"
    "v_mov_b32 v193, v192\ns_nop 1\nv_permlane16_swap_b32 v192, v193\nv_max_f32 v192, v192, v193\nv_mov_b32 v193, v192\ns_nop 1\n"
    "v_permlane32_swap_b32 v192, v193\nv_max_f32 v192, v192, v193\nv_sub_f32 v160, v160, v192\nv_sub_f32 v161, v161, v192\nv_sub_f32 v162, v162, v192\n"
    "v_sub_f32 v163, v163, v192\nv_sub_f32 v164, v164, v192\nv_sub_f32 v165, v165, v192\nv_sub_f32 v166, v166, v192\nv_sub_f32 v167, v167, v192\n"
    "v_sub_f32 v168, v168, v192\nv_sub_f32 v169, v169, v192\nv_sub_f32 v170, v170, v192\nv_sub_f32 v171, v171, v192\nv_mul_f32 v160, s89, v160\n"
    "v_mul_f32 v161, s89, v161\nv_mul_f32 v162, s89, v162\nv_mul_f32 v163, s89, v163\nv_mul_f32 v164, s89, v164\nv_mul_f32 v165, s89, v165\n"
    "v_mul_f32 v166, s89, v166\nv_mul_f32 v167, s89, v167\nv_mul_f32 v168, s89, v168\nv_mul_f32 v169, s89, v169\nv_mul_f32 v170, s89, v170\n"
    "v_mul_f32 v171, s89, v171\nv_exp_f32 v160, v160\nv_exp_f32 v161, v161\nv_exp_f32 v162, v162\nv_exp_f32 v163, v163\nv_exp_f32 v164, v164\n"
    "v_exp_f32 v165, v165\nv_exp_f32 v166, v166\nv_exp_f32 v167, v167\nv_exp_f32 v168, v168\nv_exp_f32 v169, v169\nv_exp_f32 v170, v170\n"
    "v_exp_f32 v171, v171\nv_add_f32 v192, v160, v161\nv_add_f32 v193, v162, v163\nv_add_f32 v192, v192, v193\nv_fma_f32 v192, v192, 0.5, v164\n"
    "v_add_f32 v192, v192, v165\nv_add_f32 v192, v192, v166\nv_add_f32 v192, v192, v167\nv_add_f32 v192, v192, v168\nv_add_f32 v192, v192, v169\n"
    "v_add_f32 v192, v192, v170\nv_add_f32 v192, v192, v171\nv_mov_b32 v193, v192\ns_nop 1\nv_permlane16_swap_b32 v192, v193\nv_add_f32 v192, v192, v193\n"
    "v_mov_b32 v193, v192\ns_nop 1\nv_permlane32_swap_b32 v192, v193\nv_add_f32 v192, v192, v193\nv_rcp_f32 v192, v192\ns_nop 0\n"
    "v_pk_mul_f32 v[160:161], v[160:161], v[192:193] op_sel_hi:[1,0]\nv_pk_mul_f32 v[162:163], v[162:163], v[192:193] op_sel_hi:[1,0]\n"
    "v_pk_mul_f32 v[164:165], v[164:165], v[192:193] op_sel_hi:[1,0]\nv_pk_mul_f32 v[166:167], v[166:167], v[192:193] op_sel_hi:[1,0]\n"
    "v_pk_mul_f32 v[168:169], v[168:169], v[192:193] op_sel_hi:[1,0]\nv_pk_mul_f32 v[170:171], v[170:171], v[192:193] op_sel_hi:[1,0]\n"
    "v_cvt_pk_bf16_f32 v68, v160, v161\nv_cvt_pk_bf16_f32 v69, v162, v163\nv_mov_b32 v70, v68\nv_mov_b32 v71, v69\nv_cvt_pk_bf16_f32 v64, v164, v168\n"
    "v_cvt_pk_bf16_f32 v65, v165, v169\nv_cvt_pk_bf16_f32 v66, v166, v170\nv_cvt_pk_bf16_f32 v67, v167, v171\nv_permlane16_swap_b32 v68, v70\n"
    "v_permlane16_swap_b32 v69, v71\ns_cmp_eq_u32 s91, 0\ns_cbranch_scc1 81f\ns_waitcnt vmcnt(1)\n81:\ns_waitcnt vmcnt(4)\ns_branch 9f\n7:\n"
    "s_cmp_eq_u32 s91, 0\ns_cbranch_scc0 6b\ns_mov_b32 s91, 1\nbuffer_load_dwordx4 v[216:219], v48, %[rc], s87 offen sc1\n"
    "buffer_load_dwordx4 v[220:223], v58, %[rc], s87 offen sc1\ns_waitcnt lgkmcnt(0)\ns_barrier\ns_nop 15\ns_nop 15\n"
    "buffer_load_dwordx4 v[224:227], v48, %[rc], s88 offen sc1\nbuffer_load_dwordx4 v[228:231], v58, %[rc], s88 offen sc1\ns_nop 15\ns_nop 15\n"
    "buffer_load_dwordx4 v[208:211], v48, %[rc], s89 offen sc1\nbuffer_load_dwordx4 v[212:215], v58, %[rc], s89 offen sc1\nv_mov_b64 v[160:161], 0\n"
    "v_mov_b64 v[162:163], 0\nv_mov_b64 v[164:165], 0\nv_mov_b64 v[166:167], 0\nv_mov_b64 v[168:169], 0\nv_mov_b64 v[170:171], 0\nv_mov_b64 v[172:173], 0\n"
    "v_mov_b64 v[174:175], 0\nv_mov_b32 v191, 2\ns_add_u32 %[nret], %[nret], 1\ns_waitcnt vmcnt(4)\nds_write_b128 v61, v[216:219]\n"
    "ds_write_b128 v61, v[220:223] offset:4096\nv_max3_u32 v188, v216, v217, v218\nv_max3_u32 v189, v220, v221, v222\nv_max3_u32 v188, v188, v219, v223\n"
    "v_max_u32 v188, v188, v189\nv_cmp_eq_u32 vcc, -1, v188\ns_waitcnt lgkmcnt(0)\ns_cbranch_vccz 42f\n"
    "buffer_load_dwordx4 v[216:219], v48, %[rc], s94 offen sc1\nbuffer_load_dwordx4 v[220:223], v58, %[rc], s94 offen sc1\n"
    "s_add_u32 %[nret], %[nret], 1\ns_waitcnt vmcnt(4)\nds_write_b128 v61, v[224:227]\nds_write_b128 v61, v[228:231] offset:4096\n"
    "v_max3_u32 v188, v224, v225, v226\nv_max3_u32 v189, v228, v229, v230\nv_max3_u32 v188, v188, v227, v231\nv_max_u32 v188, v188, v189\n"
    "v_cmp_eq_u32 vcc, -1, v188\ns_waitcnt lgkmcnt(0)\ns_cbranch_vccz 42f\nbuffer_load_dwordx4 v[224:227], v48, %[rc], s87 offen sc1\n"
    "buffer_load_dwordx4 v[228:231], v58, %[rc], s87 offen sc1\n41:\ns_add_u32 %[nret], %[nret], 1\ns_waitcnt vmcnt(4)\nds_write_b128 v61, v[208:211]\n"
    "ds_write_b128 v61, v[212:215] offset:4096\nv_max3_u32 v188, v208, v209, v210\nv_max3_u32 v189, v212, v213, v214\nv_max3_u32 v188, v188, v211, v215\n"
    "v_max_u32 v188, v188, v189\nv_cmp_eq_u32 vcc, -1, v188\ns_waitcnt lgkmcnt(0)\ns_cbranch_vccz 42f\n"
    "buffer_load_dwordx4 v[208:211], v48, %[rc], s88 offen sc1\nbuffer_load_dwordx4 v[212:215], v58, %[rc], s88 offen sc1\n"
    "s_add_u32 %[nret], %[nret], 1\ns_waitcnt vmcnt(4)\nds_write_b128 v61, v[216:219]\nds_write_b128 v61, v[220:223] offset:4096\n"
    "v_max3_u32 v188, v216, v217, v218\nv_max3_u32 v189, v220, v221, v222\nv_max3_u32 v188, v188, v219, v223\nv_max_u32 v188, v188, v189\n"
    "v_cmp_eq_u32 vcc, -1, v188\ns_waitcnt lgkmcnt(0)\ns_cbranch_vccz 42f\nbuffer_load_dwordx4 v[216:219], v48, %[rc], s89 offen sc1\n"
    "buffer_load_dwordx4 v[220:223], v58, %[rc], s89 offen sc1\ns_add_u32 %[nret], %[nret], 1\ns_waitcnt vmcnt(4)\nds_write_b128 v61, v[224:227]\n"
    "ds_write_b128 v61, v[228:231] offset:4096\nv_max3_u32 v188, v224, v225, v226\nv_max3_u32 v189, v228, v229, v230\nv_max3_u32 v188, v188, v227, v231\n"
    "v_max_u32 v188, v188, v189\nv_cmp_eq_u32 vcc, -1, v188\ns_waitcnt lgkmcnt(0)\ns_cbranch_vccz 42f\n"
    "buffer_load_dwordx4 v[224:227], v48, %[rc], s94 offen sc1\nbuffer_load_dwordx4 v[228:231], v58, %[rc], s94 offen sc1\n"
    "s_add_u32 %[nret], %[nret], 1\ns_waitcnt vmcnt(4)\nds_write_b128 v61, v[208:211]\nds_write_b128 v61, v[212:215] offset:4096\n"
    "v_max3_u32 v188, v208, v209, v210\nv_max3_u32 v189, v212, v213, v214\nv_max3_u32 v188, v188, v211, v215\nv_max_u32 v188, v188, v189\n"
    "v_cmp_eq_u32 vcc, -1, v188\ns_waitcnt lgkmcnt(0)\ns_cbranch_vccz 42f\nbuffer_load_dwordx4 v[208:211], v48, %[rc], s87 offen sc1\n"
    "buffer_load_dwordx4 v[212:215], v58, %[rc], s87 offen sc1\ns_add_u32 %[nret], %[nret], 1\ns_waitcnt vmcnt(4)\nds_write_b128 v61, v[216:219]\n"
    "ds_write_b128 v61, v[220:223] offset:4096\nv_max3_u32 v188, v216, v217, v218\nv_max3_u32 v189, v220, v221, v222\nv_max3_u32 v188, v188, v219, v223\n"
    "v_max_u32 v188, v188, v189\nv_cmp_eq_u32 vcc, -1, v188\ns_waitcnt lgkmcnt(0)\ns_cbranch_vccz 42f\n"
    "buffer_load_dwordx4 v[216:219], v48, %[rc], s88 offen sc1\nbuffer_load_dwordx4 v[220:223], v58, %[rc], s88 offen sc1\n"
    "s_add_u32 %[nret], %[nret], 1\ns_waitcnt vmcnt(4)\nds_write_b128 v61, v[224:227]\nds_write_b128 v61, v[228:231] offset:4096\n"
    "v_max3_u32 v188, v224, v225, v226\nv_max3_u32 v189, v228, v229, v230\nv_max3_u32 v188, v188, v227, v231\nv_max_u32 v188, v188, v189\n"
    "v_cmp_eq_u32 vcc, -1, v188\ns_waitcnt lgkmcnt(0)\ns_cbranch_vccz 42f\nbuffer_load_dwordx4 v[224:227], v48, %[rc], s89 offen sc1\n"
    "buffer_load_dwordx4 v[228:231], v58, %[rc], s89 offen sc1\ns_add_u32 %[nret], %[nret], 1\ns_waitcnt vmcnt(4)\nds_write_b128 v61, v[208:211]\n"
    "ds_write_b128 v61, v[212:215] offset:4096\nv_max3_u32 v188, v208, v209, v210\nv_max3_u32 v189, v212, v213, v214\nv_max3_u32 v188, v188, v211, v215\n"
    "v_max_u32 v188, v188, v189\nv_cmp_eq_u32 vcc, -1, v188\ns_waitcnt lgkmcnt(0)\ns_cbranch_vccz 42f\n"
    "buffer_load_dwordx4 v[208:211], v48, %[rc], s94 offen sc1\nbuffer_load_dwordx4 v[212:215], v58, %[rc], s94 offen sc1\n"
    "s_add_u32 %[nret], %[nret], 1\ns_waitcnt vmcnt(4)\nds_write_b128 v61, v[216:219]\nds_write_b128 v61, v[220:223] offset:4096\n"
    "v_max3_u32 v188, v216, v217, v218\nv_max3_u32 v189, v220, v221, v222\nv_max3_u32 v188, v188, v219, v223\nv_max_u32 v188, v188, v189\n"
    "v_cmp_eq_u32 vcc, -1, v188\ns_waitcnt lgkmcnt(0)\ns_cbranch_vccz 42f\nbuffer_load_dwordx4 v[216:219], v48, %[rc], s87 offen sc1\n"
    "buffer_load_dwordx4 v[220:223], v58, %[rc], s87 offen sc1\ns_add_u32 %[nret], %[nret], 1\ns_waitcnt vmcnt(4)\nds_write_b128 v61, v[224:227]\n"
    "ds_write_b128 v61, v[228:231] offset:4096\nv_max3_u32 v188, v224, v225, v226\nv_max3_u32 v189, v228, v229, v230\nv_max3_u32 v188, v188, v227, v231\n"
    "v_max_u32 v188, v188, v189\nv_cmp_eq_u32 vcc, -1, v188\ns_waitcnt lgkmcnt(0)\ns_cbranch_vccz 42f\n"
    "buffer_load_dwordx4 v[224:227], v48, %[rc], s88 offen sc1\nbuffer_load_dwordx4 v[228:231], v58, %[rc], s88 offen sc1\n"
    "s_add_u32 %[nret], %[nret], 1\ns_waitcnt vmcnt(4)\nds_write_b128 v61, v[208:211]\nds_write_b128 v61, v[212:215] offset:4096\n"
    "v_max3_u32 v188, v208, v209, v210\nv_max3_u32 v189, v212, v213, v214\nv_max3_u32 v188, v188, v211, v215\nv_max_u32 v188, v188, v189\n"
    "v_cmp_eq_u32 vcc, -1, v188\ns_waitcnt lgkmcnt(0)\ns_cbranch_vccz 42f\nbuffer_load_dwordx4 v[208:211], v48, %[rc], s89 offen sc1\n"
    "buffer_load_dwordx4 v[212:215], v58, %[rc], s89 offen sc1\ns_add_u32 %[nret], %[nret], 1\ns_waitcnt vmcnt(4)\nds_write_b128 v61, v[216:219]\n"
    "ds_write_b128 v61, v[220:223] offset:4096\nv_max3_u32 v188, v216, v217, v218\nv_max3_u32 v189, v220, v221, v222\nv_max3_u32 v188, v188, v219, v223\n"
    "v_max_u32 v188, v188, v189\nv_cmp_eq_u32 vcc, -1, v188\ns_waitcnt lgkmcnt(0)\ns_cbranch_vccz 42f\n"
    "buffer_load_dwordx4 v[216:219], v48, %[rc], s94 offen sc1\nbuffer_load_dwordx4 v[220:223], v58, %[rc], s94 offen sc1\n"
    "s_add_u32 %[nret], %[nret], 1\ns_waitcnt vmcnt(4)\nds_write_b128 v61, v[224:227]\nds_write_b128 v61, v[228:231] offset:4096\n"
    "v_max3_u32 v188, v224, v225, v226\nv_max3_u32 v189, v228, v229, v230\nv_max3_u32 v188, v188, v227, v231\nv_max_u32 v188, v188, v189\n"
    "v_cmp_eq_u32 vcc, -1, v188\ns_waitcnt lgkmcnt(0)\ns_cbranch_vccz 42f\nbuffer_load_dwordx4 v[224:227], v48, %[rc], s87 offen sc1\n"
    "buffer_load_dwordx4 v[228:231], v58, %[rc], s87 offen sc1\ns_sub_u32 %[bud], %[bud], 4\ns_cmp_gt_i32 %[bud], 0\ns_cbranch_scc1 41b\n42:\ns_barrier\n"
    "ds_read_b128 v[128:131], v49 offset:0\nds_read_b128 v[132:135], v49 offset:1024\nds_read_b128 v[136:139], v49 offset:2048\n"
    "ds_read_b128 v[140:143], v49 offset:3072\nds_read_b128 v[144:147], v49 offset:4096\nds_read_b128 v[148:151], v49 offset:5120\n"
    "ds_read_b128 v[152:155], v49 offset:6144\nds_read_b128 v[156:159], v49 offset:7168\ns_branch 5b\n9:\n"
    : [bud] "+s"(bud), [nret] "+s"(nret), "+{v[64:71]}"(pt)
    : [rc] "s"(rc), [sM] "s"(sM), [wM] "s"(wM), [lkq] "s"(lkq), [lm] "s"(lm), [lnx] "s"(lnx), [sK] "s"(sK), [lpg] "s"(lpg), [sX] "s"(sX), "{v[48:63]}"(cv)
    : "memory", "vcc", "scc", CLS, CLV_96_239);
}
DI void c_step(rsrc_t rc, unsigned sM, unsigned sMn, unsigned sY, unsigned sB, int wP, int wS, int wC, const v16u& cv, unsigned vpg, unsigned& prog, int& bud, unsigned& nret) {
  asm volatile(
    "s_nop 4\ns_mov_b32 s84, %[sB]\nbuffer_load_dwordx4 a[0:3], v96, %[rc], s84 offen offset:0 sc1\n"
    "buffer_load_dwordx4 a[4:7], v96, %[rc], s84 offen offset:2048 sc1\nbuffer_load_dwordx4 a[8:11], v96, %[rc], s84 offen offset:512 sc1\n"
    "buffer_load_dwordx4 a[12:15], v96, %[rc], s84 offen offset:2560 sc1\nbuffer_load_dwordx4 a[16:19], v97, %[rc], s84 offen offset:0 sc1\n"
    "buffer_load_dwordx4 a[20:23], v97, %[rc], s84 offen offset:2048 sc1\nbuffer_load_dwordx4 a[24:27], v97, %[rc], s84 offen offset:512 sc1\n"
    "buffer_load_dwordx4 a[28:31], v97, %[rc], s84 offen offset:2560 sc1\ns_add_u32 s84, s84, 0x4000\n"
    "buffer_load_dwordx4 a[32:35], v96, %[rc], s84 offen offset:0 sc1\nbuffer_load_dwordx4 a[36:39], v96, %[rc], s84 offen offset:2048 sc1\n"
    "buffer_load_dwordx4 a[40:43], v96, %[rc], s84 offen offset:512 sc1\nbuffer_load_dwordx4 a[44:47], v96, %[rc], s84 offen offset:2560 sc1\n"
    "buffer_load_dwordx4 a[48:51], v97, %[rc], s84 offen offset:0 sc1\nbuffer_load_dwordx4 a[52:55], v97, %[rc], s84 offen offset:2048 sc1\n"
    "buffer_load_dwordx4 a[56:59], v97, %[rc], s84 offen offset:512 sc1\nbuffer_load_dwordx4 a[60:63], v97, %[rc], s84 offen offset:2560 sc1\n"
    "s_add_u32 s84, s84, 0x4000\nbuffer_load_dwordx4 a[64:67], v96, %[rc], s84 offen offset:0 sc1\n"
    "buffer_load_dwordx4 a[68:71], v96, %[rc], s84 offen offset:2048 sc1\nbuffer_load_dwordx4 a[72:75], v96, %[rc], s84 offen offset:512 sc1\n"
    "buffer_load_dwordx4 a[76:79], v96, %[rc], s84 offen offset:2560 sc1\nbuffer_load_dwordx4 a[80:83], v97, %[rc], s84 offen offset:0 sc1\n"
    "buffer_load_dwordx4 a[84:87], v97, %[rc], s84 offen offset:2048 sc1\nbuffer_load_dwordx4 a[88:91], v97, %[rc], s84 offen offset:512 sc1\n"
    "buffer_load_dwordx4 a[92:95], v97, %[rc], s84 offen offset:2560 sc1\ns_add_u32 s84, s84, 0x4000\n"
    "buffer_load_dwordx4 a[96:99], v96, %[rc], s84 offen offset:0 sc1\nbuffer_load_dwordx4 a[100:103], v96, %[rc], s84 offen offset:2048 sc1\n"
    "buffer_load_dwordx4 a[104:107], v96, %[rc], s84 offen offset:512 sc1\nbuffer_load_dwordx4 a[108:111], v96, %[rc], s84 offen offset:2560 sc1\n"
    "buffer_load_dwordx4 a[112:115], v97, %[rc], s84 offen offset:0 sc1\nbuffer_load_dwordx4 a[116:119], v97, %[rc], s84 offen offset:2048 sc1\n"
    "buffer_load_dwordx4 a[120:123], v97, %[rc], s84 offen offset:512 sc1\nbuffer_load_dwordx4 a[124:127], v97, %[rc], s84 offen offset:2560 sc1\n"
    "v_mov_b64 v[200:201], 0\nv_mov_b64 v[202:203], 0\nv_mov_b64 v[204:205], 0\nv_mov_b64 v[206:207], 0\ns_mov_b32 s94, 0xaaaaaaaa\n"
    "s_mov_b32 s95, 0xaaaaaaaa\ns_mov_b32 s86, 0\ns_add_u32 s90, %[sM], 0x1000\ns_add_u32 s85, %[sY], 4096\ns_add_u32 s91, %[sMn], 98304\n"
    "s_mov_b32 s84, %[wC]\ns_branch 8f\n.p2align 6\n8:\ns_sub_u32 s84, s84, 1\ns_cbranch_scc0 8b\ns_memrealtime s[92:93]\ns_waitcnt lgkmcnt(0)\n"
    "s_lshl_b32 s92, s92, 6\ns_and_b32 s92, s92, 0xf00\ns_add_u32 s92, s92, %[sM]\nbuffer_load_dword v112, v106, %[rc], s92 offen sc1\ns_nop 2\n"
    "s_memrealtime s[92:93]\ns_waitcnt lgkmcnt(0)\ns_lshl_b32 s92, s92, 6\ns_and_b32 s92, s92, 0xf00\ns_add_u32 s92, s92, %[sM]\n"
    "buffer_load_dword v113, v106, %[rc], s92 offen sc1\ns_nop 2\ns_memrealtime s[92:93]\ns_waitcnt lgkmcnt(0)\ns_lshl_b32 s92, s92, 6\n"
    "s_and_b32 s92, s92, 0xf00\ns_add_u32 s92, s92, %[sM]\nbuffer_load_dword v114, v106, %[rc], s92 offen sc1\ns_nop 2\ns_memrealtime s[92:93]\n"
    "s_waitcnt lgkmcnt(0)\ns_lshl_b32 s92, s92, 6\ns_and_b32 s92, s92, 0xf00\ns_add_u32 s92, s92, %[sM]\n"
    "buffer_load_dword v115, v106, %[rc], s92 offen sc1\ns_nop 2\ns_memrealtime s[92:93]\ns_waitcnt lgkmcnt(0)\ns_lshl_b32 s92, s92, 6\n"
    "s_and_b32 s92, s92, 0xf00\ns_add_u32 s92, s92, %[sM]\nbuffer_load_dword v116, v106, %[rc], s92 offen sc1\ns_nop 2\ns_memrealtime s[92:93]\n"
    "s_waitcnt lgkmcnt(0)\ns_lshl_b32 s92, s92, 6\ns_and_b32 s92, s92, 0xf00\ns_add_u32 s92, s92, %[sM]\n"
    "buffer_load_dword v117, v106, %[rc], s92 offen sc1\n1:\ns_waitcnt vmcnt(5)\ns_memrealtime s[92:93]\ns_waitcnt vmcnt(5)\nv_cmp_eq_u32 vcc, -1, v112\n"
    "v_cmp_eq_u32 s[88:89], 2, v112\ns_cbranch_vccz 2f\ns_waitcnt lgkmcnt(0)\ns_lshl_b32 s92, s92, 6\ns_and_b32 s92, s92, 0xf00\ns_add_u32 s92, s92, %[sM]\n"
    "buffer_load_dword v112, v106, %[rc], s92 offen sc1\ns_waitcnt vmcnt(5)\ns_memrealtime s[92:93]\ns_waitcnt vmcnt(5)\nv_cmp_eq_u32 vcc, -1, v113\n"
    "v_cmp_eq_u32 s[88:89], 2, v113\ns_cbranch_vccz 2f\ns_waitcnt lgkmcnt(0)\ns_lshl_b32 s92, s92, 6\ns_and_b32 s92, s92, 0xf00\ns_add_u32 s92, s92, %[sM]\n"
    "buffer_load_dword v113, v106, %[rc], s92 offen sc1\ns_waitcnt vmcnt(5)\ns_memrealtime s[92:93]\ns_waitcnt vmcnt(5)\nv_cmp_eq_u32 vcc, -1, v114\n"
    "v_cmp_eq_u32 s[88:89], 2, v114\ns_cbranch_vccz 2f\ns_waitcnt lgkmcnt(0)\ns_lshl_b32 s92, s92, 6\ns_and_b32 s92, s92, 0xf00\ns_add_u32 s92, s92, %[sM]\n"
    "buffer_load_dword v114, v106, %[rc], s92 offen sc1\ns_waitcnt vmcnt(5)\ns_memrealtime s[92:93]\ns_waitcnt vmcnt(5)\nv_cmp_eq_u32 vcc, -1, v115\n"
    "v_cmp_eq_u32 s[88:89], 2, v115\ns_cbranch_vccz 2f\ns_waitcnt lgkmcnt(0)\ns_lshl_b32 s92, s92, 6\ns_and_b32 s92, s92, 0xf00\ns_add_u32 s92, s92, %[sM]\n"
    "buffer_load_dword v115, v106, %[rc], s92 offen sc1\ns_waitcnt vmcnt(5)\ns_memrealtime s[92:93]\ns_waitcnt vmcnt(5)\nv_cmp_eq_u32 vcc, -1, v116\n"
    "v_cmp_eq_u32 s[88:89], 2, v116\ns_cbranch_vccz 2f\ns_waitcnt lgkmcnt(0)\ns_lshl_b32 s92, s92, 6\ns_and_b32 s92, s92, 0xf00\ns_add_u32 s92, s92, %[sM]\n"
    "buffer_load_dword v116, v106, %[rc], s92 offen sc1\ns_waitcnt vmcnt(5)\ns_memrealtime s[92:93]\ns_waitcnt vmcnt(5)\nv_cmp_eq_u32 vcc, -1, v117\n"
    "v_cmp_eq_u32 s[88:89], 2, v117\ns_cbranch_vccz 2f\ns_waitcnt lgkmcnt(0)\ns_lshl_b32 s92, s92, 6\ns_and_b32 s92, s92, 0xf00\ns_add_u32 s92, s92, %[sM]\n"
    "buffer_load_dword v117, v106, %[rc], s92 offen sc1\ns_sub_u32 %[bud], %[bud], 1\ns_cmp_gt_i32 %[bud], 0\ns_cbranch_scc1 1b\n2:\n"
    "s_cmp_eq_u64 s[88:89], 0\ns_cbranch_scc1 12f\ns_mov_b32 s84, %[wS]\ns_branch 11f\n.p2align 6\n11:\ns_sub_u32 s84, s84, 1\ns_cbranch_scc0 11b\n12:\n"
    "s_mov_b32 s84, %[wP]\ns_branch 3f\n.p2align 6\n3:\ns_sub_u32 s84, s84, 1\ns_cbranch_scc0 3b\n4:\n"
    "buffer_load_dwordx4 v[168:171], v98, %[rc], %[sM] offen offset:0 sc1\nbuffer_load_dwordx4 v[172:175], v99, %[rc], %[sM] offen offset:0 sc1\n"
    "buffer_load_dwordx4 v[176:179], v98, %[rc], %[sM] offen offset:2048 sc1\nbuffer_load_dwordx4 v[180:183], v99, %[rc], %[sM] offen offset:2048 sc1\n"
    "buffer_load_dwordx4 v[184:187], v98, %[rc], s90 offen offset:0 sc1\nbuffer_load_dwordx4 v[188:191], v99, %[rc], s90 offen offset:0 sc1\n"
    "buffer_load_dwordx4 v[192:195], v98, %[rc], s90 offen offset:2048 sc1\nbuffer_load_dwordx4 v[196:199], v99, %[rc], s90 offen offset:2048 sc1\n"
    "buffer_load_dwordx2 v[160:161], v102, %[rc], %[sM] offen offset:0 sc1\nbuffer_load_dwordx2 v[162:163], v102, %[rc], %[sM] offen offset:128 sc1\n"
    "buffer_load_dwordx2 v[164:165], v102, %[rc], %[sM] offen offset:2048 sc1\nbuffer_load_dwordx2 v[166:167], v102, %[rc], %[sM] offen offset:2176 sc1\n"
    "buffer_load_dwordx4 v[128:131], v100, %[rc], %[sY] offen offset:0 sc1\nbuffer_load_dwordx4 v[132:135], v100, %[rc], %[sY] offen offset:1024 sc1\n"
    "buffer_load_dwordx4 v[136:139], v100, %[rc], %[sY] offen offset:2048 sc1\nbuffer_load_dwordx4 v[140:143], v100, %[rc], %[sY] offen offset:3072 sc1\n"
    "buffer_load_dwordx4 v[144:147], v100, %[rc], s85 offen offset:0 sc1\nbuffer_load_dwordx4 v[148:151], v100, %[rc], s85 offen offset:1024 sc1\n"
    "buffer_load_dwordx4 v[152:155], v100, %[rc], s85 offen offset:2048 sc1\nbuffer_load_dwordx4 v[156:159], v100, %[rc], s85 offen offset:3072 sc1\n"
    "v_accvgpr_read_b32 v120, a128\nv_accvgpr_read_b32 v121, a129\nv_accvgpr_read_b32 v122, a130\nv_accvgpr_read_b32 v123, a131\n"
    "v_min_u32 %[prog], v120, v121\nv_min3_u32 %[prog], %[prog], v122, v123\nbuffer_store_dword v105, v101, %[rc], %[sM] offen sc1\n"
    "buffer_load_dwordx4 a[128:131], %[vpg], %[rc], 0 offen sc1\ns_waitcnt vmcnt(21)\nv_smfmac_f32_16x16x64_bf16 v[200:203], v[168:171], a[0:7], v105\n"
    "v_smfmac_f32_16x16x64_bf16 v[204:207], v[168:171], a[8:15], v105\ns_waitcnt vmcnt(20)\n"
    "v_smfmac_f32_16x16x64_bf16 v[200:203], v[172:175], a[16:23], v105\nv_smfmac_f32_16x16x64_bf16 v[204:207], v[172:175], a[24:31], v105\n"
    "s_waitcnt vmcnt(19)\nv_smfmac_f32_16x16x64_bf16 v[200:203], v[176:179], a[32:39], v105\n"
    "v_smfmac_f32_16x16x64_bf16 v[204:207], v[176:179], a[40:47], v105\ns_waitcnt vmcnt(18)\n"
    "v_smfmac_f32_16x16x64_bf16 v[200:203], v[180:183], a[48:55], v105\nv_smfmac_f32_16x16x64_bf16 v[204:207], v[180:183], a[56:63], v105\n"
    "s_waitcnt vmcnt(17)\nv_smfmac_f32_16x16x64_bf16 v[200:203], v[184:187], a[64:71], v105\n"
    "v_smfmac_f32_16x16x64_bf16 v[204:207], v[184:187], a[72:79], v105\ns_waitcnt vmcnt(16)\n"
    "v_smfmac_f32_16x16x64_bf16 v[200:203], v[188:191], a[80:87], v105\nv_smfmac_f32_16x16x64_bf16 v[204:207], v[188:191], a[88:95], v105\n"
    "s_waitcnt vmcnt(15)\nv_smfmac_f32_16x16x64_bf16 v[200:203], v[192:195], a[96:103], v105\n"
    "v_smfmac_f32_16x16x64_bf16 v[204:207], v[192:195], a[104:111], v105\ns_waitcnt vmcnt(14)\n"
    "v_smfmac_f32_16x16x64_bf16 v[200:203], v[196:199], a[112:119], v105\nv_smfmac_f32_16x16x64_bf16 v[204:207], v[196:199], a[120:127], v105\n"
    "s_waitcnt vmcnt(8)\nv_smfmac_f32_16x16x64_bf16 v[200:203], v[160:163], v[128:135], v105\ns_waitcnt vmcnt(6)\n"
    "v_smfmac_f32_16x16x64_bf16 v[204:207], v[160:163], v[136:143], v105\ns_waitcnt vmcnt(4)\n"
    "v_smfmac_f32_16x16x64_bf16 v[200:203], v[164:167], v[144:151], v105\ns_waitcnt vmcnt(2)\n"
    "v_smfmac_f32_16x16x64_bf16 v[204:207], v[164:167], v[152:159], v105\ns_nop 6\nv_add_f32 v200, v200, v201\nv_add_f32 v204, v206, v207\n"
    "v_add_f32 v200, v200, v204\nv_cmp_u_f32 vcc, v200, v200\ns_nop 0\nv_mov_b32_dpp v208, v200 quad_perm:[0,0,2,2] row_mask:0xf bank_mask:0xf\n"
    "v_mov_b32_dpp v209, v200 quad_perm:[1,1,3,3] row_mask:0xf bank_mask:0xf\nv_cvt_pk_bf16_f32 v210, v208, v209\nv_cvt_pk_bf16_f32 v211, v208, v209\n"
    "s_nop 1\nv_permlane16_swap_b32 v210, v211\nv_cndmask_b32 v210, v210, v211, s[94:95]\ns_cbranch_vccnz 7f\n6:\n"
    "buffer_store_dword v210, v103, %[rc], %[sMn] offen sc1\nbuffer_store_dword v210, v104, %[rc], s91 offen sc1\ns_branch 9f\n7:\n"
    "s_xor_b32 s86, s86, 1\ns_cbranch_scc1 73f\ns_waitcnt vmcnt(0)\nv_max_u32 v208, v128, v129\nv_max3_u32 v208, v208, v130, v131\n"
    "v_max3_u32 v208, v208, v132, v133\nv_max3_u32 v208, v208, v134, v135\nv_max3_u32 v208, v208, v136, v137\nv_max3_u32 v208, v208, v138, v139\n"
    "v_max3_u32 v208, v208, v140, v141\nv_max3_u32 v208, v208, v142, v143\nv_max3_u32 v208, v208, v144, v145\nv_max3_u32 v208, v208, v146, v147\n"
    "v_max3_u32 v208, v208, v148, v149\nv_max3_u32 v208, v208, v150, v151\nv_max3_u32 v208, v208, v152, v153\nv_max3_u32 v208, v208, v154, v155\n"
    "v_max3_u32 v208, v208, v156, v157\nv_max3_u32 v208, v208, v158, v159\nv_max3_u32 v208, v208, v160, v161\nv_max3_u32 v208, v208, v162, v163\n"
    "v_max3_u32 v208, v208, v164, v165\nv_max3_u32 v208, v208, v166, v167\nv_max3_u32 v208, v208, v168, v169\nv_max3_u32 v208, v208, v170, v171\n"
    "v_max3_u32 v208, v208, v172, v173\nv_max3_u32 v208, v208, v174, v175\nv_max3_u32 v208, v208, v176, v177\nv_max3_u32 v208, v208, v178, v179\n"
    "v_max3_u32 v208, v208, v180, v181\nv_max3_u32 v208, v208, v182, v183\nv_max3_u32 v208, v208, v184, v185\nv_max3_u32 v208, v208, v186, v187\n"
    "v_max3_u32 v208, v208, v188, v189\nv_max3_u32 v208, v208, v190, v191\nv_max3_u32 v208, v208, v192, v193\nv_max3_u32 v208, v208, v194, v195\n"
    "v_max3_u32 v208, v208, v196, v197\nv_max3_u32 v208, v208, v198, v199\nv_cmp_eq_u32 vcc, -1, v208\ns_cbranch_vccz 6b\ns_cmp_lt_u32 %[nret], 20000\n"
    "s_cbranch_scc0 6b\ns_add_u32 %[nret], %[nret], 1\n73:\ns_add_u32 %[nret], %[nret], 1\nv_mov_b64 v[200:201], 0\nv_mov_b64 v[202:203], 0\n"
    "v_mov_b64 v[204:205], 0\nv_mov_b64 v[206:207], 0\ns_branch 4b\n9:\ns_waitcnt lgkmcnt(0)\n"
    : [bud] "+s"(bud), [nret] "+s"(nret), [prog] "=&v"(prog)
    : [rc] "s"(rc), [sM] "s"(sM), [sMn] "s"(sMn), [sY] "s"(sY), [sB] "s"(sB), [wP] "s"(wP), [wS] "s"(wS), [wC] "s"(wC), [vpg] "v"(vpg), "{v[96:111]}"(cv)
    : "memory", "vcc", "scc", CLS, CLV_112_215, CLA_0_131);
}
DI void v_step(rsrc_t rc, unsigned sM, unsigned sFM, unsigned sY, unsigned sYp, unsigned sSp, unsigned lm, int wM, int wW, unsigned sv, unsigned sG, const v16u& cv, int& bud, unsigned& nret) {
  asm volatile(
    "s_nop 4\nv_mov_b64 v[160:161], 0\nv_mov_b64 v[162:163], 0\nv_mov_b64 v[164:165], 0\nv_mov_b64 v[166:167], 0\nv_mov_b64 v[168:169], 0\n"
    "v_mov_b64 v[170:171], 0\nv_mov_b64 v[172:173], 0\nv_mov_b64 v[174:175], 0\nv_mov_b64 v[176:177], 0\nv_mov_b64 v[178:179], 0\nv_mov_b64 v[180:181], 0\n"
    "v_mov_b64 v[182:183], 0\nv_mov_b64 v[184:185], 0\nv_mov_b64 v[186:187], 0\nv_mov_b64 v[188:189], 0\nv_mov_b64 v[190:191], 0\nv_mov_b64 v[192:193], 0\n"
    "v_mov_b64 v[194:195], 0\nv_mov_b64 v[196:197], 0\nv_mov_b64 v[198:199], 0\nv_mov_b64 v[224:225], -1\nv_mov_b64 v[226:227], -1\n"
    "s_add_u32 s86, %[sYp], 0x10000\ns_mov_b32 s94, %[sM]\ns_mov_b32 s91, 0\ns_add_u32 s87, %[sM], 65536\ns_add_u32 s88, %[sM], 98304\n"
    "s_add_u32 s89, %[sM], 114688\nv_mov_b32 v206, %[sv]\ns_cmp_eq_u32 %[sFM], -1\ns_cbranch_scc1 2f\ns_mov_b32 s84, %[wW]\ns_branch 8f\n.p2align 6\n8:\n"
    "s_sub_u32 s84, s84, 1\ns_cbranch_scc0 8b\ns_memrealtime s[92:93]\ns_waitcnt lgkmcnt(0)\ns_lshl_b32 s92, s92, 6\ns_and_b32 s92, s92, 0xf00\n"
    "s_add_u32 s92, s92, %[sFM]\nbuffer_load_dword v216, v53, %[rc], s92 offen sc1\ns_nop 2\ns_memrealtime s[92:93]\ns_waitcnt lgkmcnt(0)\n"
    "s_lshl_b32 s92, s92, 6\ns_and_b32 s92, s92, 0xf00\ns_add_u32 s92, s92, %[sFM]\nbuffer_load_dword v217, v53, %[rc], s92 offen sc1\ns_nop 2\n"
    "s_memrealtime s[92:93]\ns_waitcnt lgkmcnt(0)\ns_lshl_b32 s92, s92, 6\ns_and_b32 s92, s92, 0xf00\ns_add_u32 s92, s92, %[sFM]\n"
    "buffer_load_dword v218, v53, %[rc], s92 offen sc1\ns_nop 2\ns_memrealtime s[92:93]\ns_waitcnt lgkmcnt(0)\ns_lshl_b32 s92, s92, 6\n"
    "s_and_b32 s92, s92, 0xf00\ns_add_u32 s92, s92, %[sFM]\nbuffer_load_dword v219, v53, %[rc], s92 offen sc1\ns_nop 2\ns_memrealtime s[92:93]\n"
    "s_waitcnt lgkmcnt(0)\ns_lshl_b32 s92, s92, 6\ns_and_b32 s92, s92, 0xf00\ns_add_u32 s92, s92, %[sFM]\n"
    "buffer_load_dword v220, v53, %[rc], s92 offen sc1\n1:\ns_waitcnt vmcnt(4)\ns_memrealtime s[92:93]\ns_waitcnt vmcnt(4)\nv_cmp_eq_u32 vcc, -1, v216\n"
    "s_cbranch_vccz 2f\ns_waitcnt lgkmcnt(0)\ns_lshl_b32 s92, s92, 6\ns_and_b32 s92, s92, 0xf00\ns_add_u32 s92, s92, %[sFM]\n"
    "buffer_load_dword v216, v53, %[rc], s92 offen sc1\ns_waitcnt vmcnt(4)\ns_memrealtime s[92:93]\ns_waitcnt vmcnt(4)\nv_cmp_eq_u32 vcc, -1, v217\n"
    "s_cbranch_vccz 2f\ns_waitcnt lgkmcnt(0)\ns_lshl_b32 s92, s92, 6\ns_and_b32 s92, s92, 0xf00\ns_add_u32 s92, s92, %[sFM]\n"
    "buffer_load_dword v217, v53, %[rc], s92 offen sc1\ns_waitcnt vmcnt(4)\ns_memrealtime s[92:93]\ns_waitcnt vmcnt(4)\nv_cmp_eq_u32 vcc, -1, v218\n"
    "s_cbranch_vccz 2f\ns_waitcnt lgkmcnt(0)\ns_lshl_b32 s92, s92, 6\ns_and_b32 s92, s92, 0xf00\ns_add_u32 s92, s92, %[sFM]\n"
    "buffer_load_dword v218, v53, %[rc], s92 offen sc1\ns_waitcnt vmcnt(4)\ns_memrealtime s[92:93]\ns_waitcnt vmcnt(4)\nv_cmp_eq_u32 vcc, -1, v219\n"
    "s_cbranch_vccz 2f\ns_waitcnt lgkmcnt(0)\ns_lshl_b32 s92, s92, 6\ns_and_b32 s92, s92, 0xf00\ns_add_u32 s92, s92, %[sFM]\n"
    "buffer_load_dword v219, v53, %[rc], s92 offen sc1\ns_waitcnt vmcnt(4)\ns_memrealtime s[92:93]\ns_waitcnt vmcnt(4)\nv_cmp_eq_u32 vcc, -1, v220\n"
    "s_cbranch_vccz 2f\ns_waitcnt lgkmcnt(0)\ns_lshl_b32 s92, s92, 6\ns_and_b32 s92, s92, 0xf00\ns_add_u32 s92, s92, %[sFM]\n"
    "buffer_load_dword v220, v53, %[rc], s92 offen sc1\ns_sub_u32 %[bud], %[bud], 1\ns_cmp_gt_i32 %[bud], 0\ns_cbranch_scc1 1b\n2:\ns_barrier\n"
    "s_waitcnt lgkmcnt(0)\ns_mov_b32 s84, %[wM]\ns_branch 3f\n.p2align 6\n3:\ns_sub_u32 s84, s84, 1\ns_cbranch_scc0 3b\n4:\ns_mov_b32 m0, %[lm]\ns_nop 0\n"
    "buffer_load_dwordx4 v48, %[rc], s94 offen sc1 lds\ns_add_u32 m0, %[lm], 4096\ns_nop 0\nbuffer_load_dwordx4 v55, %[rc], s94 offen sc1 lds\n"
    "s_nop 9\nbuffer_store_dwordx4 v[224:227], v54, %[rc], %[sYp] offen sc1\nbuffer_store_dwordx4 v[224:227], v54, %[rc], s86 offen sc1\n"
    "buffer_store_dwordx4 v[224:227], v58, %[rc], %[sSp] offen sc1\ns_waitcnt vmcnt(4)\ns_barrier\nds_read_b128 v[128:131], v49 offset:0\n"
    "ds_read_b128 v[132:135], v49 offset:1024\nds_read_b128 v[136:139], v49 offset:2048\nds_read_b128 v[140:143], v49 offset:3072\n"
    "s_waitcnt vmcnt(3)\ns_barrier\nds_read_b128 v[144:147], v49 offset:4096\nds_read_b128 v[148:151], v49 offset:5120\n"
    "ds_read_b128 v[152:155], v49 offset:6144\nds_read_b128 v[156:159], v49 offset:7168\nbuffer_store_dword v206, v56, %[rc], %[sG] offen sc1\n5:\n"
    "s_waitcnt lgkmcnt(7)\nv_smfmac_f32_16x16x64_bf16 v[160:163], v[128:131], a[0:7], v50\n"
    "v_smfmac_f32_16x16x64_bf16 v[164:167], v[128:131], a[8:15], v50\ns_waitcnt lgkmcnt(6)\n"
    "v_smfmac_f32_16x16x64_bf16 v[160:163], v[132:135], a[16:23], v50\nv_smfmac_f32_16x16x64_bf16 v[164:167], v[132:135], a[24:31], v50\n"
    "s_waitcnt lgkmcnt(5)\nv_smfmac_f32_16x16x64_bf16 v[160:163], v[136:139], a[32:39], v50\n"
    "v_smfmac_f32_16x16x64_bf16 v[164:167], v[136:139], a[40:47], v50\ns_waitcnt lgkmcnt(4)\n"
    "v_smfmac_f32_16x16x64_bf16 v[160:163], v[140:143], a[48:55], v50\nv_smfmac_f32_16x16x64_bf16 v[164:167], v[140:143], a[56:63], v50\n"
    "s_waitcnt lgkmcnt(3)\nv_smfmac_f32_16x16x64_bf16 v[160:163], v[144:147], a[64:71], v50\n"
    "v_smfmac_f32_16x16x64_bf16 v[164:167], v[144:147], a[72:79], v50\ns_waitcnt lgkmcnt(2)\n"
    "v_smfmac_f32_16x16x64_bf16 v[160:163], v[148:151], a[80:87], v50\nv_smfmac_f32_16x16x64_bf16 v[164:167], v[148:151], a[88:95], v50\n"
    "s_waitcnt lgkmcnt(1)\nv_smfmac_f32_16x16x64_bf16 v[160:163], v[152:155], a[96:103], v50\n"
    "v_smfmac_f32_16x16x64_bf16 v[164:167], v[152:155], a[104:111], v50\ns_waitcnt lgkmcnt(0)\n"
    "v_smfmac_f32_16x16x64_bf16 v[160:163], v[156:159], a[112:119], v50\nv_smfmac_f32_16x16x64_bf16 v[164:167], v[156:159], a[120:127], v50\ns_nop 6\n"
    "v_pk_add_f32 v[160:161], v[160:161], v[162:163]\nv_pk_add_f32 v[164:165], v[164:165], v[166:167]\nv_cmp_u_f32 vcc, v160, v161\n"
    "v_cvt_pk_bf16_f32 v204, v160, v164\nv_cvt_pk_bf16_f32 v205, v161, v165\nds_write2_b32 v51, v204, v205 offset1:4\ns_cbranch_vccnz 7f\n6:\n"
    "s_waitcnt lgkmcnt(0)\ns_barrier\nds_read_b128 v[128:131], v52\nds_read_b128 v[132:135], v52 offset:1088\ns_add_u32 s84, %[sY], 0x10000\n"
    "s_waitcnt lgkmcnt(1)\nv_smfmac_f32_16x16x64_bf16 v[168:171], v[128:131], a[128:135], v50\n"
    "v_smfmac_f32_16x16x64_bf16 v[176:179], v[128:131], a[160:167], v50\ns_waitcnt lgkmcnt(0)\n"
    "v_smfmac_f32_16x16x64_bf16 v[168:171], v[132:135], a[136:143], v50\nv_smfmac_f32_16x16x64_bf16 v[176:179], v[132:135], a[168:175], v50\n"
    "v_smfmac_f32_16x16x64_bf16 v[172:175], v[128:131], a[144:151], v50\nv_smfmac_f32_16x16x64_bf16 v[172:175], v[132:135], a[152:159], v50\ns_nop 2\n"
    "v_smfmac_f32_16x16x64_bf16 v[180:183], v[128:131], a[176:183], v50\nv_smfmac_f32_16x16x64_bf16 v[180:183], v[132:135], a[184:191], v50\n"
    "v_add_f32 v168, v168, v170\nv_smfmac_f32_16x16x64_bf16 v[184:187], v[128:131], a[192:199], v50\nv_add_f32 v169, v169, v171\n"
    "v_cvt_pk_bf16_f32 v208, v168, v169\nv_smfmac_f32_16x16x64_bf16 v[184:187], v[132:135], a[200:207], v50\nv_add_f32 v176, v176, v178\n"
    "v_smfmac_f32_16x16x64_bf16 v[192:195], v[128:131], a[224:231], v50\nv_add_f32 v177, v177, v179\nv_cvt_pk_bf16_f32 v210, v176, v177\n"
    "v_smfmac_f32_16x16x64_bf16 v[192:195], v[132:135], a[232:239], v50\nv_add_f32 v172, v172, v174\n"
    "v_smfmac_f32_16x16x64_bf16 v[188:191], v[128:131], a[208:215], v50\nv_add_f32 v173, v173, v175\nv_cvt_pk_bf16_f32 v209, v172, v173\n"
    "v_smfmac_f32_16x16x64_bf16 v[188:191], v[132:135], a[216:223], v50\nv_add_f32 v180, v180, v182\n"
    "v_smfmac_f32_16x16x64_bf16 v[196:199], v[128:131], a[240:247], v50\nv_add_f32 v181, v181, v183\nv_cvt_pk_bf16_f32 v211, v180, v181\n"
    "v_smfmac_f32_16x16x64_bf16 v[196:199], v[132:135], a[248:255], v50\nv_add_f32 v184, v184, v186\nv_add_f32 v185, v185, v187\n"
    "v_cvt_pk_bf16_f32 v212, v184, v185\nv_permlane32_swap_b32 v208, v210\nv_permlane32_swap_b32 v209, v211\ns_nop 1\nv_permlane16_swap_b32 v208, v209\n"
    "v_permlane16_swap_b32 v210, v211\nbuffer_store_dwordx4 v[208:211], v54, %[rc], %[sY] offen sc1\nv_add_f32 v192, v192, v194\n"
    "v_add_f32 v193, v193, v195\nv_cvt_pk_bf16_f32 v214, v192, v193\nv_add_f32 v188, v188, v190\nv_add_f32 v189, v189, v191\n"
    "v_cvt_pk_bf16_f32 v213, v188, v189\nv_add_f32 v196, v196, v198\nv_add_f32 v197, v197, v199\nv_cvt_pk_bf16_f32 v215, v196, v197\n"
    "v_permlane32_swap_b32 v212, v214\ns_nop 0\nv_permlane32_swap_b32 v213, v215\ns_nop 1\nv_permlane16_swap_b32 v212, v213\n"
    "v_permlane16_swap_b32 v214, v215\nbuffer_store_dwordx4 v[212:215], v54, %[rc], s84 offen sc1\ns_cmp_eq_u32 s91, 0\ns_cbranch_scc1 81f\n"
    "s_waitcnt vmcnt(2)\n81:\ns_waitcnt vmcnt(6)\ns_branch 9f\n7:\nv_max_u32 v200, v128, v129\nv_max3_u32 v200, v200, v130, v131\n"
    "v_max3_u32 v200, v200, v132, v133\nv_max3_u32 v200, v200, v134, v135\nv_max3_u32 v200, v200, v136, v137\nv_max3_u32 v200, v200, v138, v139\n"
    "v_max3_u32 v200, v200, v140, v141\nv_max3_u32 v200, v200, v142, v143\nv_max3_u32 v200, v200, v144, v145\nv_max3_u32 v200, v200, v146, v147\n"
    "v_max3_u32 v200, v200, v148, v149\nv_max3_u32 v200, v200, v150, v151\nv_max3_u32 v200, v200, v152, v153\nv_max3_u32 v200, v200, v154, v155\n"
    "v_max3_u32 v200, v200, v156, v157\nv_max3_u32 v200, v200, v158, v159\nv_cmp_eq_u32 vcc, -1, v200\ns_cbranch_vccz 6b\ns_cmp_eq_u32 s91, 0\n"
    "s_cbranch_scc0 6b\ns_mov_b32 s91, 1\nbuffer_load_dwordx4 v[104:107], v48, %[rc], s87 offen sc1\n"
    "buffer_load_dwordx4 v[108:111], v55, %[rc], s87 offen sc1\ns_waitcnt lgkmcnt(0)\ns_barrier\ns_nop 15\ns_nop 15\n"
    "buffer_load_dwordx4 v[112:115], v48, %[rc], s88 offen sc1\nbuffer_load_dwordx4 v[116:119], v55, %[rc], s88 offen sc1\ns_nop 15\ns_nop 15\n"
    "buffer_load_dwordx4 v[96:99], v48, %[rc], s89 offen sc1\nbuffer_load_dwordx4 v[100:103], v55, %[rc], s89 offen sc1\nv_mov_b64 v[160:161], 0\n"
    "v_mov_b64 v[162:163], 0\nv_mov_b64 v[164:165], 0\nv_mov_b64 v[166:167], 0\ns_add_u32 %[nret], %[nret], 1\ns_waitcnt vmcnt(4)\n"
    "ds_write_b128 v57, v[104:107]\nds_write_b128 v57, v[108:111] offset:4096\nv_max3_u32 v200, v104, v105, v106\nv_max3_u32 v201, v108, v109, v110\n"
    "v_max3_u32 v200, v200, v107, v111\nv_max_u32 v200, v200, v201\nv_cmp_eq_u32 vcc, -1, v200\ns_waitcnt lgkmcnt(0)\ns_cbranch_vccz 42f\n"
    "buffer_load_dwordx4 v[104:107], v48, %[rc], s94 offen sc1\nbuffer_load_dwordx4 v[108:111], v55, %[rc], s94 offen sc1\n"
    "s_add_u32 %[nret], %[nret], 1\ns_waitcnt vmcnt(4)\nds_write_b128 v57, v[112:115]\nds_write_b128 v57, v[116:119] offset:4096\n"
    "v_max3_u32 v200, v112, v113, v114\nv_max3_u32 v201, v116, v117, v118\nv_max3_u32 v200, v200, v115, v119\nv_max_u32 v200, v200, v201\n"
    "v_cmp_eq_u32 vcc, -1, v200\ns_waitcnt lgkmcnt(0)\ns_cbranch_vccz 42f\nbuffer_load_dwordx4 v[112:115], v48, %[rc], s87 offen sc1\n"
    "buffer_load_dwordx4 v[116:119], v55, %[rc], s87 offen sc1\n41:\ns_add_u32 %[nret], %[nret], 1\ns_waitcnt vmcnt(4)\nds_write_b128 v57, v[96:99]\n"
    "ds_write_b128 v57, v[100:103] offset:4096\nv_max3_u32 v200, v96, v97, v98\nv_max3_u32 v201, v100, v101, v102\nv_max3_u32 v200, v200, v99, v103\n"
    "v_max_u32 v200, v200, v201\nv_cmp_eq_u32 vcc, -1, v200\ns_waitcnt lgkmcnt(0)\ns_cbranch_vccz 42f\n"
    "buffer_load_dwordx4 v[96:99], v48, %[rc], s88 offen sc1\nbuffer_load_dwordx4 v[100:103], v55, %[rc], s88 offen sc1\n"
    "s_add_u32 %[nret], %[nret], 1\ns_waitcnt vmcnt(4)\nds_write_b128 v57, v[104:107]\nds_write_b128 v57, v[108:111] offset:4096\n"
    "v_max3_u32 v200, v104, v105, v106\nv_max3_u32 v201, v108, v109, v110\nv_max3_u32 v200, v200, v107, v111\nv_max_u32 v200, v200, v201\n"
    "v_cmp_eq_u32 vcc, -1, v200\ns_waitcnt lgkmcnt(0)\ns_cbranch_vccz 42f\nbuffer_load_dwordx4 v[104:107], v48, %[rc], s89 offen sc1\n"
    "buffer_load_dwordx4 v[108:111], v55, %[rc], s89 offen sc1\ns_add_u32 %[nret], %[nret], 1\ns_waitcnt vmcnt(4)\nds_write_b128 v57, v[112:115]\n"
    "ds_write_b128 v57, v[116:119] offset:4096\nv_max3_u32 v200, v112, v113, v114\nv_max3_u32 v201, v116, v117, v118\nv_max3_u32 v200, v200, v115, v119\n"
    "v_max_u32 v200, v200, v201\nv_cmp_eq_u32 vcc, -1, v200\ns_waitcnt lgkmcnt(0)\ns_cbranch_vccz 42f\n"
    "buffer_load_dwordx4 v[112:115], v48, %[rc], s94 offen sc1\nbuffer_load_dwordx4 v[116:119], v55, %[rc], s94 offen sc1\n"
    "s_add_u32 %[nret], %[nret], 1\ns_waitcnt vmcnt(4)\nds_write_b128 v57, v[96:99]\nds_write_b128 v57, v[100:103] offset:4096\n"
    "v_max3_u32 v200, v96, v97, v98\nv_max3_u32 v201, v100, v101, v102\nv_max3_u32 v200, v200, v99, v103\nv_max_u32 v200, v200, v201\n"
    "v_cmp_eq_u32 vcc, -1, v200\ns_waitcnt lgkmcnt(0)\ns_cbranch_vccz 42f\nbuffer_load_dwordx4 v[96:99], v48, %[rc], s87 offen sc1\n"
    "buffer_load_dwordx4 v[100:103], v55, %[rc], s87 offen sc1\ns_add_u32 %[nret], %[nret], 1\ns_waitcnt vmcnt(4)\nds_write_b128 v57, v[104:107]\n"
    "ds_write_b128 v57, v[108:111] offset:4096\nv_max3_u32 v200, v104, v105, v106\nv_max3_u32 v201, v108, v109, v110\nv_max3_u32 v200, v200, v107, v111\n"
    "v_max_u32 v200, v200, v201\nv_cmp_eq_u32 vcc, -1, v200\ns_waitcnt lgkmcnt(0)\ns_cbranch_vccz 42f\n"
    "buffer_load_dwordx4 v[104:107], v48, %[rc], s88 offen sc1\nbuffer_load_dwordx4 v[108:111], v55, %[rc], s88 offen sc1\n"
    "s_add_u32 %[nret], %[nret], 1\ns_waitcnt vmcnt(4)\nds_write_b128 v57, v[112:115]\nds_write_b128 v57, v[116:119] offset:4096\n"
    "v_max3_u32 v200, v112, v113, v114\nv_max3_u32 v201, v116, v117, v118\nv_max3_u32 v200, v200, v115, v119\nv_max_u32 v200, v200, v201\n"
    "v_cmp_eq_u32 vcc, -1, v200\ns_waitcnt lgkmcnt(0)\ns_cbranch_vccz 42f\nbuffer_load_dwordx4 v[112:115], v48, %[rc], s89 offen sc1\n"
    "buffer_load_dwordx4 v[116:119], v55, %[rc], s89 offen sc1\ns_add_u32 %[nret], %[nret], 1\ns_waitcnt vmcnt(4)\nds_write_b128 v57, v[96:99]\n"
    "ds_write_b128 v57, v[100:103] offset:4096\nv_max3_u32 v200, v96, v97, v98\nv_max3_u32 v201, v100, v101, v102\nv_max3_u32 v200, v200, v99, v103\n"
    "v_max_u32 v200, v200, v201\nv_cmp_eq_u32 vcc, -1, v200\ns_waitcnt lgkmcnt(0)\ns_cbranch_vccz 42f\n"
    "buffer_load_dwordx4 v[96:99], v48, %[rc], s94 offen sc1\nbuffer_load_dwordx4 v[100:103], v55, %[rc], s94 offen sc1\n"
    "s_add_u32 %[nret], %[nret], 1\ns_waitcnt vmcnt(4)\nds_write_b128 v57, v[104:107]\nds_write_b128 v57, v[108:111] offset:4096\n"
    "v_max3_u32 v200, v104, v105, v106\nv_max3_u32 v201, v108, v109, v110\nv_max3_u32 v200, v200, v107, v111\nv_max_u32 v200, v200, v201\n"
    "v_cmp_eq_u32 vcc, -1, v200\ns_waitcnt lgkmcnt(0)\ns_cbranch_vccz 42f\nbuffer_load_dwordx4 v[104:107], v48, %[rc], s87 offen sc1\n"
    "buffer_load_dwordx4 v[108:111], v55, %[rc], s87 offen sc1\ns_add_u32 %[nret], %[nret], 1\ns_waitcnt vmcnt(4)\nds_write_b128 v57, v[112:115]\n"
    "ds_write_b128 v57, v[116:119] offset:4096\nv_max3_u32 v200, v112, v113, v114\nv_max3_u32 v201, v116, v117, v118\nv_max3_u32 v200, v200, v115, v119\n"
    "v_max_u32 v200, v200, v201\nv_cmp_eq_u32 vcc, -1, v200\ns_waitcnt lgkmcnt(0)\ns_cbranch_vccz 42f\n"
    "buffer_load_dwordx4 v[112:115], v48, %[rc], s88 offen sc1\nbuffer_load_dwordx4 v[116:119], v55, %[rc], s88 offen sc1\n"
    "s_add_u32 %[nret], %[nret], 1\ns_waitcnt vmcnt(4)\nds_write_b128 v57, v[96:99]\nds_write_b128 v57, v[100:103] offset:4096\n"
    "v_max3_u32 v200, v96, v97, v98\nv_max3_u32 v201, v100, v101, v102\nv_max3_u32 v200, v200, v99, v103\nv_max_u32 v200, v200, v201\n"
    "v_cmp_eq_u32 vcc, -1, v200\ns_waitcnt lgkmcnt(0)\ns_cbranch_vccz 42f\nbuffer_load_dwordx4 v[96:99], v48, %[rc], s89 offen sc1\n"
    "buffer_load_dwordx4 v[100:103], v55, %[rc], s89 offen sc1\ns_add_u32 %[nret], %[nret], 1\ns_waitcnt vmcnt(4)\nds_write_b128 v57, v[104:107]\n"
    "ds_write_b128 v57, v[108:111] offset:4096\nv_max3_u32 v200, v104, v105, v106\nv_max3_u32 v201, v108, v109, v110\nv_max3_u32 v200, v200, v107, v111\n"
    "v_max_u32 v200, v200, v201\nv_cmp_eq_u32 vcc, -1, v200\ns_waitcnt lgkmcnt(0)\ns_cbranch_vccz 42f\n"
    "buffer_load_dwordx4 v[104:107], v48, %[rc], s94 offen sc1\nbuffer_load_dwordx4 v[108:111], v55, %[rc], s94 offen sc1\n"
    "s_add_u32 %[nret], %[nret], 1\ns_waitcnt vmcnt(4)\nds_write_b128 v57, v[112:115]\nds_write_b128 v57, v[116:119] offset:4096\n"
    "v_max3_u32 v200, v112, v113, v114\nv_max3_u32 v201, v116, v117, v118\nv_max3_u32 v200, v200, v115, v119\nv_max_u32 v200, v200, v201\n"
    "v_cmp_eq_u32 vcc, -1, v200\ns_waitcnt lgkmcnt(0)\ns_cbranch_vccz 42f\nbuffer_load_dwordx4 v[112:115], v48, %[rc], s87 offen sc1\n"
    "buffer_load_dwordx4 v[116:119], v55, %[rc], s87 offen sc1\ns_sub_u32 %[bud], %[bud], 4\ns_cmp_gt_i32 %[bud], 0\ns_cbranch_scc1 41b\n42:\ns_barrier\n"
    "ds_read_b128 v[128:131], v49 offset:0\nds_read_b128 v[132:135], v49 offset:1024\nds_read_b128 v[136:139], v49 offset:2048\n"
    "ds_read_b128 v[140:143], v49 offset:3072\nds_read_b128 v[144:147], v49 offset:4096\nds_read_b128 v[148:151], v49 offset:5120\n"
    "ds_read_b128 v[152:155], v49 offset:6144\nds_read_b128 v[156:159], v49 offset:7168\ns_branch 5b\n9:\ns_nop 1\n"
    : [bud] "+s"(bud), [nret] "+s"(nret)
    : [rc] "s"(rc), [sM] "s"(sM), [sFM] "s"(sFM), [sY] "s"(sY), [sYp] "s"(sYp), [sSp] "s"(sSp), [lm] "s"(lm), [wM] "s"(wM), [wW] "s"(wW), [sv] "s"(sv), [sG] "s"(sG), "{v[48:63]}"(cv)
    : "memory", "vcc", "scc", CLS, CLV_96_227);
}
DI void dma_rows(rsrc_t rc, unsigned sH, unsigned ld, unsigned vdm) {
  asm volatile(
    "s_nop 4\ns_mov_b32 s86, %[sH]\ns_mov_b32 m0, %[ld]\nv_xor_b32 v64, 0, %[vdm]\nbuffer_load_dwordx4 v64, %[rc], s86 offen sc1 lds\n"
    "s_add_u32 s86, s86, 1024\ns_add_u32 m0, m0, 1024\nv_xor_b32 v65, 16, %[vdm]\nbuffer_load_dwordx4 v65, %[rc], s86 offen sc1 lds\n"
    "s_add_u32 s86, s86, 1024\ns_add_u32 m0, m0, 1024\nv_xor_b32 v64, 32, %[vdm]\nbuffer_load_dwordx4 v64, %[rc], s86 offen sc1 lds\n"
    "s_add_u32 s86, s86, 1024\ns_add_u32 m0, m0, 1024\nv_xor_b32 v65, 48, %[vdm]\nbuffer_load_dwordx4 v65, %[rc], s86 offen sc1 lds\n"
    "s_add_u32 s86, s86, 1024\ns_add_u32 m0, m0, 1024\nv_xor_b32 v64, 64, %[vdm]\nbuffer_load_dwordx4 v64, %[rc], s86 offen sc1 lds\n"
    "s_add_u32 s86, s86, 1024\ns_add_u32 m0, m0, 1024\nv_xor_b32 v65, 80, %[vdm]\nbuffer_load_dwordx4 v65, %[rc], s86 offen sc1 lds\n"
    "s_add_u32 s86, s86, 1024\ns_add_u32 m0, m0, 1024\nv_xor_b32 v64, 96, %[vdm]\nbuffer_load_dwordx4 v64, %[rc], s86 offen sc1 lds\n"
    "s_add_u32 s86, s86, 1024\ns_add_u32 m0, m0, 1024\nv_xor_b32 v65, 112, %[vdm]\nbuffer_load_dwordx4 v65, %[rc], s86 offen sc1 lds\n"
    :: [rc] "s"(rc), [sH] "s"(sH), [ld] "s"(ld), [vdm] "v"(vdm) : "memory", "scc", CLS, "v64", "v65");
}
DI void f1_loop(rsrc_t rc, unsigned lr, unsigned lend, unsigned sH, unsigned sHmax, unsigned ld, unsigned par, unsigned wv, const v16u& cv) {
  asm volatile(
    "s_nop 4\ns_mov_b32 s87, 0\ns_mov_b32 s86, %[sH]\ns_mov_b32 s90, %[ld]\ns_mov_b32 s91, %[lr]\ns_mov_b32 s84, 4096\nv_add_u32 v64, s91, v48\n"
    "v_add_u32 v65, s91, v49\nv_add_u32 v66, s91, v50\nv_add_u32 v67, s91, v51\ns_waitcnt vmcnt(8)\ns_barrier\nds_read_b128 v[72:75], v64 offset:0\n"
    "ds_read_b128 v[76:79], v65 offset:0\nds_read_b128 v[80:83], v66 offset:0\nds_read_b128 v[84:87], v67 offset:0\nds_read_b128 v[88:91], v64 offset:256\n"
    "ds_read_b128 v[92:95], v65 offset:256\ns_waitcnt vmcnt(0)\n.p2align 6\n1:\ns_and_b32 s85, s87, 7\ns_or_b32 s85, s85, %[wv]\ns_cmp_lg_u32 s85, 0\n"
    "s_cbranch_scc1 22f\ns_cmp_lt_u32 s87, 24\ns_cbranch_scc1 22f\ns_sub_u32 s85, s87, 21\ns_mov_b32 s88, 200000\n21:\n"
    "buffer_load_dword v70, v58, %[rc], 0 offen sc1\ns_waitcnt vmcnt(0)\nv_cmp_gt_u32 vcc, s85, v70\ns_cbranch_vccz 22f\ns_sleep 8\n"
    "s_sub_u32 s88, s88, 1\ns_cmp_gt_i32 s88, 0\ns_cbranch_scc1 21b\n22:\ns_waitcnt vmcnt(3)\ns_barrier\ns_waitcnt lgkmcnt(5)\n"
    "v_mfma_f32_16x16x32_bf16 v[104:107], a[0:3], v[72:75], 0\nds_read_b128 v[96:99], v66 offset:256\n"
    "v_mfma_f32_16x16x32_bf16 v[108:111], a[8:11], v[72:75], 0\nv_mfma_f32_16x16x32_bf16 v[112:115], a[16:19], v[72:75], 0\n"
    "v_mfma_f32_16x16x32_bf16 v[116:119], a[24:27], v[72:75], 0\ns_waitcnt lgkmcnt(5)\nv_mfma_f32_16x16x32_bf16 v[104:107], a[4:7], v[76:79], v[104:107]\n"
    "ds_read_b128 v[100:103], v67 offset:256\nv_mfma_f32_16x16x32_bf16 v[108:111], a[12:15], v[76:79], v[108:111]\n"
    "v_mfma_f32_16x16x32_bf16 v[112:115], a[20:23], v[76:79], v[112:115]\nv_mfma_f32_16x16x32_bf16 v[116:119], a[28:31], v[76:79], v[116:119]\n"
    "s_waitcnt lgkmcnt(5)\nv_mfma_f32_16x16x32_bf16 v[104:107], a[32:35], v[80:83], v[104:107]\nds_read_b128 v[72:75], v64 offset:512\n"
    "v_mfma_f32_16x16x32_bf16 v[108:111], a[40:43], v[80:83], v[108:111]\nv_cvt_pk_bf16_f32 v144, v120, v124\n"
    "v_mfma_f32_16x16x32_bf16 v[112:115], a[48:51], v[80:83], v[112:115]\nv_cvt_pk_bf16_f32 v145, v128, v132\n"
    "v_mfma_f32_16x16x32_bf16 v[116:119], a[56:59], v[80:83], v[116:119]\ns_waitcnt lgkmcnt(5)\n"
    "v_mfma_f32_16x16x32_bf16 v[104:107], a[36:39], v[84:87], v[104:107]\nv_cvt_pk_bf16_f32 v146, v121, v125\nds_read_b128 v[76:79], v65 offset:512\n"
    "v_mfma_f32_16x16x32_bf16 v[108:111], a[44:47], v[84:87], v[108:111]\nv_cvt_pk_bf16_f32 v147, v129, v133\n"
    "v_mfma_f32_16x16x32_bf16 v[112:115], a[52:55], v[84:87], v[112:115]\nbuffer_store_dwordx4 v[144:147], v53, %[rc], s84 offen offset:0 sc1\n"
    "v_mfma_f32_16x16x32_bf16 v[116:119], a[60:63], v[84:87], v[116:119]\ns_waitcnt lgkmcnt(5)\n"
    "v_mfma_f32_16x16x32_bf16 v[104:107], a[64:67], v[88:91], v[104:107]\nv_cvt_pk_bf16_f32 v148, v122, v126\nds_read_b128 v[80:83], v66 offset:512\n"
    "v_mfma_f32_16x16x32_bf16 v[108:111], a[72:75], v[88:91], v[108:111]\nv_cvt_pk_bf16_f32 v149, v130, v134\n"
    "v_mfma_f32_16x16x32_bf16 v[112:115], a[80:83], v[88:91], v[112:115]\nv_cvt_pk_bf16_f32 v150, v123, v127\n"
    "v_mfma_f32_16x16x32_bf16 v[116:119], a[88:91], v[88:91], v[116:119]\ns_and_b32 s88, s87, 31\ns_waitcnt lgkmcnt(5)\n"
    "v_mfma_f32_16x16x32_bf16 v[104:107], a[68:71], v[92:95], v[104:107]\nv_cvt_pk_bf16_f32 v151, v131, v135\nds_read_b128 v[84:87], v67 offset:512\n"
    "v_mfma_f32_16x16x32_bf16 v[108:111], a[76:79], v[92:95], v[108:111]\nbuffer_store_dwordx4 v[148:151], v53, %[rc], s84 offen offset:1024 sc1\n"
    "v_mfma_f32_16x16x32_bf16 v[112:115], a[84:87], v[92:95], v[112:115]\ns_lshr_b32 s85, s88, 1\n"
    "v_mfma_f32_16x16x32_bf16 v[116:119], a[92:95], v[92:95], v[116:119]\ns_bitcmp1_b32 %[par], s85\ns_cselect_b32 s85, 0x2000, 0\ns_waitcnt lgkmcnt(5)\n"
    "v_mfma_f32_16x16x32_bf16 v[104:107], a[96:99], v[96:99], v[104:107]\nds_read_b128 v[88:91], v64 offset:768\n"
    "v_mfma_f32_16x16x32_bf16 v[108:111], a[104:107], v[96:99], v[108:111]\ns_lshl_b32 s84, s88, 20\ns_add_u32 s84, s84, s85\n"
    "v_mfma_f32_16x16x32_bf16 v[112:115], a[112:115], v[96:99], v[112:115]\ns_mov_b32 m0, s90\nv_xor_b32 v68, 0, v52\n"
    "v_mfma_f32_16x16x32_bf16 v[116:119], a[120:123], v[96:99], v[116:119]\nbuffer_load_dwordx4 v68, %[rc], s86 offen sc1 lds\ns_waitcnt lgkmcnt(5)\n"
    "v_mfma_f32_16x16x32_bf16 v[104:107], a[100:103], v[100:103], v[104:107]\ns_add_u32 s86, s86, 1024\nds_read_b128 v[92:95], v65 offset:768\n"
    "v_mfma_f32_16x16x32_bf16 v[108:111], a[108:111], v[100:103], v[108:111]\nv_mfma_f32_16x16x32_bf16 v[112:115], a[116:119], v[100:103], v[112:115]\n"
    "s_add_u32 m0, m0, 1024\nv_xor_b32 v68, 16, v52\nv_mfma_f32_16x16x32_bf16 v[116:119], a[124:127], v[100:103], v[116:119]\n"
    "buffer_load_dwordx4 v68, %[rc], s86 offen sc1 lds\ns_waitcnt lgkmcnt(5)\nv_mfma_f32_16x16x32_bf16 v[104:107], a[128:131], v[72:75], v[104:107]\n"
    "s_add_u32 s86, s86, 1024\nds_read_b128 v[96:99], v66 offset:768\nv_mfma_f32_16x16x32_bf16 v[108:111], a[136:139], v[72:75], v[108:111]\n"
    "v_mfma_f32_16x16x32_bf16 v[112:115], a[144:147], v[72:75], v[112:115]\ns_add_u32 m0, m0, 1024\nv_xor_b32 v68, 32, v52\n"
    "v_mfma_f32_16x16x32_bf16 v[116:119], a[152:155], v[72:75], v[116:119]\nbuffer_load_dwordx4 v68, %[rc], s86 offen sc1 lds\ns_waitcnt lgkmcnt(5)\n"
    "v_mfma_f32_16x16x32_bf16 v[104:107], a[132:135], v[76:79], v[104:107]\ns_add_u32 s86, s86, 1024\nds_read_b128 v[100:103], v67 offset:768\n"
    "v_mfma_f32_16x16x32_bf16 v[108:111], a[140:143], v[76:79], v[108:111]\nv_mfma_f32_16x16x32_bf16 v[112:115], a[148:151], v[76:79], v[112:115]\n"
    "s_add_u32 m0, m0, 1024\nv_xor_b32 v68, 48, v52\nv_mfma_f32_16x16x32_bf16 v[116:119], a[156:159], v[76:79], v[116:119]\n"
    "buffer_load_dwordx4 v68, %[rc], s86 offen sc1 lds\ns_waitcnt lgkmcnt(5)\nv_mfma_f32_16x16x32_bf16 v[104:107], a[160:163], v[80:83], v[104:107]\n"
    "s_add_u32 s86, s86, 1024\nds_read_b128 v[72:75], v64 offset:16384\nv_mfma_f32_16x16x32_bf16 v[108:111], a[168:171], v[80:83], v[108:111]\n"
    "v_mfma_f32_16x16x32_bf16 v[112:115], a[176:179], v[80:83], v[112:115]\ns_add_u32 m0, m0, 1024\nv_xor_b32 v68, 64, v52\n"
    "v_mfma_f32_16x16x32_bf16 v[116:119], a[184:187], v[80:83], v[116:119]\nbuffer_load_dwordx4 v68, %[rc], s86 offen sc1 lds\ns_waitcnt lgkmcnt(5)\n"
    "v_mfma_f32_16x16x32_bf16 v[104:107], a[164:167], v[84:87], v[104:107]\ns_add_u32 s86, s86, 1024\nds_read_b128 v[76:79], v65 offset:16384\n"
    "v_mfma_f32_16x16x32_bf16 v[108:111], a[172:175], v[84:87], v[108:111]\nv_mfma_f32_16x16x32_bf16 v[112:115], a[180:183], v[84:87], v[112:115]\n"
    "s_add_u32 m0, m0, 1024\nv_xor_b32 v68, 80, v52\nv_mfma_f32_16x16x32_bf16 v[116:119], a[188:191], v[84:87], v[116:119]\n"
    "buffer_load_dwordx4 v68, %[rc], s86 offen sc1 lds\ns_waitcnt lgkmcnt(5)\nv_mfma_f32_16x16x32_bf16 v[104:107], a[192:195], v[88:91], v[104:107]\n"
    "s_add_u32 s86, s86, 1024\nds_read_b128 v[80:83], v66 offset:16384\nv_mfma_f32_16x16x32_bf16 v[108:111], a[200:203], v[88:91], v[108:111]\n"
    "v_mfma_f32_16x16x32_bf16 v[112:115], a[208:211], v[88:91], v[112:115]\ns_add_u32 m0, m0, 1024\nv_xor_b32 v68, 96, v52\n"
    "v_mfma_f32_16x16x32_bf16 v[116:119], a[216:219], v[88:91], v[116:119]\nbuffer_load_dwordx4 v68, %[rc], s86 offen sc1 lds\ns_waitcnt lgkmcnt(5)\n"
    "v_mfma_f32_16x16x32_bf16 v[104:107], a[196:199], v[92:95], v[104:107]\ns_add_u32 s86, s86, 1024\nds_read_b128 v[84:87], v67 offset:16384\n"
    "v_mfma_f32_16x16x32_bf16 v[108:111], a[204:207], v[92:95], v[108:111]\nv_mfma_f32_16x16x32_bf16 v[112:115], a[212:215], v[92:95], v[112:115]\n"
    "s_add_u32 m0, m0, 1024\nv_xor_b32 v68, 112, v52\nv_mfma_f32_16x16x32_bf16 v[116:119], a[220:223], v[92:95], v[116:119]\n"
    "buffer_load_dwordx4 v68, %[rc], s86 offen sc1 lds\ns_waitcnt lgkmcnt(5)\nv_mfma_f32_16x16x32_bf16 v[104:107], a[224:227], v[96:99], v[104:107]\n"
    "s_add_u32 s86, s86, 0x6400\nds_read_b128 v[88:91], v64 offset:16640\nv_mfma_f32_16x16x32_bf16 v[108:111], a[232:235], v[96:99], v[108:111]\n"
    "s_min_u32 s86, s86, %[sHmax]\ns_add_u32 s90, s90, 0x8000\nv_mfma_f32_16x16x32_bf16 v[112:115], a[240:243], v[96:99], v[112:115]\n"
    "s_cmp_ge_u32 s90, %[lend]\ns_cselect_b32 s85, 0x18000, 0\nv_mfma_f32_16x16x32_bf16 v[116:119], a[248:251], v[96:99], v[116:119]\n"
    "s_sub_u32 s90, s90, s85\ns_waitcnt lgkmcnt(5)\nv_mfma_f32_16x16x32_bf16 v[104:107], a[228:231], v[100:103], v[104:107]\n"
    "ds_read_b128 v[92:95], v65 offset:16640\nv_mfma_f32_16x16x32_bf16 v[108:111], a[236:239], v[100:103], v[108:111]\nv_mov_b32 v70, s87\n"
    "s_cmp_gt_u32 s87, 3\nv_mfma_f32_16x16x32_bf16 v[112:115], a[244:247], v[100:103], v[112:115]\ns_cbranch_scc1 33f\ns_waitcnt vmcnt(8)\ns_barrier\n"
    "buffer_store_dword v70, v59, %[rc], 0 offen sc1\n33:\nv_mfma_f32_16x16x32_bf16 v[116:119], a[252:255], v[100:103], v[116:119]\ns_waitcnt lgkmcnt(5)\n"
    "v_mfma_f32_16x16x32_bf16 v[120:123], a[0:3], v[72:75], 0\nds_read_b128 v[96:99], v66 offset:16640\n"
    "v_mfma_f32_16x16x32_bf16 v[124:127], a[8:11], v[72:75], 0\nv_mfma_f32_16x16x32_bf16 v[128:131], a[16:19], v[72:75], 0\n"
    "v_mfma_f32_16x16x32_bf16 v[132:135], a[24:27], v[72:75], 0\ns_waitcnt lgkmcnt(5)\nv_mfma_f32_16x16x32_bf16 v[120:123], a[4:7], v[76:79], v[120:123]\n"
    "ds_read_b128 v[100:103], v67 offset:16640\nv_mfma_f32_16x16x32_bf16 v[124:127], a[12:15], v[76:79], v[124:127]\n"
    "v_mfma_f32_16x16x32_bf16 v[128:131], a[20:23], v[76:79], v[128:131]\nv_mfma_f32_16x16x32_bf16 v[132:135], a[28:31], v[76:79], v[132:135]\n"
    "s_waitcnt lgkmcnt(5)\nv_mfma_f32_16x16x32_bf16 v[120:123], a[32:35], v[80:83], v[120:123]\nds_read_b128 v[72:75], v64 offset:16896\n"
    "v_mfma_f32_16x16x32_bf16 v[124:127], a[40:43], v[80:83], v[124:127]\nv_cvt_pk_bf16_f32 v136, v104, v108\n"
    "v_mfma_f32_16x16x32_bf16 v[128:131], a[48:51], v[80:83], v[128:131]\nv_cvt_pk_bf16_f32 v137, v112, v116\n"
    "v_mfma_f32_16x16x32_bf16 v[132:135], a[56:59], v[80:83], v[132:135]\ns_waitcnt lgkmcnt(5)\n"
    "v_mfma_f32_16x16x32_bf16 v[120:123], a[36:39], v[84:87], v[120:123]\nv_cvt_pk_bf16_f32 v138, v105, v109\nds_read_b128 v[76:79], v65 offset:16896\n"
    "v_mfma_f32_16x16x32_bf16 v[124:127], a[44:47], v[84:87], v[124:127]\nv_cvt_pk_bf16_f32 v139, v113, v117\n"
    "v_mfma_f32_16x16x32_bf16 v[128:131], a[52:55], v[84:87], v[128:131]\nbuffer_store_dwordx4 v[136:139], v53, %[rc], s84 offen offset:0 sc1\n"
    "v_mfma_f32_16x16x32_bf16 v[132:135], a[60:63], v[84:87], v[132:135]\ns_waitcnt lgkmcnt(5)\n"
    "v_mfma_f32_16x16x32_bf16 v[120:123], a[64:67], v[88:91], v[120:123]\nv_cvt_pk_bf16_f32 v140, v106, v110\nds_read_b128 v[80:83], v66 offset:16896\n"
    "v_mfma_f32_16x16x32_bf16 v[124:127], a[72:75], v[88:91], v[124:127]\nv_cvt_pk_bf16_f32 v141, v114, v118\n"
    "v_mfma_f32_16x16x32_bf16 v[128:131], a[80:83], v[88:91], v[128:131]\nv_cvt_pk_bf16_f32 v142, v107, v111\n"
    "v_mfma_f32_16x16x32_bf16 v[132:135], a[88:91], v[88:91], v[132:135]\ns_waitcnt lgkmcnt(5)\n"
    "v_mfma_f32_16x16x32_bf16 v[120:123], a[68:71], v[92:95], v[120:123]\nv_cvt_pk_bf16_f32 v143, v115, v119\nds_read_b128 v[84:87], v67 offset:16896\n"
    "v_mfma_f32_16x16x32_bf16 v[124:127], a[76:79], v[92:95], v[124:127]\nbuffer_store_dwordx4 v[140:143], v53, %[rc], s84 offen offset:1024 sc1\n"
    "v_mfma_f32_16x16x32_bf16 v[128:131], a[84:87], v[92:95], v[128:131]\nv_mfma_f32_16x16x32_bf16 v[132:135], a[92:95], v[92:95], v[132:135]\n"
    "s_waitcnt lgkmcnt(5)\nv_mfma_f32_16x16x32_bf16 v[120:123], a[96:99], v[96:99], v[120:123]\nds_read_b128 v[88:91], v64 offset:17152\n"
    "v_mfma_f32_16x16x32_bf16 v[124:127], a[104:107], v[96:99], v[124:127]\nv_mfma_f32_16x16x32_bf16 v[128:131], a[112:115], v[96:99], v[128:131]\n"
    "s_add_u32 s84, s84, 4096\nv_mfma_f32_16x16x32_bf16 v[132:135], a[120:123], v[96:99], v[132:135]\ns_waitcnt lgkmcnt(5)\n"
    "v_mfma_f32_16x16x32_bf16 v[120:123], a[100:103], v[100:103], v[120:123]\nds_read_b128 v[92:95], v65 offset:17152\n"
    "v_mfma_f32_16x16x32_bf16 v[124:127], a[108:111], v[100:103], v[124:127]\nv_mfma_f32_16x16x32_bf16 v[128:131], a[116:119], v[100:103], v[128:131]\n"
    "v_mfma_f32_16x16x32_bf16 v[132:135], a[124:127], v[100:103], v[132:135]\ns_waitcnt lgkmcnt(5)\n"
    "v_mfma_f32_16x16x32_bf16 v[120:123], a[128:131], v[72:75], v[120:123]\nds_read_b128 v[96:99], v66 offset:17152\n"
    "v_mfma_f32_16x16x32_bf16 v[124:127], a[136:139], v[72:75], v[124:127]\nv_mfma_f32_16x16x32_bf16 v[128:131], a[144:147], v[72:75], v[128:131]\n"
    "v_mfma_f32_16x16x32_bf16 v[132:135], a[152:155], v[72:75], v[132:135]\ns_waitcnt lgkmcnt(5)\n"
    "v_mfma_f32_16x16x32_bf16 v[120:123], a[132:135], v[76:79], v[120:123]\ns_add_u32 s91, s91, 0x8000\ns_cmp_ge_u32 s91, %[lend]\n"
    "ds_read_b128 v[100:103], v67 offset:17152\nv_mfma_f32_16x16x32_bf16 v[124:127], a[140:143], v[76:79], v[124:127]\ns_cselect_b32 s85, 0x18000, 0\n"
    "s_sub_u32 s91, s91, s85\nv_mfma_f32_16x16x32_bf16 v[128:131], a[148:151], v[76:79], v[128:131]\nv_add_u32 v64, s91, v48\nv_add_u32 v65, s91, v49\n"
    "v_mfma_f32_16x16x32_bf16 v[132:135], a[156:159], v[76:79], v[132:135]\nv_add_u32 v66, s91, v50\nv_add_u32 v67, s91, v51\ns_waitcnt lgkmcnt(5)\n"
    "v_mfma_f32_16x16x32_bf16 v[120:123], a[160:163], v[80:83], v[120:123]\nds_read_b128 v[72:75], v64 offset:0\n"
    "v_mfma_f32_16x16x32_bf16 v[124:127], a[168:171], v[80:83], v[124:127]\nv_mfma_f32_16x16x32_bf16 v[128:131], a[176:179], v[80:83], v[128:131]\n"
    "v_mfma_f32_16x16x32_bf16 v[132:135], a[184:187], v[80:83], v[132:135]\ns_waitcnt lgkmcnt(5)\n"
    "v_mfma_f32_16x16x32_bf16 v[120:123], a[164:167], v[84:87], v[120:123]\nds_read_b128 v[76:79], v65 offset:0\n"
    "v_mfma_f32_16x16x32_bf16 v[124:127], a[172:175], v[84:87], v[124:127]\nv_mfma_f32_16x16x32_bf16 v[128:131], a[180:183], v[84:87], v[128:131]\n"
    "s_cmp_gt_u32 s87, 3\ns_cselect_b32 s89, 0, 0x80000000\nv_mfma_f32_16x16x32_bf16 v[132:135], a[188:191], v[84:87], v[132:135]\ns_waitcnt lgkmcnt(5)\n"
    "v_mfma_f32_16x16x32_bf16 v[120:123], a[192:195], v[88:91], v[120:123]\nv_or_b32 v71, s89, v54\nds_read_b128 v[80:83], v66 offset:0\n"
    "v_mfma_f32_16x16x32_bf16 v[124:127], a[200:203], v[88:91], v[124:127]\nv_mfma_f32_16x16x32_bf16 v[128:131], a[208:211], v[88:91], v[128:131]\n"
    "v_mfma_f32_16x16x32_bf16 v[132:135], a[216:219], v[88:91], v[132:135]\ns_waitcnt lgkmcnt(5)\n"
    "v_mfma_f32_16x16x32_bf16 v[120:123], a[196:199], v[92:95], v[120:123]\nds_read_b128 v[84:87], v67 offset:0\n"
    "v_mfma_f32_16x16x32_bf16 v[124:127], a[204:207], v[92:95], v[124:127]\nv_mfma_f32_16x16x32_bf16 v[128:131], a[212:215], v[92:95], v[128:131]\n"
    "s_waitcnt vmcnt(10)\nv_mfma_f32_16x16x32_bf16 v[132:135], a[220:223], v[92:95], v[132:135]\nbuffer_store_dword v70, v71, %[rc], 0 offen sc1\n"
    "s_waitcnt lgkmcnt(5)\nv_mfma_f32_16x16x32_bf16 v[120:123], a[224:227], v[96:99], v[120:123]\nds_read_b128 v[88:91], v64 offset:256\n"
    "v_mfma_f32_16x16x32_bf16 v[124:127], a[232:235], v[96:99], v[124:127]\nv_mfma_f32_16x16x32_bf16 v[128:131], a[240:243], v[96:99], v[128:131]\n"
    "v_mfma_f32_16x16x32_bf16 v[132:135], a[248:251], v[96:99], v[132:135]\ns_waitcnt lgkmcnt(5)\n"
    "v_mfma_f32_16x16x32_bf16 v[120:123], a[228:231], v[100:103], v[120:123]\nds_read_b128 v[92:95], v65 offset:256\n"
    "v_mfma_f32_16x16x32_bf16 v[124:127], a[236:239], v[100:103], v[124:127]\ns_add_u32 s87, s87, 1\n"
    "v_mfma_f32_16x16x32_bf16 v[128:131], a[244:247], v[100:103], v[128:131]\ns_cmp_lt_u32 s87, 64\n"
    "v_mfma_f32_16x16x32_bf16 v[132:135], a[252:255], v[100:103], v[132:135]\ns_cbranch_scc1 1b\ns_nop 7\nv_cvt_pk_bf16_f32 v144, v120, v124\n"
    "v_cvt_pk_bf16_f32 v145, v128, v132\nv_cvt_pk_bf16_f32 v146, v121, v125\nv_cvt_pk_bf16_f32 v147, v129, v133\n"
    "buffer_store_dwordx4 v[144:147], v53, %[rc], s84 offen offset:0 sc1\nv_cvt_pk_bf16_f32 v148, v122, v126\nv_cvt_pk_bf16_f32 v149, v130, v134\n"
    "v_cvt_pk_bf16_f32 v150, v123, v127\nv_cvt_pk_bf16_f32 v151, v131, v135\nbuffer_store_dwordx4 v[148:151], v53, %[rc], s84 offen offset:1024 sc1\n"
    "s_waitcnt vmcnt(0) lgkmcnt(0)\n"
    :
    : [rc] "s"(rc), [lr] "s"(lr), [lend] "s"(lend), [sH] "s"(sH), [sHmax] "s"(sHmax), [ld] "s"(ld), [par] "s"(par), [wv] "s"(wv), "{v[48:63]}"(cv)
    : "memory", "vcc", "scc", CLS, CLV_64_151);
}
DI void f2_loop(rsrc_t rc, unsigned lr, unsigned lend, unsigned sH, unsigned sHmax, unsigned ld, unsigned par, unsigned wv, const v16u& cv) {
  asm volatile(
    "s_nop 4\ns_mov_b32 s87, 0\ns_mov_b32 s86, %[sH]\ns_mov_b32 s90, %[ld]\ns_mov_b32 s91, %[lr]\ns_mov_b32 s84, 0x60000\nv_add_u32 v64, s91, v48\n"
    "v_add_u32 v65, s91, v49\nv_add_u32 v66, s91, v50\nv_add_u32 v67, s91, v51\nv_mov_b32 v184, v55\nv_mov_b32 v185, v56\n"
    "s_waitcnt vmcnt(16)\ns_barrier\nds_read_b128 v[72:75], v64 offset:0\nds_read_b128 v[76:79], v64 offset:16384\nds_read_b128 v[80:83], v65 offset:0\n"
    "ds_read_b128 v[84:87], v65 offset:16384\nds_read_b128 v[88:91], v66 offset:0\nds_read_b128 v[92:95], v66 offset:16384\ns_waitcnt lgkmcnt(5)\n"
    "v_mfma_f32_16x16x32_bf16 v[104:107], a[0:3], v[72:75], 0\nds_read_b128 v[96:99], v67 offset:0\n"
    "v_mfma_f32_16x16x32_bf16 v[108:111], a[8:11], v[72:75], 0\ns_waitcnt lgkmcnt(5)\nv_mfma_f32_16x16x32_bf16 v[112:115], a[0:3], v[76:79], 0\n"
    "ds_read_b128 v[100:103], v67 offset:16384\nv_mfma_f32_16x16x32_bf16 v[116:119], a[8:11], v[76:79], 0\ns_waitcnt lgkmcnt(5)\n"
    "v_mfma_f32_16x16x32_bf16 v[104:107], a[4:7], v[80:83], v[104:107]\nds_read_b128 v[72:75], v64 offset:256\n"
    "v_mfma_f32_16x16x32_bf16 v[108:111], a[12:15], v[80:83], v[108:111]\ns_waitcnt lgkmcnt(5)\n"
    "v_mfma_f32_16x16x32_bf16 v[112:115], a[4:7], v[84:87], v[112:115]\nds_read_b128 v[76:79], v64 offset:16640\n"
    "v_mfma_f32_16x16x32_bf16 v[116:119], a[12:15], v[84:87], v[116:119]\ns_waitcnt lgkmcnt(5)\n"
    "v_mfma_f32_16x16x32_bf16 v[104:107], a[16:19], v[88:91], v[104:107]\nds_read_b128 v[80:83], v65 offset:256\n"
    "v_mfma_f32_16x16x32_bf16 v[108:111], a[24:27], v[88:91], v[108:111]\ns_waitcnt lgkmcnt(5)\n"
    "v_mfma_f32_16x16x32_bf16 v[112:115], a[16:19], v[92:95], v[112:115]\nds_read_b128 v[84:87], v65 offset:16640\n"
    "v_mfma_f32_16x16x32_bf16 v[116:119], a[24:27], v[92:95], v[116:119]\ns_waitcnt lgkmcnt(5)\n"
    "v_mfma_f32_16x16x32_bf16 v[104:107], a[20:23], v[96:99], v[104:107]\nds_read_b128 v[88:91], v66 offset:256\n"
    "v_mfma_f32_16x16x32_bf16 v[108:111], a[28:31], v[96:99], v[108:111]\ns_waitcnt lgkmcnt(5)\n"
    "v_mfma_f32_16x16x32_bf16 v[112:115], a[20:23], v[100:103], v[112:115]\nds_read_b128 v[92:95], v66 offset:16640\n"
    "v_mfma_f32_16x16x32_bf16 v[116:119], a[28:31], v[100:103], v[116:119]\ns_waitcnt lgkmcnt(5)\n"
    "v_mfma_f32_16x16x32_bf16 v[104:107], a[32:35], v[72:75], v[104:107]\nds_read_b128 v[96:99], v67 offset:256\n"
    "v_mfma_f32_16x16x32_bf16 v[108:111], a[40:43], v[72:75], v[108:111]\ns_waitcnt lgkmcnt(5)\n"
    "v_mfma_f32_16x16x32_bf16 v[112:115], a[32:35], v[76:79], v[112:115]\nds_read_b128 v[100:103], v67 offset:16640\n"
    "v_mfma_f32_16x16x32_bf16 v[116:119], a[40:43], v[76:79], v[116:119]\ns_waitcnt lgkmcnt(5)\n"
    "v_mfma_f32_16x16x32_bf16 v[104:107], a[36:39], v[80:83], v[104:107]\nds_read_b128 v[72:75], v64 offset:512\n"
    "v_mfma_f32_16x16x32_bf16 v[108:111], a[44:47], v[80:83], v[108:111]\ns_waitcnt lgkmcnt(5)\n"
    "v_mfma_f32_16x16x32_bf16 v[112:115], a[36:39], v[84:87], v[112:115]\nds_read_b128 v[76:79], v64 offset:16896\n"
    "v_mfma_f32_16x16x32_bf16 v[116:119], a[44:47], v[84:87], v[116:119]\ns_waitcnt lgkmcnt(5)\n"
    "v_mfma_f32_16x16x32_bf16 v[104:107], a[48:51], v[88:91], v[104:107]\nds_read_b128 v[80:83], v65 offset:512\n"
    "v_mfma_f32_16x16x32_bf16 v[108:111], a[56:59], v[88:91], v[108:111]\ns_waitcnt lgkmcnt(5)\n"
    "v_mfma_f32_16x16x32_bf16 v[112:115], a[48:51], v[92:95], v[112:115]\nds_read_b128 v[84:87], v65 offset:16896\n"
    "v_mfma_f32_16x16x32_bf16 v[116:119], a[56:59], v[92:95], v[116:119]\ns_waitcnt lgkmcnt(5)\n"
    "v_mfma_f32_16x16x32_bf16 v[104:107], a[52:55], v[96:99], v[104:107]\nds_read_b128 v[88:91], v66 offset:512\n"
    "v_mfma_f32_16x16x32_bf16 v[108:111], a[60:63], v[96:99], v[108:111]\ns_waitcnt lgkmcnt(5)\n"
    "v_mfma_f32_16x16x32_bf16 v[112:115], a[52:55], v[100:103], v[112:115]\nds_read_b128 v[92:95], v66 offset:16896\n"
    "v_mfma_f32_16x16x32_bf16 v[116:119], a[60:63], v[100:103], v[116:119]\ns_waitcnt lgkmcnt(5)\n"
    "v_mfma_f32_16x16x32_bf16 v[104:107], a[64:67], v[72:75], v[104:107]\nds_read_b128 v[96:99], v67 offset:512\n"
    "v_mfma_f32_16x16x32_bf16 v[108:111], a[72:75], v[72:75], v[108:111]\ns_waitcnt lgkmcnt(5)\n"
    "v_mfma_f32_16x16x32_bf16 v[112:115], a[64:67], v[76:79], v[112:115]\nds_read_b128 v[100:103], v67 offset:16896\n"
    "v_mfma_f32_16x16x32_bf16 v[116:119], a[72:75], v[76:79], v[116:119]\ns_waitcnt lgkmcnt(5)\n"
    "v_mfma_f32_16x16x32_bf16 v[104:107], a[68:71], v[80:83], v[104:107]\nds_read_b128 v[72:75], v64 offset:768\n"
    "v_mfma_f32_16x16x32_bf16 v[108:111], a[76:79], v[80:83], v[108:111]\ns_waitcnt lgkmcnt(5)\n"
    "v_mfma_f32_16x16x32_bf16 v[112:115], a[68:71], v[84:87], v[112:115]\nds_read_b128 v[76:79], v64 offset:17152\n"
    "v_mfma_f32_16x16x32_bf16 v[116:119], a[76:79], v[84:87], v[116:119]\ns_waitcnt lgkmcnt(5)\n"
    "v_mfma_f32_16x16x32_bf16 v[104:107], a[80:83], v[88:91], v[104:107]\nds_read_b128 v[80:83], v65 offset:768\n"
    "v_mfma_f32_16x16x32_bf16 v[108:111], a[88:91], v[88:91], v[108:111]\ns_waitcnt lgkmcnt(5)\n"
    "v_mfma_f32_16x16x32_bf16 v[112:115], a[80:83], v[92:95], v[112:115]\nds_read_b128 v[84:87], v65 offset:17152\n"
    "v_mfma_f32_16x16x32_bf16 v[116:119], a[88:91], v[92:95], v[116:119]\ns_waitcnt lgkmcnt(5)\n"
    "v_mfma_f32_16x16x32_bf16 v[104:107], a[84:87], v[96:99], v[104:107]\nds_read_b128 v[88:91], v66 offset:768\n"
    "v_mfma_f32_16x16x32_bf16 v[108:111], a[92:95], v[96:99], v[108:111]\ns_waitcnt lgkmcnt(5)\n"
    "v_mfma_f32_16x16x32_bf16 v[112:115], a[84:87], v[100:103], v[112:115]\nds_read_b128 v[92:95], v66 offset:17152\n"
    "v_mfma_f32_16x16x32_bf16 v[116:119], a[92:95], v[100:103], v[116:119]\ns_waitcnt lgkmcnt(5)\n"
    "v_mfma_f32_16x16x32_bf16 v[104:107], a[96:99], v[72:75], v[104:107]\nds_read_b128 v[96:99], v67 offset:768\n"
    "v_mfma_f32_16x16x32_bf16 v[108:111], a[104:107], v[72:75], v[108:111]\ns_waitcnt lgkmcnt(5)\n"
    "v_mfma_f32_16x16x32_bf16 v[112:115], a[96:99], v[76:79], v[112:115]\nds_read_b128 v[100:103], v67 offset:17152\n"
    "v_mfma_f32_16x16x32_bf16 v[116:119], a[104:107], v[76:79], v[116:119]\ns_waitcnt lgkmcnt(5)\n"
    "v_mfma_f32_16x16x32_bf16 v[104:107], a[100:103], v[80:83], v[104:107]\nv_mfma_f32_16x16x32_bf16 v[108:111], a[108:111], v[80:83], v[108:111]\n"
    "s_waitcnt lgkmcnt(4)\nv_mfma_f32_16x16x32_bf16 v[112:115], a[100:103], v[84:87], v[112:115]\n"
    "v_mfma_f32_16x16x32_bf16 v[116:119], a[108:111], v[84:87], v[116:119]\ns_waitcnt lgkmcnt(3)\n"
    "v_mfma_f32_16x16x32_bf16 v[104:107], a[112:115], v[88:91], v[104:107]\nv_mfma_f32_16x16x32_bf16 v[108:111], a[120:123], v[88:91], v[108:111]\n"
    "s_waitcnt lgkmcnt(2)\nv_mfma_f32_16x16x32_bf16 v[112:115], a[112:115], v[92:95], v[112:115]\n"
    "v_mfma_f32_16x16x32_bf16 v[116:119], a[120:123], v[92:95], v[116:119]\ns_waitcnt lgkmcnt(1)\n"
    "v_mfma_f32_16x16x32_bf16 v[104:107], a[116:119], v[96:99], v[104:107]\nv_mfma_f32_16x16x32_bf16 v[108:111], a[124:127], v[96:99], v[108:111]\n"
    "s_waitcnt lgkmcnt(0)\nv_mfma_f32_16x16x32_bf16 v[112:115], a[116:119], v[100:103], v[112:115]\n"
    "v_mfma_f32_16x16x32_bf16 v[116:119], a[124:127], v[100:103], v[116:119]\ns_add_u32 s91, s91, 0x8000\ns_cmp_ge_u32 s91, %[lend]\n"
    "s_cselect_b32 s85, 0x18000, 0\ns_sub_u32 s91, s91, s85\nv_add_u32 v64, s91, v48\nv_add_u32 v65, s91, v49\nv_add_u32 v66, s91, v50\n"
    "v_add_u32 v67, s91, v51\nv_cvt_pk_bf16_f32 v120, v104, v108\nv_cvt_pk_bf16_f32 v121, v105, v109\nv_cvt_pk_bf16_f32 v122, v106, v110\n"
    "v_cvt_pk_bf16_f32 v123, v107, v111\nds_write_b128 v184, v[120:123] offset:0\nv_cvt_pk_bf16_f32 v124, v112, v116\nv_cvt_pk_bf16_f32 v125, v113, v117\n"
    "v_cvt_pk_bf16_f32 v126, v114, v118\nv_cvt_pk_bf16_f32 v127, v115, v119\nds_write_b128 v184, v[124:127] offset:4096\nv_xor_b32 v184, 0x2000, v184\n"
    "s_waitcnt vmcnt(8)\ns_barrier\nds_read_b128 v[72:75], v64 offset:0\nds_read_b128 v[76:79], v64 offset:16384\nds_read_b128 v[80:83], v65 offset:0\n"
    "ds_read_b128 v[84:87], v65 offset:16384\nds_read_b128 v[88:91], v66 offset:0\nds_read_b128 v[92:95], v66 offset:16384\ns_waitcnt vmcnt(0)\n.p2align 6\n"
    "1:\ns_and_b32 s85, s87, 7\ns_or_b32 s85, s85, %[wv]\ns_cmp_lg_u32 s85, 0\ns_cbranch_scc1 22f\ns_cmp_lt_u32 s87, 24\ns_cbranch_scc1 22f\n"
    "s_sub_u32 s85, s87, 21\ns_mov_b32 s88, 200000\n21:\nbuffer_load_dword v70, v58, %[rc], 0 offen sc1\ns_waitcnt vmcnt(0)\nv_cmp_gt_u32 vcc, s85, v70\n"
    "s_cbranch_vccz 22f\ns_sleep 8\ns_sub_u32 s88, s88, 1\ns_cmp_gt_i32 s88, 0\ns_cbranch_scc1 21b\n22:\ns_waitcnt vmcnt(4) lgkmcnt(0)\ns_barrier\n"
    "v_mfma_f32_16x16x32_bf16 v[104:107], a[0:3], v[72:75], 0\nds_read_b128 v[96:99], v67 offset:0\nv_cvt_pk_bf16_f32 v176, v160, v164\n"
    "v_mfma_f32_16x16x32_bf16 v[108:111], a[8:11], v[72:75], 0\nds_read_b128 v[128:131], v185 offset:0\nds_read_b128 v[132:135], v185 offset:4096\n"
    "v_mfma_f32_16x16x32_bf16 v[112:115], a[0:3], v[76:79], 0\nds_read_b128 v[100:103], v67 offset:16384\nv_cvt_pk_bf16_f32 v177, v161, v165\n"
    "v_mfma_f32_16x16x32_bf16 v[116:119], a[8:11], v[76:79], 0\nds_read_b128 v[136:139], v185 offset:1024\nds_read_b128 v[140:143], v185 offset:5120\n"
    "v_mfma_f32_16x16x32_bf16 v[104:107], a[4:7], v[80:83], v[104:107]\nds_read_b128 v[72:75], v64 offset:256\nv_cvt_pk_bf16_f32 v178, v162, v166\n"
    "v_mfma_f32_16x16x32_bf16 v[108:111], a[12:15], v[80:83], v[108:111]\nds_read_b128 v[144:147], v185 offset:2048\n"
    "ds_read_b128 v[148:151], v185 offset:6144\nv_mfma_f32_16x16x32_bf16 v[112:115], a[4:7], v[84:87], v[112:115]\nds_read_b128 v[76:79], v64 offset:16640\n"
    "v_cvt_pk_bf16_f32 v179, v163, v167\nv_mfma_f32_16x16x32_bf16 v[116:119], a[12:15], v[84:87], v[116:119]\nds_read_b128 v[152:155], v185 offset:3072\n"
    "v_mfma_f32_16x16x32_bf16 v[104:107], a[16:19], v[88:91], v[104:107]\nds_read_b128 v[80:83], v65 offset:256\n"
    "buffer_store_dwordx4 v[176:179], v53, %[rc], s84 offen sc1\nv_mfma_f32_16x16x32_bf16 v[108:111], a[24:27], v[88:91], v[108:111]\n"
    "ds_read_b128 v[156:159], v185 offset:7168\ns_add_u32 s84, s84, 0x10000\nv_mfma_f32_16x16x32_bf16 v[112:115], a[16:19], v[92:95], v[112:115]\n"
    "ds_read_b128 v[84:87], v65 offset:16640\nv_cvt_pk_bf16_f32 v180, v168, v172\nv_mfma_f32_16x16x32_bf16 v[116:119], a[24:27], v[92:95], v[116:119]\n"
    "v_xor_b32 v185, 0x2000, v185\nv_cvt_pk_bf16_f32 v181, v169, v173\ns_waitcnt lgkmcnt(13)\n"
    "v_mfma_f32_16x16x32_bf16 v[104:107], a[20:23], v[96:99], v[104:107]\nds_read_b128 v[88:91], v66 offset:256\nv_cvt_pk_bf16_f32 v182, v170, v174\n"
    "v_mfma_f32_16x16x32_bf16 v[108:111], a[28:31], v[96:99], v[108:111]\nv_cvt_pk_bf16_f32 v183, v171, v175\ns_waitcnt lgkmcnt(11)\n"
    "v_mfma_f32_16x16x32_bf16 v[112:115], a[20:23], v[100:103], v[112:115]\nds_read_b128 v[92:95], v66 offset:16640\n"
    "v_mfma_f32_16x16x32_bf16 v[116:119], a[28:31], v[100:103], v[116:119]\nbuffer_store_dwordx4 v[180:183], v53, %[rc], s84 offen sc1\n"
    "s_waitcnt lgkmcnt(3)\nv_mfma_f32_16x16x32_bf16 v[160:163], v[128:131], a[128:131], 0\ns_mov_b32 m0, s90\nv_xor_b32 v68, 0, v52\n"
    "v_mfma_f32_16x16x32_bf16 v[164:167], v[132:135], a[128:131], 0\nbuffer_load_dwordx4 v68, %[rc], s86 offen sc1 lds\ns_add_u32 s86, s86, 1024\n"
    "v_mfma_f32_16x16x32_bf16 v[160:163], v[136:139], a[132:135], v[160:163]\ns_add_u32 m0, m0, 1024\nv_xor_b32 v69, 16, v52\n"
    "v_mfma_f32_16x16x32_bf16 v[164:167], v[140:143], a[132:135], v[164:167]\nbuffer_load_dwordx4 v69, %[rc], s86 offen sc1 lds\ns_add_u32 s86, s86, 1024\n"
    "v_mfma_f32_16x16x32_bf16 v[160:163], v[144:147], a[136:139], v[160:163]\nv_mfma_f32_16x16x32_bf16 v[164:167], v[148:151], a[136:139], v[164:167]\n"
    "v_mfma_f32_16x16x32_bf16 v[160:163], v[152:155], a[140:143], v[160:163]\ns_and_b32 s88, s87, 31\ns_bitcmp1_b32 %[par], s88\n"
    "v_mfma_f32_16x16x32_bf16 v[164:167], v[156:159], a[140:143], v[164:167]\ns_cselect_b32 s85, 0x2000, 0\ns_lshl_b32 s84, s88, 21\n"
    "v_mfma_f32_16x16x32_bf16 v[104:107], a[32:35], v[72:75], v[104:107]\nds_read_b128 v[96:99], v67 offset:256\n"
    "v_mfma_f32_16x16x32_bf16 v[108:111], a[40:43], v[72:75], v[108:111]\ns_add_u32 s84, s84, s85\n"
    "v_mfma_f32_16x16x32_bf16 v[112:115], a[32:35], v[76:79], v[112:115]\nds_read_b128 v[100:103], v67 offset:16640\n"
    "v_mfma_f32_16x16x32_bf16 v[116:119], a[40:43], v[76:79], v[116:119]\nv_mfma_f32_16x16x32_bf16 v[104:107], a[36:39], v[80:83], v[104:107]\n"
    "ds_read_b128 v[72:75], v64 offset:512\nv_mfma_f32_16x16x32_bf16 v[108:111], a[44:47], v[80:83], v[108:111]\nv_cvt_pk_bf16_f32 v176, v160, v164\n"
    "s_waitcnt lgkmcnt(5)\nv_mfma_f32_16x16x32_bf16 v[112:115], a[36:39], v[84:87], v[112:115]\nds_read_b128 v[76:79], v64 offset:16896\n"
    "v_cvt_pk_bf16_f32 v177, v161, v165\nv_mfma_f32_16x16x32_bf16 v[116:119], a[44:47], v[84:87], v[116:119]\nv_cvt_pk_bf16_f32 v178, v162, v166\n"
    "v_mfma_f32_16x16x32_bf16 v[168:171], v[128:131], a[144:147], 0\nv_cvt_pk_bf16_f32 v179, v163, v167\n"
    "v_mfma_f32_16x16x32_bf16 v[172:175], v[132:135], a[144:147], 0\nbuffer_store_dwordx4 v[176:179], v53, %[rc], s84 offen sc1\n"
    "v_mfma_f32_16x16x32_bf16 v[168:171], v[136:139], a[148:151], v[168:171]\ns_add_u32 s84, s84, 0x10000\n"
    "v_mfma_f32_16x16x32_bf16 v[172:175], v[140:143], a[148:151], v[172:175]\ns_add_u32 m0, m0, 1024\nv_xor_b32 v68, 32, v52\n"
    "v_mfma_f32_16x16x32_bf16 v[168:171], v[144:147], a[152:155], v[168:171]\nbuffer_load_dwordx4 v68, %[rc], s86 offen sc1 lds\ns_add_u32 s86, s86, 1024\n"
    "v_mfma_f32_16x16x32_bf16 v[172:175], v[148:151], a[152:155], v[172:175]\ns_add_u32 m0, m0, 1024\nv_xor_b32 v69, 48, v52\n"
    "v_mfma_f32_16x16x32_bf16 v[168:171], v[152:155], a[156:159], v[168:171]\nbuffer_load_dwordx4 v69, %[rc], s86 offen sc1 lds\ns_add_u32 s86, s86, 1024\n"
    "v_mfma_f32_16x16x32_bf16 v[172:175], v[156:159], a[156:159], v[172:175]\ns_waitcnt lgkmcnt(5)\n"
    "v_mfma_f32_16x16x32_bf16 v[104:107], a[48:51], v[88:91], v[104:107]\nds_read_b128 v[80:83], v65 offset:512\n"
    "v_mfma_f32_16x16x32_bf16 v[108:111], a[56:59], v[88:91], v[108:111]\ns_waitcnt lgkmcnt(5)\n"
    "v_mfma_f32_16x16x32_bf16 v[112:115], a[48:51], v[92:95], v[112:115]\nds_read_b128 v[84:87], v65 offset:16896\n"
    "v_mfma_f32_16x16x32_bf16 v[116:119], a[56:59], v[92:95], v[116:119]\ns_waitcnt lgkmcnt(5)\n"
    "v_mfma_f32_16x16x32_bf16 v[104:107], a[52:55], v[96:99], v[104:107]\nds_read_b128 v[88:91], v66 offset:512\n"
    "v_mfma_f32_16x16x32_bf16 v[108:111], a[60:63], v[96:99], v[108:111]\nv_cvt_pk_bf16_f32 v180, v168, v172\ns_waitcnt lgkmcnt(5)\n"
    "v_mfma_f32_16x16x32_bf16 v[112:115], a[52:55], v[100:103], v[112:115]\nds_read_b128 v[92:95], v66 offset:16896\nv_cvt_pk_bf16_f32 v181, v169, v173\n"
    "v_mfma_f32_16x16x32_bf16 v[116:119], a[60:63], v[100:103], v[116:119]\nv_cvt_pk_bf16_f32 v182, v170, v174\n"
    "v_mfma_f32_16x16x32_bf16 v[160:163], v[128:131], a[160:163], 0\nv_cvt_pk_bf16_f32 v183, v171, v175\n"
    "v_mfma_f32_16x16x32_bf16 v[164:167], v[132:135], a[160:163], 0\nbuffer_store_dwordx4 v[180:183], v53, %[rc], s84 offen sc1\n"
    "v_mfma_f32_16x16x32_bf16 v[160:163], v[136:139], a[164:167], v[160:163]\ns_add_u32 s84, s84, 0x10000\n"
    "v_mfma_f32_16x16x32_bf16 v[164:167], v[140:143], a[164:167], v[164:167]\ns_add_u32 m0, m0, 1024\nv_xor_b32 v68, 64, v52\n"
    "v_mfma_f32_16x16x32_bf16 v[160:163], v[144:147], a[168:171], v[160:163]\nbuffer_load_dwordx4 v68, %[rc], s86 offen sc1 lds\ns_add_u32 s86, s86, 1024\n"
    "v_mfma_f32_16x16x32_bf16 v[164:167], v[148:151], a[168:171], v[164:167]\ns_add_u32 m0, m0, 1024\nv_xor_b32 v69, 80, v52\n"
    "v_mfma_f32_16x16x32_bf16 v[160:163], v[152:155], a[172:175], v[160:163]\nbuffer_load_dwordx4 v69, %[rc], s86 offen sc1 lds\ns_add_u32 s86, s86, 1024\n"
    "v_mfma_f32_16x16x32_bf16 v[164:167], v[156:159], a[172:175], v[164:167]\ns_waitcnt lgkmcnt(5)\n"
    "v_mfma_f32_16x16x32_bf16 v[104:107], a[64:67], v[72:75], v[104:107]\nds_read_b128 v[96:99], v67 offset:512\n"
    "v_mfma_f32_16x16x32_bf16 v[108:111], a[72:75], v[72:75], v[108:111]\nv_mov_b32 v70, s87\ns_cmp_gt_u32 s87, 3\ns_waitcnt lgkmcnt(5)\n"
    "v_mfma_f32_16x16x32_bf16 v[112:115], a[64:67], v[76:79], v[112:115]\nds_read_b128 v[100:103], v67 offset:16896\n"
    "v_mfma_f32_16x16x32_bf16 v[116:119], a[72:75], v[76:79], v[116:119]\ns_cbranch_scc1 33f\ns_waitcnt vmcnt(8)\ns_barrier\n"
    "buffer_store_dword v70, v59, %[rc], 0 offen sc1\n33:\ns_waitcnt lgkmcnt(5)\nv_mfma_f32_16x16x32_bf16 v[104:107], a[68:71], v[80:83], v[104:107]\n"
    "ds_read_b128 v[72:75], v64 offset:768\nv_mfma_f32_16x16x32_bf16 v[108:111], a[76:79], v[80:83], v[108:111]\nv_cvt_pk_bf16_f32 v176, v160, v164\n"
    "s_waitcnt lgkmcnt(5)\nv_mfma_f32_16x16x32_bf16 v[112:115], a[68:71], v[84:87], v[112:115]\nds_read_b128 v[76:79], v64 offset:17152\n"
    "v_cvt_pk_bf16_f32 v177, v161, v165\nv_mfma_f32_16x16x32_bf16 v[116:119], a[76:79], v[84:87], v[116:119]\nv_cvt_pk_bf16_f32 v178, v162, v166\n"
    "v_mfma_f32_16x16x32_bf16 v[168:171], v[128:131], a[176:179], 0\nv_cvt_pk_bf16_f32 v179, v163, v167\n"
    "v_mfma_f32_16x16x32_bf16 v[172:175], v[132:135], a[176:179], 0\nbuffer_store_dwordx4 v[176:179], v53, %[rc], s84 offen sc1\n"
    "v_mfma_f32_16x16x32_bf16 v[168:171], v[136:139], a[180:183], v[168:171]\ns_add_u32 s84, s84, 0x10000\n"
    "v_mfma_f32_16x16x32_bf16 v[172:175], v[140:143], a[180:183], v[172:175]\ns_add_u32 m0, m0, 1024\nv_xor_b32 v68, 96, v52\n"
    "v_mfma_f32_16x16x32_bf16 v[168:171], v[144:147], a[184:187], v[168:171]\nbuffer_load_dwordx4 v68, %[rc], s86 offen sc1 lds\ns_add_u32 s86, s86, 1024\n"
    "v_mfma_f32_16x16x32_bf16 v[172:175], v[148:151], a[184:187], v[172:175]\ns_add_u32 m0, m0, 1024\nv_xor_b32 v69, 112, v52\n"
    "v_mfma_f32_16x16x32_bf16 v[168:171], v[152:155], a[188:191], v[168:171]\nbuffer_load_dwordx4 v69, %[rc], s86 offen sc1 lds\ns_add_u32 s86, s86, 0x6400\n"
    "v_mfma_f32_16x16x32_bf16 v[172:175], v[156:159], a[188:191], v[172:175]\ns_waitcnt lgkmcnt(5)\n"
    "v_mfma_f32_16x16x32_bf16 v[104:107], a[80:83], v[88:91], v[104:107]\nds_read_b128 v[80:83], v65 offset:768\n"
    "v_mfma_f32_16x16x32_bf16 v[108:111], a[88:91], v[88:91], v[108:111]\ns_add_u32 s91, s91, 0x8000\ns_cmp_ge_u32 s91, %[lend]\ns_waitcnt lgkmcnt(5)\n"
    "v_mfma_f32_16x16x32_bf16 v[112:115], a[80:83], v[92:95], v[112:115]\nds_read_b128 v[84:87], v65 offset:17152\n"
    "v_mfma_f32_16x16x32_bf16 v[116:119], a[88:91], v[92:95], v[116:119]\ns_cselect_b32 s85, 0x18000, 0\ns_sub_u32 s91, s91, s85\ns_waitcnt lgkmcnt(5)\n"
    "v_mfma_f32_16x16x32_bf16 v[104:107], a[84:87], v[96:99], v[104:107]\nds_read_b128 v[88:91], v66 offset:768\n"
    "v_mfma_f32_16x16x32_bf16 v[108:111], a[92:95], v[96:99], v[108:111]\nv_cvt_pk_bf16_f32 v180, v168, v172\ns_waitcnt lgkmcnt(5)\n"
    "v_mfma_f32_16x16x32_bf16 v[112:115], a[84:87], v[100:103], v[112:115]\nds_read_b128 v[92:95], v66 offset:17152\nv_cvt_pk_bf16_f32 v181, v169, v173\n"
    "v_mfma_f32_16x16x32_bf16 v[116:119], a[92:95], v[100:103], v[116:119]\nv_cvt_pk_bf16_f32 v182, v170, v174\n"
    "v_mfma_f32_16x16x32_bf16 v[160:163], v[128:131], a[192:195], 0\nv_cvt_pk_bf16_f32 v183, v171, v175\n"
    "v_mfma_f32_16x16x32_bf16 v[164:167], v[132:135], a[192:195], 0\nbuffer_store_dwordx4 v[180:183], v53, %[rc], s84 offen sc1\n"
    "v_mfma_f32_16x16x32_bf16 v[160:163], v[136:139], a[196:199], v[160:163]\ns_add_u32 s84, s84, 0x10000\n"
    "v_mfma_f32_16x16x32_bf16 v[164:167], v[140:143], a[196:199], v[164:167]\ns_min_u32 s86, s86, %[sHmax]\ns_add_u32 s90, s90, 0x8000\n"
    "v_mfma_f32_16x16x32_bf16 v[160:163], v[144:147], a[200:203], v[160:163]\ns_cmp_ge_u32 s90, %[lend]\ns_cselect_b32 s85, 0x18000, 0\n"
    "v_mfma_f32_16x16x32_bf16 v[164:167], v[148:151], a[200:203], v[164:167]\ns_sub_u32 s90, s90, s85\nv_add_u32 v64, s91, v48\n"
    "v_mfma_f32_16x16x32_bf16 v[160:163], v[152:155], a[204:207], v[160:163]\nv_add_u32 v65, s91, v49\nv_add_u32 v66, s91, v50\n"
    "v_mfma_f32_16x16x32_bf16 v[164:167], v[156:159], a[204:207], v[164:167]\ns_waitcnt lgkmcnt(5)\n"
    "v_mfma_f32_16x16x32_bf16 v[104:107], a[96:99], v[72:75], v[104:107]\nds_read_b128 v[96:99], v67 offset:768\n"
    "v_mfma_f32_16x16x32_bf16 v[108:111], a[104:107], v[72:75], v[108:111]\ns_cmp_gt_u32 s87, 3\ns_cselect_b32 s89, 0, 0x80000000\ns_waitcnt lgkmcnt(5)\n"
    "v_mfma_f32_16x16x32_bf16 v[112:115], a[96:99], v[76:79], v[112:115]\nds_read_b128 v[100:103], v67 offset:17152\n"
    "v_mfma_f32_16x16x32_bf16 v[116:119], a[104:107], v[76:79], v[116:119]\nv_or_b32 v71, s89, v54\ns_waitcnt lgkmcnt(5)\n"
    "v_mfma_f32_16x16x32_bf16 v[104:107], a[100:103], v[80:83], v[104:107]\nds_read_b128 v[72:75], v64 offset:0\n"
    "v_mfma_f32_16x16x32_bf16 v[108:111], a[108:111], v[80:83], v[108:111]\nv_cvt_pk_bf16_f32 v176, v160, v164\ns_waitcnt lgkmcnt(5)\n"
    "v_mfma_f32_16x16x32_bf16 v[112:115], a[100:103], v[84:87], v[112:115]\nds_read_b128 v[76:79], v64 offset:16384\nv_cvt_pk_bf16_f32 v177, v161, v165\n"
    "v_mfma_f32_16x16x32_bf16 v[116:119], a[108:111], v[84:87], v[116:119]\nv_cvt_pk_bf16_f32 v178, v162, v166\n"
    "v_mfma_f32_16x16x32_bf16 v[168:171], v[128:131], a[208:211], 0\nv_cvt_pk_bf16_f32 v179, v163, v167\n"
    "v_mfma_f32_16x16x32_bf16 v[172:175], v[132:135], a[208:211], 0\nbuffer_store_dwordx4 v[176:179], v53, %[rc], s84 offen sc1\n"
    "v_mfma_f32_16x16x32_bf16 v[168:171], v[136:139], a[212:215], v[168:171]\ns_add_u32 s84, s84, 0x10000\n"
    "v_mfma_f32_16x16x32_bf16 v[172:175], v[140:143], a[212:215], v[172:175]\nv_add_u32 v67, s91, v51\n"
    "v_mfma_f32_16x16x32_bf16 v[168:171], v[144:147], a[216:219], v[168:171]\nv_mfma_f32_16x16x32_bf16 v[172:175], v[148:151], a[216:219], v[172:175]\n"
    "s_waitcnt vmcnt(13)\nv_mfma_f32_16x16x32_bf16 v[168:171], v[152:155], a[220:223], v[168:171]\nbuffer_store_dword v70, v71, %[rc], 0 offen sc1\n"
    "v_mfma_f32_16x16x32_bf16 v[172:175], v[156:159], a[220:223], v[172:175]\ns_waitcnt lgkmcnt(5)\n"
    "v_mfma_f32_16x16x32_bf16 v[104:107], a[112:115], v[88:91], v[104:107]\nds_read_b128 v[80:83], v65 offset:0\n"
    "v_mfma_f32_16x16x32_bf16 v[108:111], a[120:123], v[88:91], v[108:111]\ns_waitcnt lgkmcnt(5)\n"
    "v_mfma_f32_16x16x32_bf16 v[112:115], a[112:115], v[92:95], v[112:115]\nds_read_b128 v[84:87], v65 offset:16384\n"
    "v_mfma_f32_16x16x32_bf16 v[116:119], a[120:123], v[92:95], v[116:119]\ns_waitcnt lgkmcnt(5)\n"
    "v_mfma_f32_16x16x32_bf16 v[104:107], a[116:119], v[96:99], v[104:107]\nds_read_b128 v[88:91], v66 offset:0\n"
    "v_mfma_f32_16x16x32_bf16 v[108:111], a[124:127], v[96:99], v[108:111]\nv_cvt_pk_bf16_f32 v180, v168, v172\ns_waitcnt lgkmcnt(5)\n"
    "v_mfma_f32_16x16x32_bf16 v[112:115], a[116:119], v[100:103], v[112:115]\nds_read_b128 v[92:95], v66 offset:16384\nv_cvt_pk_bf16_f32 v181, v169, v173\n"
    "v_mfma_f32_16x16x32_bf16 v[116:119], a[124:127], v[100:103], v[116:119]\nv_cvt_pk_bf16_f32 v182, v170, v174\n"
    "v_mfma_f32_16x16x32_bf16 v[160:163], v[128:131], a[224:227], 0\nv_cvt_pk_bf16_f32 v183, v171, v175\n"
    "v_mfma_f32_16x16x32_bf16 v[164:167], v[132:135], a[224:227], 0\nbuffer_store_dwordx4 v[180:183], v53, %[rc], s84 offen sc1\n"
    "v_mfma_f32_16x16x32_bf16 v[160:163], v[136:139], a[228:231], v[160:163]\ns_add_u32 s84, s84, 0x10000\n"
    "v_mfma_f32_16x16x32_bf16 v[164:167], v[140:143], a[228:231], v[164:167]\nv_cvt_pk_bf16_f32 v120, v104, v108\nv_cvt_pk_bf16_f32 v121, v105, v109\n"
    "v_mfma_f32_16x16x32_bf16 v[160:163], v[144:147], a[232:235], v[160:163]\nv_cvt_pk_bf16_f32 v122, v106, v110\nv_cvt_pk_bf16_f32 v123, v107, v111\n"
    "v_mfma_f32_16x16x32_bf16 v[164:167], v[148:151], a[232:235], v[164:167]\nds_write_b128 v184, v[120:123] offset:0\n"
    "v_mfma_f32_16x16x32_bf16 v[160:163], v[152:155], a[236:239], v[160:163]\nv_cvt_pk_bf16_f32 v124, v112, v116\nv_cvt_pk_bf16_f32 v125, v113, v117\n"
    "v_mfma_f32_16x16x32_bf16 v[164:167], v[156:159], a[236:239], v[164:167]\nv_cvt_pk_bf16_f32 v126, v114, v118\nv_cvt_pk_bf16_f32 v127, v115, v119\n"
    "v_mfma_f32_16x16x32_bf16 v[168:171], v[128:131], a[240:243], 0\nds_write_b128 v184, v[124:127] offset:4096\n"
    "v_mfma_f32_16x16x32_bf16 v[172:175], v[132:135], a[240:243], 0\nv_xor_b32 v184, 0x2000, v184\n"
    "v_mfma_f32_16x16x32_bf16 v[168:171], v[136:139], a[244:247], v[168:171]\nv_mfma_f32_16x16x32_bf16 v[172:175], v[140:143], a[244:247], v[172:175]\n"
    "v_mfma_f32_16x16x32_bf16 v[168:171], v[144:147], a[248:251], v[168:171]\nv_mfma_f32_16x16x32_bf16 v[172:175], v[148:151], a[248:251], v[172:175]\n"
    "s_add_u32 s87, s87, 1\nv_mfma_f32_16x16x32_bf16 v[168:171], v[152:155], a[252:255], v[168:171]\ns_cmp_lt_u32 s87, 64\n"
    "v_mfma_f32_16x16x32_bf16 v[172:175], v[156:159], a[252:255], v[172:175]\ns_cbranch_scc1 1b\nv_cvt_pk_bf16_f32 v176, v160, v164\n"
    "v_cvt_pk_bf16_f32 v177, v161, v165\nv_cvt_pk_bf16_f32 v178, v162, v166\nv_cvt_pk_bf16_f32 v179, v163, v167\n"
    "buffer_store_dwordx4 v[176:179], v53, %[rc], s84 offen sc1\ns_add_u32 s84, s84, 0x10000\ns_nop 1\nv_cvt_pk_bf16_f32 v180, v168, v172\n"
    "v_cvt_pk_bf16_f32 v181, v169, v173\nv_cvt_pk_bf16_f32 v182, v170, v174\nv_cvt_pk_bf16_f32 v183, v171, v175\n"
    "buffer_store_dwordx4 v[180:183], v53, %[rc], s84 offen sc1\ns_add_u32 s84, s84, 0x10000\ns_waitcnt vmcnt(0) lgkmcnt(0)\n"
    :
    : [rc] "s"(rc), [lr] "s"(lr), [lend] "s"(lend), [sH] "s"(sH), [sHmax] "s"(sHmax), [ld] "s"(ld), [par] "s"(par), [wv] "s"(wv), "{v[48:63]}"(cv)
    : "memory", "vcc", "scc", CLS, CLV_64_185);
}
DI void x3_step(rsrc_t rc, unsigned lr, unsigned ls, unsigned sO, unsigned sL, unsigned nd, unsigned pv, unsigned sH, unsigned ld, unsigned sS, unsigned ls2, const v16u& cv, const v16u& cs, const v16u& bi, unsigned& pre) {
  asm volatile(
    "s_nop 4\nv_add_u32 v64, %[lr], v48\nv_add_u32 v65, %[lr], v49\nv_add_u32 v66, %[lr], v50\nv_add_u32 v67, %[lr], v51\n"
    "v_add_u32 v156, %[ls], v56\ns_mov_b32 s84, 0xffff0000\ns_add_u32 s89, %[sO], 0x20000\ns_waitcnt vmcnt(2)\ns_barrier\n"
    "ds_read_b128 v[72:75], v64 offset:0\nds_read_b128 v[76:79], v65 offset:0\nds_read_b128 v[80:83], v66 offset:0\nds_read_b128 v[84:87], v67 offset:0\n"
    "ds_read_b128 v[88:91], v64 offset:256\nds_read_b128 v[92:95], v65 offset:256\ns_waitcnt lgkmcnt(5)\n"
    "v_mfma_f32_16x16x32_bf16 v[104:107], a[0:3], v[72:75], 0\nds_read_b128 v[96:99], v66 offset:256\n"
    "v_mfma_f32_16x16x32_bf16 v[108:111], a[4:7], v[72:75], 0\nbuffer_load_dword v164, v54, %[rc], %[sL] offen sc1\n"
    "v_mfma_f32_16x16x32_bf16 v[112:115], a[8:11], v[72:75], 0\nds_read2_b64 v[144:147], v156 offset1:32\n"
    "v_mfma_f32_16x16x32_bf16 v[116:119], a[12:15], v[72:75], 0\nds_read2_b64 v[148:151], v156 offset0:64 offset1:96\ns_waitcnt lgkmcnt(7)\n"
    "v_mfma_f32_16x16x32_bf16 v[104:107], a[16:19], v[76:79], v[104:107]\nds_read_b128 v[100:103], v67 offset:256\n"
    "v_mfma_f32_16x16x32_bf16 v[108:111], a[20:23], v[76:79], v[108:111]\nv_mfma_f32_16x16x32_bf16 v[112:115], a[24:27], v[76:79], v[112:115]\n"
    "v_mfma_f32_16x16x32_bf16 v[116:119], a[28:31], v[76:79], v[116:119]\ns_waitcnt lgkmcnt(7)\n"
    "v_mfma_f32_16x16x32_bf16 v[104:107], a[32:35], v[80:83], v[104:107]\nds_read_b128 v[72:75], v64 offset:512\n"
    "v_mfma_f32_16x16x32_bf16 v[108:111], a[36:39], v[80:83], v[108:111]\ns_waitcnt lgkmcnt(2)\nv_add_f32 v152, v144, v146\n"
    "v_mfma_f32_16x16x32_bf16 v[112:115], a[40:43], v[80:83], v[112:115]\nv_add_f32 v153, v148, v150\nv_add_f32 v152, v152, v153\n"
    "v_mfma_f32_16x16x32_bf16 v[116:119], a[44:47], v[80:83], v[116:119]\nv_mov_b32 v153, v152\nv_add_f32 v145, v145, v147\n"
    "v_mfma_f32_16x16x32_bf16 v[104:107], a[48:51], v[84:87], v[104:107]\nds_read_b128 v[76:79], v65 offset:512\n"
    "v_mfma_f32_16x16x32_bf16 v[108:111], a[52:55], v[84:87], v[108:111]\nv_add_f32 v149, v149, v151\nv_permlane16_swap_b32 v152, v153\n"
    "v_mfma_f32_16x16x32_bf16 v[112:115], a[56:59], v[84:87], v[112:115]\nv_add_f32 v152, v152, v153\nv_mul_f32 v152, 0x3e000000, v152\n"
    "v_mfma_f32_16x16x32_bf16 v[116:119], a[60:63], v[84:87], v[116:119]\nv_add_f32 v145, v145, v149\nv_sub_f32 v144, v144, v152\n"
    "v_mfma_f32_16x16x32_bf16 v[104:107], a[64:67], v[88:91], v[104:107]\nds_read_b128 v[80:83], v66 offset:512\n"
    "v_mfma_f32_16x16x32_bf16 v[108:111], a[68:71], v[88:91], v[108:111]\nv_sub_f32 v146, v146, v152\nv_sub_f32 v148, v148, v152\n"
    "v_mfma_f32_16x16x32_bf16 v[112:115], a[72:75], v[88:91], v[112:115]\nv_sub_f32 v150, v150, v152\nv_mul_f32 v144, v144, v144\n"
    "v_mfma_f32_16x16x32_bf16 v[116:119], a[76:79], v[88:91], v[116:119]\nv_fmac_f32 v144, v146, v146\nv_fmac_f32 v144, v148, v148\n"
    "v_mfma_f32_16x16x32_bf16 v[104:107], a[80:83], v[92:95], v[104:107]\nds_read_b128 v[84:87], v67 offset:512\n"
    "v_mfma_f32_16x16x32_bf16 v[108:111], a[84:87], v[92:95], v[108:111]\nv_fmac_f32 v144, v150, v150\nv_fmac_f32 v145, 0x42800000, v144\n"
    "v_mfma_f32_16x16x32_bf16 v[112:115], a[88:91], v[92:95], v[112:115]\nv_mov_b32 v153, v145\nv_cvt_pk_bf16_f32 v152, v152, v152\n"
    "v_mfma_f32_16x16x32_bf16 v[116:119], a[92:95], v[92:95], v[116:119]\nv_and_b32 v152, s84, v152\nv_permlane16_swap_b32 v145, v153\n"
    "v_mfma_f32_16x16x32_bf16 v[104:107], a[96:99], v[96:99], v[104:107]\nds_read_b128 v[88:91], v64 offset:768\n"
    "v_mfma_f32_16x16x32_bf16 v[108:111], a[100:103], v[96:99], v[108:111]\nv_add_f32 v153, v145, v153\nv_mul_f32 v153, 0x3b000000, v153\n"
    "v_mfma_f32_16x16x32_bf16 v[112:115], a[104:107], v[96:99], v[112:115]\nv_cvt_pk_bf16_f32 v153, v153, v153\nv_and_b32 v153, s84, v153\n"
    "v_mfma_f32_16x16x32_bf16 v[116:119], a[108:111], v[96:99], v[116:119]\nv_add_f32 v153, 0x3727c5ac, v153\nv_cvt_pk_bf16_f32 v153, v153, v153\n"
    "s_waitcnt lgkmcnt(5)\nv_mfma_f32_16x16x32_bf16 v[104:107], a[112:115], v[100:103], v[104:107]\nds_read_b128 v[92:95], v65 offset:768\n"
    "v_mfma_f32_16x16x32_bf16 v[108:111], a[116:119], v[100:103], v[108:111]\nv_and_b32 v153, s84, v153\nv_sqrt_f32 v153, v153\n"
    "v_mfma_f32_16x16x32_bf16 v[112:115], a[120:123], v[100:103], v[112:115]\ns_nop 0\nv_cvt_pk_bf16_f32 v153, v153, v153\n"
    "v_mfma_f32_16x16x32_bf16 v[116:119], a[124:127], v[100:103], v[116:119]\nv_and_b32 v153, s84, v153\nv_rcp_f32 v160, v153\ns_waitcnt lgkmcnt(5)\n"
    "v_mfma_f32_16x16x32_bf16 v[104:107], a[128:131], v[72:75], v[104:107]\nds_read_b128 v[96:99], v66 offset:768\n"
    "v_mfma_f32_16x16x32_bf16 v[108:111], a[132:135], v[72:75], v[108:111]\ns_nop 0\nv_mul_f32 v162, -v152, v160\n"
    "v_mfma_f32_16x16x32_bf16 v[112:115], a[136:139], v[72:75], v[112:115]\nv_mov_b32 v161, v160\nv_mov_b32 v163, v162\n"
    "v_mfma_f32_16x16x32_bf16 v[116:119], a[140:143], v[72:75], v[116:119]\ns_nop 0\nv_permlane32_swap_b32 v160, v161\ns_waitcnt lgkmcnt(5)\n"
    "v_mfma_f32_16x16x32_bf16 v[104:107], a[144:147], v[76:79], v[104:107]\nds_read_b128 v[100:103], v67 offset:768\n"
    "v_mfma_f32_16x16x32_bf16 v[108:111], a[148:151], v[76:79], v[108:111]\nv_permlane32_swap_b32 v162, v163\n"
    "v_mfma_f32_16x16x32_bf16 v[112:115], a[152:155], v[76:79], v[112:115]\nv_mfma_f32_16x16x32_bf16 v[116:119], a[156:159], v[76:79], v[116:119]\n"
    "s_waitcnt lgkmcnt(5)\nv_mfma_f32_16x16x32_bf16 v[104:107], a[160:163], v[80:83], v[104:107]\nds_read_b128 v[72:75], v64 offset:16384\n"
    "v_mfma_f32_16x16x32_bf16 v[108:111], a[164:167], v[80:83], v[108:111]\nv_mfma_f32_16x16x32_bf16 v[112:115], a[168:171], v[80:83], v[112:115]\n"
    "v_mfma_f32_16x16x32_bf16 v[116:119], a[172:175], v[80:83], v[116:119]\ns_waitcnt lgkmcnt(5)\n"
    "v_mfma_f32_16x16x32_bf16 v[104:107], a[176:179], v[84:87], v[104:107]\nds_read_b128 v[76:79], v65 offset:16384\n"
    "v_mfma_f32_16x16x32_bf16 v[108:111], a[180:183], v[84:87], v[108:111]\ns_waitcnt vmcnt(0)\nv_cmp_gt_u32 vcc, %[nd], v164\n"
    "v_mfma_f32_16x16x32_bf16 v[112:115], a[184:187], v[84:87], v[112:115]\nv_mov_b32 v164, %[pv]\nbuffer_store_dword v164, v55, %[rc], 0 offen sc1\n"
    "v_mfma_f32_16x16x32_bf16 v[116:119], a[188:191], v[84:87], v[116:119]\ns_cmp_eq_u64 vcc, 0\ns_cselect_b32 %[pre], 1, 0\ns_waitcnt lgkmcnt(5)\n"
    "v_mfma_f32_16x16x32_bf16 v[104:107], a[192:195], v[88:91], v[104:107]\nds_read_b128 v[80:83], v66 offset:16384\n"
    "v_mfma_f32_16x16x32_bf16 v[108:111], a[196:199], v[88:91], v[108:111]\ns_cselect_b32 s85, 0, 0x80000000\n"
    "v_mfma_f32_16x16x32_bf16 v[112:115], a[200:203], v[88:91], v[112:115]\nv_or_b32 v165, s85, v52\nv_or_b32 v166, s85, v57\n"
    "v_mfma_f32_16x16x32_bf16 v[116:119], a[204:207], v[88:91], v[116:119]\ns_mov_b32 m0, %[ld]\nv_xor_b32 v68, 0, v165\ns_waitcnt lgkmcnt(5)\n"
    "v_mfma_f32_16x16x32_bf16 v[104:107], a[208:211], v[92:95], v[104:107]\nds_read_b128 v[84:87], v67 offset:16384\ns_add_u32 s86, %[sH], 0\n"
    "buffer_load_dwordx4 v68, %[rc], s86 offen sc1 lds\nv_mfma_f32_16x16x32_bf16 v[108:111], a[212:215], v[92:95], v[108:111]\ns_add_u32 m0, m0, 1024\n"
    "v_xor_b32 v69, 16, v165\nv_mfma_f32_16x16x32_bf16 v[112:115], a[216:219], v[92:95], v[112:115]\ns_add_u32 s86, %[sH], 1024\n"
    "buffer_load_dwordx4 v69, %[rc], s86 offen sc1 lds\nv_mfma_f32_16x16x32_bf16 v[116:119], a[220:223], v[92:95], v[116:119]\ns_waitcnt lgkmcnt(5)\n"
    "v_mfma_f32_16x16x32_bf16 v[104:107], a[224:227], v[96:99], v[104:107]\nds_read_b128 v[88:91], v64 offset:16640\ns_add_u32 m0, m0, 1024\n"
    "v_xor_b32 v68, 32, v165\nv_mfma_f32_16x16x32_bf16 v[108:111], a[228:231], v[96:99], v[108:111]\ns_add_u32 s86, %[sH], 2048\n"
    "buffer_load_dwordx4 v68, %[rc], s86 offen sc1 lds\nv_mfma_f32_16x16x32_bf16 v[112:115], a[232:235], v[96:99], v[112:115]\ns_add_u32 m0, m0, 1024\n"
    "v_xor_b32 v69, 48, v165\nv_mfma_f32_16x16x32_bf16 v[116:119], a[236:239], v[96:99], v[116:119]\ns_add_u32 s86, %[sH], 3072\n"
    "buffer_load_dwordx4 v69, %[rc], s86 offen sc1 lds\ns_waitcnt lgkmcnt(5)\nv_mfma_f32_16x16x32_bf16 v[104:107], a[240:243], v[100:103], v[104:107]\n"
    "ds_read_b128 v[92:95], v65 offset:16640\nv_mfma_f32_16x16x32_bf16 v[108:111], a[244:247], v[100:103], v[108:111]\ns_add_u32 m0, m0, 1024\n"
    "v_xor_b32 v68, 64, v165\nv_mfma_f32_16x16x32_bf16 v[112:115], a[248:251], v[100:103], v[112:115]\ns_add_u32 s86, %[sH], 4096\n"
    "buffer_load_dwordx4 v68, %[rc], s86 offen sc1 lds\nv_mfma_f32_16x16x32_bf16 v[116:119], a[252:255], v[100:103], v[116:119]\ns_add_u32 m0, m0, 1024\n"
    "v_xor_b32 v69, 80, v165\ns_waitcnt lgkmcnt(5)\nv_mfma_f32_16x16x32_bf16 v[120:123], a[0:3], v[72:75], 0\nds_read_b128 v[96:99], v66 offset:16640\n"
    "s_add_u32 s86, %[sH], 5120\nbuffer_load_dwordx4 v69, %[rc], s86 offen sc1 lds\nv_mfma_f32_16x16x32_bf16 v[124:127], a[4:7], v[72:75], 0\n"
    "v_mfma_f32_16x16x32_bf16 v[128:131], a[8:11], v[72:75], 0\ns_add_u32 m0, m0, 1024\nv_xor_b32 v68, 96, v165\n"
    "v_mfma_f32_16x16x32_bf16 v[132:135], a[12:15], v[72:75], 0\ns_add_u32 s86, %[sH], 6144\nbuffer_load_dwordx4 v68, %[rc], s86 offen sc1 lds\n"
    "s_waitcnt lgkmcnt(5)\nv_mfma_f32_16x16x32_bf16 v[120:123], a[16:19], v[76:79], v[120:123]\nds_read_b128 v[100:103], v67 offset:16640\n"
    "s_add_u32 m0, m0, 1024\nv_xor_b32 v69, 112, v165\nv_mfma_f32_16x16x32_bf16 v[124:127], a[20:23], v[76:79], v[124:127]\ns_add_u32 s86, %[sH], 7168\n"
    "buffer_load_dwordx4 v69, %[rc], s86 offen sc1 lds\nv_mfma_f32_16x16x32_bf16 v[128:131], a[24:27], v[76:79], v[128:131]\ns_mov_b32 m0, %[ls2]\n"
    "v_mfma_f32_16x16x32_bf16 v[132:135], a[28:31], v[76:79], v[132:135]\nbuffer_load_dwordx4 v166, %[rc], %[sS] offen sc1 lds\ns_waitcnt lgkmcnt(5)\n"
    "v_mfma_f32_16x16x32_bf16 v[120:123], a[32:35], v[80:83], v[120:123]\nds_read_b128 v[72:75], v64 offset:16896\n"
    "v_mfma_f32_16x16x32_bf16 v[124:127], a[36:39], v[80:83], v[124:127]\nv_mfma_f32_16x16x32_bf16 v[128:131], a[40:43], v[80:83], v[128:131]\n"
    "v_fma_f32 v70, v162, v16, v32\nv_fma_f32 v104, v160, v104, v70\nv_mfma_f32_16x16x32_bf16 v[132:135], a[44:47], v[80:83], v[132:135]\n"
    "v_fma_f32 v70, v162, v17, v33\ns_waitcnt lgkmcnt(5)\nv_mfma_f32_16x16x32_bf16 v[120:123], a[48:51], v[84:87], v[120:123]\n"
    "ds_read_b128 v[76:79], v65 offset:16896\nv_fma_f32 v105, v160, v105, v70\nv_mfma_f32_16x16x32_bf16 v[124:127], a[52:55], v[84:87], v[124:127]\n"
    "v_fma_f32 v70, v162, v18, v34\nv_fma_f32 v106, v160, v106, v70\nv_mfma_f32_16x16x32_bf16 v[128:131], a[56:59], v[84:87], v[128:131]\n"
    "v_fma_f32 v70, v162, v19, v35\nv_fma_f32 v107, v160, v107, v70\nv_mfma_f32_16x16x32_bf16 v[132:135], a[60:63], v[84:87], v[132:135]\n"
    "v_fma_f32 v70, v162, v20, v36\ns_waitcnt lgkmcnt(5)\nv_mfma_f32_16x16x32_bf16 v[120:123], a[64:67], v[88:91], v[120:123]\n"
    "ds_read_b128 v[80:83], v66 offset:16896\nv_fma_f32 v108, v160, v108, v70\nv_mfma_f32_16x16x32_bf16 v[124:127], a[68:71], v[88:91], v[124:127]\n"
    "v_fma_f32 v70, v162, v21, v37\nv_fma_f32 v109, v160, v109, v70\nv_mfma_f32_16x16x32_bf16 v[128:131], a[72:75], v[88:91], v[128:131]\n"
    "v_fma_f32 v70, v162, v22, v38\nv_fma_f32 v110, v160, v110, v70\nv_mfma_f32_16x16x32_bf16 v[132:135], a[76:79], v[88:91], v[132:135]\n"
    "v_fma_f32 v70, v162, v23, v39\ns_waitcnt lgkmcnt(5)\nv_mfma_f32_16x16x32_bf16 v[120:123], a[80:83], v[92:95], v[120:123]\n"
    "ds_read_b128 v[84:87], v67 offset:16896\nv_fma_f32 v111, v160, v111, v70\nv_mfma_f32_16x16x32_bf16 v[124:127], a[84:87], v[92:95], v[124:127]\n"
    "v_fma_f32 v70, v162, v24, v40\nv_fma_f32 v112, v160, v112, v70\nv_mfma_f32_16x16x32_bf16 v[128:131], a[88:91], v[92:95], v[128:131]\n"
    "v_fma_f32 v70, v162, v25, v41\nv_fma_f32 v113, v160, v113, v70\nv_mfma_f32_16x16x32_bf16 v[132:135], a[92:95], v[92:95], v[132:135]\n"
    "v_fma_f32 v70, v162, v26, v42\ns_waitcnt lgkmcnt(5)\nv_mfma_f32_16x16x32_bf16 v[120:123], a[96:99], v[96:99], v[120:123]\n"
    "ds_read_b128 v[88:91], v64 offset:17152\nv_fma_f32 v114, v160, v114, v70\nv_mfma_f32_16x16x32_bf16 v[124:127], a[100:103], v[96:99], v[124:127]\n"
    "v_fma_f32 v70, v162, v27, v43\nv_fma_f32 v115, v160, v115, v70\nv_mfma_f32_16x16x32_bf16 v[128:131], a[104:107], v[96:99], v[128:131]\n"
    "v_fma_f32 v70, v162, v28, v44\nv_fma_f32 v116, v160, v116, v70\nv_mfma_f32_16x16x32_bf16 v[132:135], a[108:111], v[96:99], v[132:135]\n"
    "v_fma_f32 v70, v162, v29, v45\ns_waitcnt lgkmcnt(5)\nv_mfma_f32_16x16x32_bf16 v[120:123], a[112:115], v[100:103], v[120:123]\n"
    "ds_read_b128 v[92:95], v65 offset:17152\nv_fma_f32 v117, v160, v117, v70\nv_mfma_f32_16x16x32_bf16 v[124:127], a[116:119], v[100:103], v[124:127]\n"
    "v_fma_f32 v70, v162, v30, v46\nv_fma_f32 v118, v160, v118, v70\nv_mfma_f32_16x16x32_bf16 v[128:131], a[120:123], v[100:103], v[128:131]\n"
    "v_fma_f32 v70, v162, v31, v47\nv_fma_f32 v119, v160, v119, v70\nv_mfma_f32_16x16x32_bf16 v[132:135], a[124:127], v[100:103], v[132:135]\n"
    "v_cvt_pk_bf16_f32 v136, v104, v108\ns_waitcnt lgkmcnt(5)\nv_mfma_f32_16x16x32_bf16 v[120:123], a[128:131], v[72:75], v[120:123]\n"
    "ds_read_b128 v[96:99], v66 offset:17152\nv_cvt_pk_bf16_f32 v137, v105, v109\nv_mfma_f32_16x16x32_bf16 v[124:127], a[132:135], v[72:75], v[124:127]\n"
    "v_cvt_pk_bf16_f32 v138, v106, v110\nv_cvt_pk_bf16_f32 v139, v107, v111\nv_mfma_f32_16x16x32_bf16 v[128:131], a[136:139], v[72:75], v[128:131]\n"
    "v_cvt_pk_bf16_f32 v140, v112, v116\nv_cvt_pk_bf16_f32 v141, v113, v117\nv_mfma_f32_16x16x32_bf16 v[132:135], a[140:143], v[72:75], v[132:135]\n"
    "v_cvt_pk_bf16_f32 v142, v114, v118\ns_waitcnt lgkmcnt(5)\nv_mfma_f32_16x16x32_bf16 v[120:123], a[144:147], v[76:79], v[120:123]\n"
    "ds_read_b128 v[100:103], v67 offset:17152\nv_cvt_pk_bf16_f32 v143, v115, v119\nv_mfma_f32_16x16x32_bf16 v[124:127], a[148:151], v[76:79], v[124:127]\n"
    "v_lshlrev_b32 v104, 16, v136\nv_and_b32 v108, s84, v136\nv_mfma_f32_16x16x32_bf16 v[128:131], a[152:155], v[76:79], v[128:131]\n"
    "v_lshlrev_b32 v112, 16, v140\nv_and_b32 v116, s84, v140\nv_mfma_f32_16x16x32_bf16 v[132:135], a[156:159], v[76:79], v[132:135]\n"
    "v_lshlrev_b32 v105, 16, v137\ns_waitcnt lgkmcnt(5)\nv_mfma_f32_16x16x32_bf16 v[120:123], a[160:163], v[80:83], v[120:123]\nv_and_b32 v109, s84, v137\n"
    "v_lshlrev_b32 v113, 16, v141\nv_mfma_f32_16x16x32_bf16 v[124:127], a[164:167], v[80:83], v[124:127]\nv_and_b32 v117, s84, v141\n"
    "v_lshlrev_b32 v106, 16, v138\nv_mfma_f32_16x16x32_bf16 v[128:131], a[168:171], v[80:83], v[128:131]\nv_and_b32 v110, s84, v138\n"
    "v_lshlrev_b32 v114, 16, v142\nv_mfma_f32_16x16x32_bf16 v[132:135], a[172:175], v[80:83], v[132:135]\nv_and_b32 v118, s84, v142\ns_waitcnt lgkmcnt(4)\n"
    "v_mfma_f32_16x16x32_bf16 v[120:123], a[176:179], v[84:87], v[120:123]\nv_lshlrev_b32 v107, 16, v139\nv_and_b32 v111, s84, v139\n"
    "v_mfma_f32_16x16x32_bf16 v[124:127], a[180:183], v[84:87], v[124:127]\nv_lshlrev_b32 v115, 16, v143\nv_and_b32 v119, s84, v143\n"
    "v_mfma_f32_16x16x32_bf16 v[128:131], a[184:187], v[84:87], v[128:131]\nv_mul_f32 v104, v104, v112\nv_mul_f32 v105, v105, v113\n"
    "v_mfma_f32_16x16x32_bf16 v[132:135], a[188:191], v[84:87], v[132:135]\nv_mul_f32 v106, v106, v114\ns_waitcnt lgkmcnt(3)\n"
    "v_mfma_f32_16x16x32_bf16 v[120:123], a[192:195], v[88:91], v[120:123]\nv_mul_f32 v107, v107, v115\nv_mul_f32 v108, v108, v116\n"
    "v_mfma_f32_16x16x32_bf16 v[124:127], a[196:199], v[88:91], v[124:127]\nv_mul_f32 v109, v109, v117\nv_mul_f32 v110, v110, v118\n"
    "v_mfma_f32_16x16x32_bf16 v[128:131], a[200:203], v[88:91], v[128:131]\nv_mul_f32 v111, v111, v119\nv_cvt_pk_bf16_f32 v112, v104, v108\n"
    "v_mfma_f32_16x16x32_bf16 v[132:135], a[204:207], v[88:91], v[132:135]\nv_cvt_pk_bf16_f32 v113, v105, v109\ns_waitcnt lgkmcnt(2)\n"
    "v_mfma_f32_16x16x32_bf16 v[120:123], a[208:211], v[92:95], v[120:123]\nv_cvt_pk_bf16_f32 v114, v106, v110\nv_cvt_pk_bf16_f32 v115, v107, v111\n"
    "v_mfma_f32_16x16x32_bf16 v[124:127], a[212:215], v[92:95], v[124:127]\nbuffer_store_dwordx4 v[112:115], v53, %[rc], %[sO] offen sc1\n"
    "v_mfma_f32_16x16x32_bf16 v[128:131], a[216:219], v[92:95], v[128:131]\nv_mfma_f32_16x16x32_bf16 v[132:135], a[220:223], v[92:95], v[132:135]\n"
    "s_waitcnt lgkmcnt(1)\nv_mfma_f32_16x16x32_bf16 v[120:123], a[224:227], v[96:99], v[120:123]\n"
    "v_mfma_f32_16x16x32_bf16 v[124:127], a[228:231], v[96:99], v[124:127]\nv_mfma_f32_16x16x32_bf16 v[128:131], a[232:235], v[96:99], v[128:131]\n"
    "v_mfma_f32_16x16x32_bf16 v[132:135], a[236:239], v[96:99], v[132:135]\ns_waitcnt lgkmcnt(0)\n"
    "v_mfma_f32_16x16x32_bf16 v[120:123], a[240:243], v[100:103], v[120:123]\nv_mfma_f32_16x16x32_bf16 v[124:127], a[244:247], v[100:103], v[124:127]\n"
    "v_mfma_f32_16x16x32_bf16 v[128:131], a[248:251], v[100:103], v[128:131]\nv_mfma_f32_16x16x32_bf16 v[132:135], a[252:255], v[100:103], v[132:135]\n"
    "s_nop 3\nv_fma_f32 v70, v163, v16, v32\nv_fma_f32 v120, v161, v120, v70\nv_fma_f32 v70, v163, v17, v33\nv_fma_f32 v121, v161, v121, v70\n"
    "v_fma_f32 v70, v163, v18, v34\nv_fma_f32 v122, v161, v122, v70\nv_fma_f32 v70, v163, v19, v35\nv_fma_f32 v123, v161, v123, v70\n"
    "v_fma_f32 v70, v163, v20, v36\nv_fma_f32 v124, v161, v124, v70\nv_fma_f32 v70, v163, v21, v37\nv_fma_f32 v125, v161, v125, v70\n"
    "v_fma_f32 v70, v163, v22, v38\nv_fma_f32 v126, v161, v126, v70\nv_fma_f32 v70, v163, v23, v39\nv_fma_f32 v127, v161, v127, v70\n"
    "v_fma_f32 v70, v163, v24, v40\nv_fma_f32 v128, v161, v128, v70\nv_fma_f32 v70, v163, v25, v41\nv_fma_f32 v129, v161, v129, v70\n"
    "v_fma_f32 v70, v163, v26, v42\nv_fma_f32 v130, v161, v130, v70\nv_fma_f32 v70, v163, v27, v43\nv_fma_f32 v131, v161, v131, v70\n"
    "v_fma_f32 v70, v163, v28, v44\nv_fma_f32 v132, v161, v132, v70\nv_fma_f32 v70, v163, v29, v45\nv_fma_f32 v133, v161, v133, v70\n"
    "v_fma_f32 v70, v163, v30, v46\nv_fma_f32 v134, v161, v134, v70\nv_fma_f32 v70, v163, v31, v47\nv_fma_f32 v135, v161, v135, v70\n"
    "v_cvt_pk_bf16_f32 v136, v120, v124\nv_cvt_pk_bf16_f32 v137, v121, v125\nv_cvt_pk_bf16_f32 v138, v122, v126\nv_cvt_pk_bf16_f32 v139, v123, v127\n"
    "v_cvt_pk_bf16_f32 v140, v128, v132\nv_cvt_pk_bf16_f32 v141, v129, v133\nv_cvt_pk_bf16_f32 v142, v130, v134\nv_cvt_pk_bf16_f32 v143, v131, v135\n"
    "v_lshlrev_b32 v120, 16, v136\nv_and_b32 v124, s84, v136\nv_lshlrev_b32 v128, 16, v140\nv_and_b32 v132, s84, v140\nv_lshlrev_b32 v121, 16, v137\n"
    "v_and_b32 v125, s84, v137\nv_lshlrev_b32 v129, 16, v141\nv_and_b32 v133, s84, v141\nv_lshlrev_b32 v122, 16, v138\nv_and_b32 v126, s84, v138\n"
    "v_lshlrev_b32 v130, 16, v142\nv_and_b32 v134, s84, v142\nv_lshlrev_b32 v123, 16, v139\nv_and_b32 v127, s84, v139\nv_lshlrev_b32 v131, 16, v143\n"
    "v_and_b32 v135, s84, v143\nv_mul_f32 v120, v120, v128\nv_mul_f32 v121, v121, v129\nv_mul_f32 v122, v122, v130\nv_mul_f32 v123, v123, v131\n"
    "v_mul_f32 v124, v124, v132\nv_mul_f32 v125, v125, v133\nv_mul_f32 v126, v126, v134\nv_mul_f32 v127, v127, v135\nv_cvt_pk_bf16_f32 v128, v120, v124\n"
    "v_cvt_pk_bf16_f32 v129, v121, v125\nv_cvt_pk_bf16_f32 v130, v122, v126\nv_cvt_pk_bf16_f32 v131, v123, v127\n"
    "buffer_store_dwordx4 v[128:131], v53, %[rc], s89 offen sc1\ns_nop 1\n"
    : [pre] "=&s"(pre)
    : [rc] "s"(rc), [lr] "s"(lr), [ls] "s"(ls), [sO] "s"(sO), [sL] "s"(sL), [nd] "s"(nd), [pv] "s"(pv), [sH] "s"(sH), [ld] "s"(ld), [sS] "s"(sS), [ls2] "s"(ls2), "{v[16:31]}"(cs), "{v[32:47]}"(bi), "{v[48:63]}"(cv)
    : "memory", "vcc", "scc", CLS, CLV_64_167);
}
DI void x3_prep(rsrc_t rc, unsigned vsb, unsigned vc1, v16u& cs, v16u& bi) {
  asm volatile(
    "s_nop 4\nbuffer_load_dwordx4 v[64:67], %[vsb], %[rc], 0 offen offset:0 sc1\nbuffer_load_dwordx4 v[68:71], %[vsb], %[rc], 0 offen offset:64 sc1\n"
    "buffer_load_dwordx4 v[72:75], %[vsb], %[rc], 0 offen offset:128 sc1\nbuffer_load_dwordx4 v[76:79], %[vsb], %[rc], 0 offen offset:192 sc1\n"
    "buffer_load_dwordx4 v[80:83], %[vsb], %[rc], 0 offen offset:256 sc1\nbuffer_load_dwordx4 v[84:87], %[vsb], %[rc], 0 offen offset:320 sc1\n"
    "buffer_load_dwordx4 v[88:91], %[vsb], %[rc], 0 offen offset:384 sc1\nbuffer_load_dwordx4 v[92:95], %[vsb], %[rc], 0 offen offset:448 sc1\n"
    "buffer_load_dwordx4 v[96:99], %[vsb], %[rc], 0 offen offset:512 sc1\nbuffer_load_dwordx4 v[100:103], %[vsb], %[rc], 0 offen offset:576 sc1\n"
    "buffer_load_dwordx4 v[104:107], %[vsb], %[rc], 0 offen offset:640 sc1\nbuffer_load_dwordx4 v[108:111], %[vsb], %[rc], 0 offen offset:704 sc1\n"
    "buffer_load_dwordx4 v[112:115], %[vsb], %[rc], 0 offen offset:768 sc1\nbuffer_load_dwordx4 v[116:119], %[vsb], %[rc], 0 offen offset:832 sc1\n"
    "buffer_load_dwordx4 v[120:123], %[vsb], %[rc], 0 offen offset:896 sc1\nbuffer_load_dwordx4 v[124:127], %[vsb], %[rc], 0 offen offset:960 sc1\n"
    "buffer_load_dwordx4 v[128:131], %[vc1], %[rc], 0 offen offset:0 sc1\nbuffer_load_dwordx4 v[132:135], %[vc1], %[rc], 0 offen offset:16 sc1\n"
    "buffer_load_dwordx4 v[136:139], %[vc1], %[rc], 0 offen offset:128 sc1\nbuffer_load_dwordx4 v[140:143], %[vc1], %[rc], 0 offen offset:144 sc1\n"
    "buffer_load_dwordx4 v[144:147], %[vc1], %[rc], 0 offen offset:256 sc1\nbuffer_load_dwordx4 v[148:151], %[vc1], %[rc], 0 offen offset:272 sc1\n"
    "buffer_load_dwordx4 v[152:155], %[vc1], %[rc], 0 offen offset:384 sc1\nbuffer_load_dwordx4 v[156:159], %[vc1], %[rc], 0 offen offset:400 sc1\n"
    "buffer_load_dwordx4 v[160:163], %[vc1], %[rc], 0 offen offset:512 sc1\nbuffer_load_dwordx4 v[164:167], %[vc1], %[rc], 0 offen offset:528 sc1\n"
    "buffer_load_dwordx4 v[168:171], %[vc1], %[rc], 0 offen offset:640 sc1\nbuffer_load_dwordx4 v[172:175], %[vc1], %[rc], 0 offen offset:656 sc1\n"
    "buffer_load_dwordx4 v[176:179], %[vc1], %[rc], 0 offen offset:768 sc1\nbuffer_load_dwordx4 v[180:183], %[vc1], %[rc], 0 offen offset:784 sc1\n"
    "buffer_load_dwordx4 v[184:187], %[vc1], %[rc], 0 offen offset:896 sc1\nbuffer_load_dwordx4 v[188:191], %[vc1], %[rc], 0 offen offset:912 sc1\n"
    "s_mov_b32 s84, 0xffff0000\ns_waitcnt vmcnt(16)\nv_mfma_f32_16x16x32_bf16 v[32:35], a[0:3], v[64:67], 0\n"
    "v_mfma_f32_16x16x32_bf16 v[36:39], a[4:7], v[64:67], 0\nv_mfma_f32_16x16x32_bf16 v[40:43], a[8:11], v[64:67], 0\n"
    "v_mfma_f32_16x16x32_bf16 v[44:47], a[12:15], v[64:67], 0\nv_mfma_f32_16x16x32_bf16 v[32:35], a[16:19], v[68:71], v[32:35]\n"
    "v_mfma_f32_16x16x32_bf16 v[36:39], a[20:23], v[68:71], v[36:39]\nv_mfma_f32_16x16x32_bf16 v[40:43], a[24:27], v[68:71], v[40:43]\n"
    "v_mfma_f32_16x16x32_bf16 v[44:47], a[28:31], v[68:71], v[44:47]\nv_mfma_f32_16x16x32_bf16 v[32:35], a[32:35], v[72:75], v[32:35]\n"
    "v_mfma_f32_16x16x32_bf16 v[36:39], a[36:39], v[72:75], v[36:39]\nv_mfma_f32_16x16x32_bf16 v[40:43], a[40:43], v[72:75], v[40:43]\n"
    "v_mfma_f32_16x16x32_bf16 v[44:47], a[44:47], v[72:75], v[44:47]\nv_mfma_f32_16x16x32_bf16 v[32:35], a[48:51], v[76:79], v[32:35]\n"
    "v_mfma_f32_16x16x32_bf16 v[36:39], a[52:55], v[76:79], v[36:39]\nv_mfma_f32_16x16x32_bf16 v[40:43], a[56:59], v[76:79], v[40:43]\n"
    "v_mfma_f32_16x16x32_bf16 v[44:47], a[60:63], v[76:79], v[44:47]\nv_mfma_f32_16x16x32_bf16 v[32:35], a[64:67], v[80:83], v[32:35]\n"
    "v_mfma_f32_16x16x32_bf16 v[36:39], a[68:71], v[80:83], v[36:39]\nv_mfma_f32_16x16x32_bf16 v[40:43], a[72:75], v[80:83], v[40:43]\n"
    "v_mfma_f32_16x16x32_bf16 v[44:47], a[76:79], v[80:83], v[44:47]\nv_mfma_f32_16x16x32_bf16 v[32:35], a[80:83], v[84:87], v[32:35]\n"
    "v_mfma_f32_16x16x32_bf16 v[36:39], a[84:87], v[84:87], v[36:39]\nv_mfma_f32_16x16x32_bf16 v[40:43], a[88:91], v[84:87], v[40:43]\n"
    "v_mfma_f32_16x16x32_bf16 v[44:47], a[92:95], v[84:87], v[44:47]\nv_mfma_f32_16x16x32_bf16 v[32:35], a[96:99], v[88:91], v[32:35]\n"
    "v_mfma_f32_16x16x32_bf16 v[36:39], a[100:103], v[88:91], v[36:39]\nv_mfma_f32_16x16x32_bf16 v[40:43], a[104:107], v[88:91], v[40:43]\n"
    "v_mfma_f32_16x16x32_bf16 v[44:47], a[108:111], v[88:91], v[44:47]\nv_mfma_f32_16x16x32_bf16 v[32:35], a[112:115], v[92:95], v[32:35]\n"
    "v_mfma_f32_16x16x32_bf16 v[36:39], a[116:119], v[92:95], v[36:39]\nv_mfma_f32_16x16x32_bf16 v[40:43], a[120:123], v[92:95], v[40:43]\n"
    "v_mfma_f32_16x16x32_bf16 v[44:47], a[124:127], v[92:95], v[44:47]\nv_mfma_f32_16x16x32_bf16 v[32:35], a[128:131], v[96:99], v[32:35]\n"
    "v_mfma_f32_16x16x32_bf16 v[36:39], a[132:135], v[96:99], v[36:39]\nv_mfma_f32_16x16x32_bf16 v[40:43], a[136:139], v[96:99], v[40:43]\n"
    "v_mfma_f32_16x16x32_bf16 v[44:47], a[140:143], v[96:99], v[44:47]\nv_mfma_f32_16x16x32_bf16 v[32:35], a[144:147], v[100:103], v[32:35]\n"
    "v_mfma_f32_16x16x32_bf16 v[36:39], a[148:151], v[100:103], v[36:39]\nv_mfma_f32_16x16x32_bf16 v[40:43], a[152:155], v[100:103], v[40:43]\n"
    "v_mfma_f32_16x16x32_bf16 v[44:47], a[156:159], v[100:103], v[44:47]\nv_mfma_f32_16x16x32_bf16 v[32:35], a[160:163], v[104:107], v[32:35]\n"
    "v_mfma_f32_16x16x32_bf16 v[36:39], a[164:167], v[104:107], v[36:39]\nv_mfma_f32_16x16x32_bf16 v[40:43], a[168:171], v[104:107], v[40:43]\n"
    "v_mfma_f32_16x16x32_bf16 v[44:47], a[172:175], v[104:107], v[44:47]\nv_mfma_f32_16x16x32_bf16 v[32:35], a[176:179], v[108:111], v[32:35]\n"
    "v_mfma_f32_16x16x32_bf16 v[36:39], a[180:183], v[108:111], v[36:39]\nv_mfma_f32_16x16x32_bf16 v[40:43], a[184:187], v[108:111], v[40:43]\n"
    "v_mfma_f32_16x16x32_bf16 v[44:47], a[188:191], v[108:111], v[44:47]\nv_mfma_f32_16x16x32_bf16 v[32:35], a[192:195], v[112:115], v[32:35]\n"
    "v_mfma_f32_16x16x32_bf16 v[36:39], a[196:199], v[112:115], v[36:39]\nv_mfma_f32_16x16x32_bf16 v[40:43], a[200:203], v[112:115], v[40:43]\n"
    "v_mfma_f32_16x16x32_bf16 v[44:47], a[204:207], v[112:115], v[44:47]\nv_mfma_f32_16x16x32_bf16 v[32:35], a[208:211], v[116:119], v[32:35]\n"
    "v_mfma_f32_16x16x32_bf16 v[36:39], a[212:215], v[116:119], v[36:39]\nv_mfma_f32_16x16x32_bf16 v[40:43], a[216:219], v[116:119], v[40:43]\n"
    "v_mfma_f32_16x16x32_bf16 v[44:47], a[220:223], v[116:119], v[44:47]\nv_mfma_f32_16x16x32_bf16 v[32:35], a[224:227], v[120:123], v[32:35]\n"
    "v_mfma_f32_16x16x32_bf16 v[36:39], a[228:231], v[120:123], v[36:39]\nv_mfma_f32_16x16x32_bf16 v[40:43], a[232:235], v[120:123], v[40:43]\n"
    "v_mfma_f32_16x16x32_bf16 v[44:47], a[236:239], v[120:123], v[44:47]\nv_mfma_f32_16x16x32_bf16 v[32:35], a[240:243], v[124:127], v[32:35]\n"
    "v_mfma_f32_16x16x32_bf16 v[36:39], a[244:247], v[124:127], v[36:39]\nv_mfma_f32_16x16x32_bf16 v[40:43], a[248:251], v[124:127], v[40:43]\n"
    "v_mfma_f32_16x16x32_bf16 v[44:47], a[252:255], v[124:127], v[44:47]\nbuffer_load_dwordx4 v[64:67], %[vc1], %[rc], 0 offen offset:1024 sc1\n"
    "buffer_load_dwordx4 v[68:71], %[vc1], %[rc], 0 offen offset:1040 sc1\nbuffer_load_dwordx4 v[72:75], %[vc1], %[rc], 0 offen offset:1152 sc1\n"
    "buffer_load_dwordx4 v[76:79], %[vc1], %[rc], 0 offen offset:1168 sc1\nbuffer_load_dwordx4 v[80:83], %[vc1], %[rc], 0 offen offset:1280 sc1\n"
    "buffer_load_dwordx4 v[84:87], %[vc1], %[rc], 0 offen offset:1296 sc1\nbuffer_load_dwordx4 v[88:91], %[vc1], %[rc], 0 offen offset:1408 sc1\n"
    "buffer_load_dwordx4 v[92:95], %[vc1], %[rc], 0 offen offset:1424 sc1\nbuffer_load_dwordx4 v[96:99], %[vc1], %[rc], 0 offen offset:1536 sc1\n"
    "buffer_load_dwordx4 v[100:103], %[vc1], %[rc], 0 offen offset:1552 sc1\nbuffer_load_dwordx4 v[104:107], %[vc1], %[rc], 0 offen offset:1664 sc1\n"
    "buffer_load_dwordx4 v[108:111], %[vc1], %[rc], 0 offen offset:1680 sc1\nbuffer_load_dwordx4 v[112:115], %[vc1], %[rc], 0 offen offset:1792 sc1\n"
    "buffer_load_dwordx4 v[116:119], %[vc1], %[rc], 0 offen offset:1808 sc1\nbuffer_load_dwordx4 v[120:123], %[vc1], %[rc], 0 offen offset:1920 sc1\n"
    "buffer_load_dwordx4 v[124:127], %[vc1], %[rc], 0 offen offset:1936 sc1\ns_waitcnt vmcnt(16)\nv_accvgpr_read_b32 v48, a0\nv_accvgpr_read_b32 v49, a1\n"
    "v_accvgpr_read_b32 v50, a2\nv_accvgpr_read_b32 v51, a3\nv_lshlrev_b32 v52, 16, v48\nv_and_b32 v53, s84, v48\nv_lshlrev_b32 v54, 16, v49\n"
    "v_and_b32 v55, s84, v49\nv_lshlrev_b32 v56, 16, v50\nv_and_b32 v57, s84, v50\nv_lshlrev_b32 v58, 16, v51\nv_and_b32 v59, s84, v51\n"
    "v_pk_mul_f32 v[52:53], v[52:53], v[128:129]\nv_pk_mul_f32 v[54:55], v[54:55], v[130:131]\nv_pk_mul_f32 v[56:57], v[56:57], v[132:133]\n"
    "v_pk_mul_f32 v[58:59], v[58:59], v[134:135]\nv_cvt_pk_bf16_f32 v48, v52, v53\nv_cvt_pk_bf16_f32 v49, v54, v55\nv_cvt_pk_bf16_f32 v50, v56, v57\n"
    "v_cvt_pk_bf16_f32 v51, v58, v59\nv_accvgpr_write_b32 a0, v48\nv_accvgpr_write_b32 a1, v49\nv_accvgpr_write_b32 a2, v50\nv_accvgpr_write_b32 a3, v51\n"
    "v_accvgpr_read_b32 v48, a4\nv_accvgpr_read_b32 v49, a5\nv_accvgpr_read_b32 v50, a6\nv_accvgpr_read_b32 v51, a7\nv_lshlrev_b32 v52, 16, v48\n"
    "v_and_b32 v53, s84, v48\nv_lshlrev_b32 v54, 16, v49\nv_and_b32 v55, s84, v49\nv_lshlrev_b32 v56, 16, v50\nv_and_b32 v57, s84, v50\n"
    "v_lshlrev_b32 v58, 16, v51\nv_and_b32 v59, s84, v51\nv_pk_mul_f32 v[52:53], v[52:53], v[128:129]\nv_pk_mul_f32 v[54:55], v[54:55], v[130:131]\n"
    "v_pk_mul_f32 v[56:57], v[56:57], v[132:133]\nv_pk_mul_f32 v[58:59], v[58:59], v[134:135]\nv_cvt_pk_bf16_f32 v48, v52, v53\n"
    "v_cvt_pk_bf16_f32 v49, v54, v55\nv_cvt_pk_bf16_f32 v50, v56, v57\nv_cvt_pk_bf16_f32 v51, v58, v59\nv_accvgpr_write_b32 a4, v48\n"
    "v_accvgpr_write_b32 a5, v49\nv_accvgpr_write_b32 a6, v50\nv_accvgpr_write_b32 a7, v51\nv_accvgpr_read_b32 v48, a8\nv_accvgpr_read_b32 v49, a9\n"
    "v_accvgpr_read_b32 v50, a10\nv_accvgpr_read_b32 v51, a11\nv_lshlrev_b32 v52, 16, v48\nv_and_b32 v53, s84, v48\nv_lshlrev_b32 v54, 16, v49\n"
    "v_and_b32 v55, s84, v49\nv_lshlrev_b32 v56, 16, v50\nv_and_b32 v57, s84, v50\nv_lshlrev_b32 v58, 16, v51\nv_and_b32 v59, s84, v51\n"
    "v_pk_mul_f32 v[52:53], v[52:53], v[128:129]\nv_pk_mul_f32 v[54:55], v[54:55], v[130:131]\nv_pk_mul_f32 v[56:57], v[56:57], v[132:133]\n"
    "v_pk_mul_f32 v[58:59], v[58:59], v[134:135]\nv_cvt_pk_bf16_f32 v48, v52, v53\nv_cvt_pk_bf16_f32 v49, v54, v55\nv_cvt_pk_bf16_f32 v50, v56, v57\n"
    "v_cvt_pk_bf16_f32 v51, v58, v59\nv_accvgpr_write_b32 a8, v48\nv_accvgpr_write_b32 a9, v49\nv_accvgpr_write_b32 a10, v50\nv_accvgpr_write_b32 a11, v51\n"
    "v_accvgpr_read_b32 v48, a12\nv_accvgpr_read_b32 v49, a13\nv_accvgpr_read_b32 v50, a14\nv_accvgpr_read_b32 v51, a15\nv_lshlrev_b32 v52, 16, v48\n"
    "v_and_b32 v53, s84, v48\nv_lshlrev_b32 v54, 16, v49\nv_and_b32 v55, s84, v49\nv_lshlrev_b32 v56, 16, v50\nv_and_b32 v57, s84, v50\n"
    "v_lshlrev_b32 v58, 16, v51\nv_and_b32 v59, s84, v51\nv_pk_mul_f32 v[52:53], v[52:53], v[128:129]\nv_pk_mul_f32 v[54:55], v[54:55], v[130:131]\n"
    "v_pk_mul_f32 v[56:57], v[56:57], v[132:133]\nv_pk_mul_f32 v[58:59], v[58:59], v[134:135]\nv_cvt_pk_bf16_f32 v48, v52, v53\n"
    "v_cvt_pk_bf16_f32 v49, v54, v55\nv_cvt_pk_bf16_f32 v50, v56, v57\nv_cvt_pk_bf16_f32 v51, v58, v59\nv_accvgpr_write_b32 a12, v48\n"
    "v_accvgpr_write_b32 a13, v49\nv_accvgpr_write_b32 a14, v50\nv_accvgpr_write_b32 a15, v51\nv_accvgpr_read_b32 v48, a16\nv_accvgpr_read_b32 v49, a17\n"
    "v_accvgpr_read_b32 v50, a18\nv_accvgpr_read_b32 v51, a19\nv_lshlrev_b32 v52, 16, v48\nv_and_b32 v53, s84, v48\nv_lshlrev_b32 v54, 16, v49\n"
    "v_and_b32 v55, s84, v49\nv_lshlrev_b32 v56, 16, v50\nv_and_b32 v57, s84, v50\nv_lshlrev_b32 v58, 16, v51\nv_and_b32 v59, s84, v51\n"
    "v_pk_mul_f32 v[52:53], v[52:53], v[136:137]\nv_pk_mul_f32 v[54:55], v[54:55], v[138:139]\nv_pk_mul_f32 v[56:57], v[56:57], v[140:141]\n"
    "v_pk_mul_f32 v[58:59], v[58:59], v[142:143]\nv_cvt_pk_bf16_f32 v48, v52, v53\nv_cvt_pk_bf16_f32 v49, v54, v55\nv_cvt_pk_bf16_f32 v50, v56, v57\n"
    "v_cvt_pk_bf16_f32 v51, v58, v59\nv_accvgpr_write_b32 a16, v48\nv_accvgpr_write_b32 a17, v49\nv_accvgpr_write_b32 a18, v50\n"
    "v_accvgpr_write_b32 a19, v51\nv_accvgpr_read_b32 v48, a20\nv_accvgpr_read_b32 v49, a21\nv_accvgpr_read_b32 v50, a22\nv_accvgpr_read_b32 v51, a23\n"
    "v_lshlrev_b32 v52, 16, v48\nv_and_b32 v53, s84, v48\nv_lshlrev_b32 v54, 16, v49\nv_and_b32 v55, s84, v49\nv_lshlrev_b32 v56, 16, v50\n"
    "v_and_b32 v57, s84, v50\nv_lshlrev_b32 v58, 16, v51\nv_and_b32 v59, s84, v51\nv_pk_mul_f32 v[52:53], v[52:53], v[136:137]\n"
    "v_pk_mul_f32 v[54:55], v[54:55], v[138:139]\nv_pk_mul_f32 v[56:57], v[56:57], v[140:141]\nv_pk_mul_f32 v[58:59], v[58:59], v[142:143]\n"
    "v_cvt_pk_bf16_f32 v48, v52, v53\nv_cvt_pk_bf16_f32 v49, v54, v55\nv_cvt_pk_bf16_f32 v50, v56, v57\nv_cvt_pk_bf16_f32 v51, v58, v59\n"
    "v_accvgpr_write_b32 a20, v48\nv_accvgpr_write_b32 a21, v49\nv_accvgpr_write_b32 a22, v50\nv_accvgpr_write_b32 a23, v51\nv_accvgpr_read_b32 v48, a24\n"
    "v_accvgpr_read_b32 v49, a25\nv_accvgpr_read_b32 v50, a26\nv_accvgpr_read_b32 v51, a27\nv_lshlrev_b32 v52, 16, v48\nv_and_b32 v53, s84, v48\n"
    "v_lshlrev_b32 v54, 16, v49\nv_and_b32 v55, s84, v49\nv_lshlrev_b32 v56, 16, v50\nv_and_b32 v57, s84, v50\nv_lshlrev_b32 v58, 16, v51\n"
    "v_and_b32 v59, s84, v51\nv_pk_mul_f32 v[52:53], v[52:53], v[136:137]\nv_pk_mul_f32 v[54:55], v[54:55], v[138:139]\n"
    "v_pk_mul_f32 v[56:57], v[56:57], v[140:141]\nv_pk_mul_f32 v[58:59], v[58:59], v[142:143]\nv_cvt_pk_bf16_f32 v48, v52, v53\n"
    "v_cvt_pk_bf16_f32 v49, v54, v55\nv_cvt_pk_bf16_f32 v50, v56, v57\nv_cvt_pk_bf16_f32 v51, v58, v59\nv_accvgpr_write_b32 a24, v48\n"
    "v_accvgpr_write_b32 a25, v49\nv_accvgpr_write_b32 a26, v50\nv_accvgpr_write_b32 a27, v51\nv_accvgpr_read_b32 v48, a28\nv_accvgpr_read_b32 v49, a29\n"
    "v_accvgpr_read_b32 v50, a30\nv_accvgpr_read_b32 v51, a31\nv_lshlrev_b32 v52, 16, v48\nv_and_b32 v53, s84, v48\nv_lshlrev_b32 v54, 16, v49\n"
    "v_and_b32 v55, s84, v49\nv_lshlrev_b32 v56, 16, v50\nv_and_b32 v57, s84, v50\nv_lshlrev_b32 v58, 16, v51\nv_and_b32 v59, s84, v51\n"
    "v_pk_mul_f32 v[52:53], v[52:53], v[136:137]\nv_pk_mul_f32 v[54:55], v[54:55], v[138:139]\nv_pk_mul_f32 v[56:57], v[56:57], v[140:141]\n"
    "v_pk_mul_f32 v[58:59], v[58:59], v[142:143]\nv_cvt_pk_bf16_f32 v48, v52, v53\nv_cvt_pk_bf16_f32 v49, v54, v55\nv_cvt_pk_bf16_f32 v50, v56, v57\n"
    "v_cvt_pk_bf16_f32 v51, v58, v59\nv_accvgpr_write_b32 a28, v48\nv_accvgpr_write_b32 a29, v49\nv_accvgpr_write_b32 a30, v50\n"
    "v_accvgpr_write_b32 a31, v51\nv_accvgpr_read_b32 v48, a32\nv_accvgpr_read_b32 v49, a33\nv_accvgpr_read_b32 v50, a34\nv_accvgpr_read_b32 v51, a35\n"
    "v_lshlrev_b32 v52, 16, v48\nv_and_b32 v53, s84, v48\nv_lshlrev_b32 v54, 16, v49\nv_and_b32 v55, s84, v49\nv_lshlrev_b32 v56, 16, v50\n"
    "v_and_b32 v57, s84, v50\nv_lshlrev_b32 v58, 16, v51\nv_and_b32 v59, s84, v51\nv_pk_mul_f32 v[52:53], v[52:53], v[144:145]\n"
    "v_pk_mul_f32 v[54:55], v[54:55], v[146:147]\nv_pk_mul_f32 v[56:57], v[56:57], v[148:149]\nv_pk_mul_f32 v[58:59], v[58:59], v[150:151]\n"
    "v_cvt_pk_bf16_f32 v48, v52, v53\nv_cvt_pk_bf16_f32 v49, v54, v55\nv_cvt_pk_bf16_f32 v50, v56, v57\nv_cvt_pk_bf16_f32 v51, v58, v59\n"
    "v_accvgpr_write_b32 a32, v48\nv_accvgpr_write_b32 a33, v49\nv_accvgpr_write_b32 a34, v50\nv_accvgpr_write_b32 a35, v51\nv_accvgpr_read_b32 v48, a36\n"
    "v_accvgpr_read_b32 v49, a37\nv_accvgpr_read_b32 v50, a38\nv_accvgpr_read_b32 v51, a39\nv_lshlrev_b32 v52, 16, v48\nv_and_b32 v53, s84, v48\n"
    "v_lshlrev_b32 v54, 16, v49\nv_and_b32 v55, s84, v49\nv_lshlrev_b32 v56, 16, v50\nv_and_b32 v57, s84, v50\nv_lshlrev_b32 v58, 16, v51\n"
    "v_and_b32 v59, s84, v51\nv_pk_mul_f32 v[52:53], v[52:53], v[144:145]\nv_pk_mul_f32 v[54:55], v[54:55], v[146:147]\n"
    "v_pk_mul_f32 v[56:57], v[56:57], v[148:149]\nv_pk_mul_f32 v[58:59], v[58:59], v[150:151]\nv_cvt_pk_bf16_f32 v48, v52, v53\n"
    "v_cvt_pk_bf16_f32 v49, v54, v55\nv_cvt_pk_bf16_f32 v50, v56, v57\nv_cvt_pk_bf16_f32 v51, v58, v59\nv_accvgpr_write_b32 a36, v48\n"
    "v_accvgpr_write_b32 a37, v49\nv_accvgpr_write_b32 a38, v50\nv_accvgpr_write_b32 a39, v51\nv_accvgpr_read_b32 v48, a40\nv_accvgpr_read_b32 v49, a41\n"
    "v_accvgpr_read_b32 v50, a42\nv_accvgpr_read_b32 v51, a43\nv_lshlrev_b32 v52, 16, v48\nv_and_b32 v53, s84, v48\nv_lshlrev_b32 v54, 16, v49\n"
    "v_and_b32 v55, s84, v49\nv_lshlrev_b32 v56, 16, v50\nv_and_b32 v57, s84, v50\nv_lshlrev_b32 v58, 16, v51\nv_and_b32 v59, s84, v51\n"
    "v_pk_mul_f32 v[52:53], v[52:53], v[144:145]\nv_pk_mul_f32 v[54:55], v[54:55], v[146:147]\nv_pk_mul_f32 v[56:57], v[56:57], v[148:149]\n"
    "v_pk_mul_f32 v[58:59], v[58:59], v[150:151]\nv_cvt_pk_bf16_f32 v48, v52, v53\nv_cvt_pk_bf16_f32 v49, v54, v55\nv_cvt_pk_bf16_f32 v50, v56, v57\n"
    "v_cvt_pk_bf16_f32 v51, v58, v59\nv_accvgpr_write_b32 a40, v48\nv_accvgpr_write_b32 a41, v49\nv_accvgpr_write_b32 a42, v50\n"
    "v_accvgpr_write_b32 a43, v51\nv_accvgpr_read_b32 v48, a44\nv_accvgpr_read_b32 v49, a45\nv_accvgpr_read_b32 v50, a46\nv_accvgpr_read_b32 v51, a47\n"
    "v_lshlrev_b32 v52, 16, v48\nv_and_b32 v53, s84, v48\nv_lshlrev_b32 v54, 16, v49\nv_and_b32 v55, s84, v49\nv_lshlrev_b32 v56, 16, v50\n"
    "v_and_b32 v57, s84, v50\nv_lshlrev_b32 v58, 16, v51\nv_and_b32 v59, s84, v51\nv_pk_mul_f32 v[52:53], v[52:53], v[144:145]\n"
    "v_pk_mul_f32 v[54:55], v[54:55], v[146:147]\nv_pk_mul_f32 v[56:57], v[56:57], v[148:149]\nv_pk_mul_f32 v[58:59], v[58:59], v[150:151]\n"
    "v_cvt_pk_bf16_f32 v48, v52, v53\nv_cvt_pk_bf16_f32 v49, v54, v55\nv_cvt_pk_bf16_f32 v50, v56, v57\nv_cvt_pk_bf16_f32 v51, v58, v59\n"
    "v_accvgpr_write_b32 a44, v48\nv_accvgpr_write_b32 a45, v49\nv_accvgpr_write_b32 a46, v50\nv_accvgpr_write_b32 a47, v51\nv_accvgpr_read_b32 v48, a48\n"
    "v_accvgpr_read_b32 v49, a49\nv_accvgpr_read_b32 v50, a50\nv_accvgpr_read_b32 v51, a51\nv_lshlrev_b32 v52, 16, v48\nv_and_b32 v53, s84, v48\n"
    "v_lshlrev_b32 v54, 16, v49\nv_and_b32 v55, s84, v49\nv_lshlrev_b32 v56, 16, v50\nv_and_b32 v57, s84, v50\nv_lshlrev_b32 v58, 16, v51\n"
    "v_and_b32 v59, s84, v51\nv_pk_mul_f32 v[52:53], v[52:53], v[152:153]\nv_pk_mul_f32 v[54:55], v[54:55], v[154:155]\n"
    "v_pk_mul_f32 v[56:57], v[56:57], v[156:157]\nv_pk_mul_f32 v[58:59], v[58:59], v[158:159]\nv_cvt_pk_bf16_f32 v48, v52, v53\n"
    "v_cvt_pk_bf16_f32 v49, v54, v55\nv_cvt_pk_bf16_f32 v50, v56, v57\nv_cvt_pk_bf16_f32 v51, v58, v59\nv_accvgpr_write_b32 a48, v48\n"
    "v_accvgpr_write_b32 a49, v49\nv_accvgpr_write_b32 a50, v50\nv_accvgpr_write_b32 a51, v51\nv_accvgpr_read_b32 v48, a52\nv_accvgpr_read_b32 v49, a53\n"
    "v_accvgpr_read_b32 v50, a54\nv_accvgpr_read_b32 v51, a55\nv_lshlrev_b32 v52, 16, v48\nv_and_b32 v53, s84, v48\nv_lshlrev_b32 v54, 16, v49\n"
    "v_and_b32 v55, s84, v49\nv_lshlrev_b32 v56, 16, v50\nv_and_b32 v57, s84, v50\nv_lshlrev_b32 v58, 16, v51\nv_and_b32 v59, s84, v51\n"
    "v_pk_mul_f32 v[52:53], v[52:53], v[152:153]\nv_pk_mul_f32 v[54:55], v[54:55], v[154:155]\nv_pk_mul_f32 v[56:57], v[56:57], v[156:157]\n"
    "v_pk_mul_f32 v[58:59], v[58:59], v[158:159]\nv_cvt_pk_bf16_f32 v48, v52, v53\nv_cvt_pk_bf16_f32 v49, v54, v55\nv_cvt_pk_bf16_f32 v50, v56, v57\n"
    "v_cvt_pk_bf16_f32 v51, v58, v59\nv_accvgpr_write_b32 a52, v48\nv_accvgpr_write_b32 a53, v49\nv_accvgpr_write_b32 a54, v50\n"
    "v_accvgpr_write_b32 a55, v51\nv_accvgpr_read_b32 v48, a56\nv_accvgpr_read_b32 v49, a57\nv_accvgpr_read_b32 v50, a58\nv_accvgpr_read_b32 v51, a59\n"
    "v_lshlrev_b32 v52, 16, v48\nv_and_b32 v53, s84, v48\nv_lshlrev_b32 v54, 16, v49\nv_and_b32 v55, s84, v49\nv_lshlrev_b32 v56, 16, v50\n"
    "v_and_b32 v57, s84, v50\nv_lshlrev_b32 v58, 16, v51\nv_and_b32 v59, s84, v51\nv_pk_mul_f32 v[52:53], v[52:53], v[152:153]\n"
    "v_pk_mul_f32 v[54:55], v[54:55], v[154:155]\nv_pk_mul_f32 v[56:57], v[56:57], v[156:157]\nv_pk_mul_f32 v[58:59], v[58:59], v[158:159]\n"
    "v_cvt_pk_bf16_f32 v48, v52, v53\nv_cvt_pk_bf16_f32 v49, v54, v55\nv_cvt_pk_bf16_f32 v50, v56, v57\nv_cvt_pk_bf16_f32 v51, v58, v59\n"
    "v_accvgpr_write_b32 a56, v48\nv_accvgpr_write_b32 a57, v49\nv_accvgpr_write_b32 a58, v50\nv_accvgpr_write_b32 a59, v51\nv_accvgpr_read_b32 v48, a60\n"
    "v_accvgpr_read_b32 v49, a61\nv_accvgpr_read_b32 v50, a62\nv_accvgpr_read_b32 v51, a63\nv_lshlrev_b32 v52, 16, v48\nv_and_b32 v53, s84, v48\n"
    "v_lshlrev_b32 v54, 16, v49\nv_and_b32 v55, s84, v49\nv_lshlrev_b32 v56, 16, v50\nv_and_b32 v57, s84, v50\nv_lshlrev_b32 v58, 16, v51\n"
    "v_and_b32 v59, s84, v51\nv_pk_mul_f32 v[52:53], v[52:53], v[152:153]\nv_pk_mul_f32 v[54:55], v[54:55], v[154:155]\n"
    "v_pk_mul_f32 v[56:57], v[56:57], v[156:157]\nv_pk_mul_f32 v[58:59], v[58:59], v[158:159]\nv_cvt_pk_bf16_f32 v48, v52, v53\n"
    "v_cvt_pk_bf16_f32 v49, v54, v55\nv_cvt_pk_bf16_f32 v50, v56, v57\nv_cvt_pk_bf16_f32 v51, v58, v59\nv_accvgpr_write_b32 a60, v48\n"
    "v_accvgpr_write_b32 a61, v49\nv_accvgpr_write_b32 a62, v50\nv_accvgpr_write_b32 a63, v51\nv_accvgpr_read_b32 v48, a64\nv_accvgpr_read_b32 v49, a65\n"
    "v_accvgpr_read_b32 v50, a66\nv_accvgpr_read_b32 v51, a67\nv_lshlrev_b32 v52, 16, v48\nv_and_b32 v53, s84, v48\nv_lshlrev_b32 v54, 16, v49\n"
    "v_and_b32 v55, s84, v49\nv_lshlrev_b32 v56, 16, v50\nv_and_b32 v57, s84, v50\nv_lshlrev_b32 v58, 16, v51\nv_and_b32 v59, s84, v51\n"
    "v_pk_mul_f32 v[52:53], v[52:53], v[160:161]\nv_pk_mul_f32 v[54:55], v[54:55], v[162:163]\nv_pk_mul_f32 v[56:57], v[56:57], v[164:165]\n"
    "v_pk_mul_f32 v[58:59], v[58:59], v[166:167]\nv_cvt_pk_bf16_f32 v48, v52, v53\nv_cvt_pk_bf16_f32 v49, v54, v55\nv_cvt_pk_bf16_f32 v50, v56, v57\n"
    "v_cvt_pk_bf16_f32 v51, v58, v59\nv_accvgpr_write_b32 a64, v48\nv_accvgpr_write_b32 a65, v49\nv_accvgpr_write_b32 a66, v50\n"
    "v_accvgpr_write_b32 a67, v51\nv_accvgpr_read_b32 v48, a68\nv_accvgpr_read_b32 v49, a69\nv_accvgpr_read_b32 v50, a70\nv_accvgpr_read_b32 v51, a71\n"
    "v_lshlrev_b32 v52, 16, v48\nv_and_b32 v53, s84, v48\nv_lshlrev_b32 v54, 16, v49\nv_and_b32 v55, s84, v49\nv_lshlrev_b32 v56, 16, v50\n"
    "v_and_b32 v57, s84, v50\nv_lshlrev_b32 v58, 16, v51\nv_and_b32 v59, s84, v51\nv_pk_mul_f32 v[52:53], v[52:53], v[160:161]\n"
    "v_pk_mul_f32 v[54:55], v[54:55], v[162:163]\nv_pk_mul_f32 v[56:57], v[56:57], v[164:165]\nv_pk_mul_f32 v[58:59], v[58:59], v[166:167]\n"
    "v_cvt_pk_bf16_f32 v48, v52, v53\nv_cvt_pk_bf16_f32 v49, v54, v55\nv_cvt_pk_bf16_f32 v50, v56, v57\nv_cvt_pk_bf16_f32 v51, v58, v59\n"
    "v_accvgpr_write_b32 a68, v48\nv_accvgpr_write_b32 a69, v49\nv_accvgpr_write_b32 a70, v50\nv_accvgpr_write_b32 a71, v51\nv_accvgpr_read_b32 v48, a72\n"
    "v_accvgpr_read_b32 v49, a73\nv_accvgpr_read_b32 v50, a74\nv_accvgpr_read_b32 v51, a75\nv_lshlrev_b32 v52, 16, v48\nv_and_b32 v53, s84, v48\n"
    "v_lshlrev_b32 v54, 16, v49\nv_and_b32 v55, s84, v49\nv_lshlrev_b32 v56, 16, v50\nv_and_b32 v57, s84, v50\nv_lshlrev_b32 v58, 16, v51\n"
    "v_and_b32 v59, s84, v51\nv_pk_mul_f32 v[52:53], v[52:53], v[160:161]\nv_pk_mul_f32 v[54:55], v[54:55], v[162:163]\n"
    "v_pk_mul_f32 v[56:57], v[56:57], v[164:165]\nv_pk_mul_f32 v[58:59], v[58:59], v[166:167]\nv_cvt_pk_bf16_f32 v48, v52, v53\n"
    "v_cvt_pk_bf16_f32 v49, v54, v55\nv_cvt_pk_bf16_f32 v50, v56, v57\nv_cvt_pk_bf16_f32 v51, v58, v59\nv_accvgpr_write_b32 a72, v48\n"
    "v_accvgpr_write_b32 a73, v49\nv_accvgpr_write_b32 a74, v50\nv_accvgpr_write_b32 a75, v51\nv_accvgpr_read_b32 v48, a76\nv_accvgpr_read_b32 v49, a77\n"
    "v_accvgpr_read_b32 v50, a78\nv_accvgpr_read_b32 v51, a79\nv_lshlrev_b32 v52, 16, v48\nv_and_b32 v53, s84, v48\nv_lshlrev_b32 v54, 16, v49\n"
    "v_and_b32 v55, s84, v49\nv_lshlrev_b32 v56, 16, v50\nv_and_b32 v57, s84, v50\nv_lshlrev_b32 v58, 16, v51\nv_and_b32 v59, s84, v51\n"
    "v_pk_mul_f32 v[52:53], v[52:53], v[160:161]\nv_pk_mul_f32 v[54:55], v[54:55], v[162:163]\nv_pk_mul_f32 v[56:57], v[56:57], v[164:165]\n"
    "v_pk_mul_f32 v[58:59], v[58:59], v[166:167]\nv_cvt_pk_bf16_f32 v48, v52, v53\nv_cvt_pk_bf16_f32 v49, v54, v55\nv_cvt_pk_bf16_f32 v50, v56, v57\n"
    "v_cvt_pk_bf16_f32 v51, v58, v59\nv_accvgpr_write_b32 a76, v48\nv_accvgpr_write_b32 a77, v49\nv_accvgpr_write_b32 a78, v50\n"
    "v_accvgpr_write_b32 a79, v51\nv_accvgpr_read_b32 v48, a80\nv_accvgpr_read_b32 v49, a81\nv_accvgpr_read_b32 v50, a82\nv_accvgpr_read_b32 v51, a83\n"
    "v_lshlrev_b32 v52, 16, v48\nv_and_b32 v53, s84, v48\nv_lshlrev_b32 v54, 16, v49\nv_and_b32 v55, s84, v49\nv_lshlrev_b32 v56, 16, v50\n"
    "v_and_b32 v57, s84, v50\nv_lshlrev_b32 v58, 16, v51\nv_and_b32 v59, s84, v51\nv_pk_mul_f32 v[52:53], v[52:53], v[168:169]\n"
    "v_pk_mul_f32 v[54:55], v[54:55], v[170:171]\nv_pk_mul_f32 v[56:57], v[56:57], v[172:173]\nv_pk_mul_f32 v[58:59], v[58:59], v[174:175]\n"
    "v_cvt_pk_bf16_f32 v48, v52, v53\nv_cvt_pk_bf16_f32 v49, v54, v55\nv_cvt_pk_bf16_f32 v50, v56, v57\nv_cvt_pk_bf16_f32 v51, v58, v59\n"
    "v_accvgpr_write_b32 a80, v48\nv_accvgpr_write_b32 a81, v49\nv_accvgpr_write_b32 a82, v50\nv_accvgpr_write_b32 a83, v51\nv_accvgpr_read_b32 v48, a84\n"
    "v_accvgpr_read_b32 v49, a85\nv_accvgpr_read_b32 v50, a86\nv_accvgpr_read_b32 v51, a87\nv_lshlrev_b32 v52, 16, v48\nv_and_b32 v53, s84, v48\n"
    "v_lshlrev_b32 v54, 16, v49\nv_and_b32 v55, s84, v49\nv_lshlrev_b32 v56, 16, v50\nv_and_b32 v57, s84, v50\nv_lshlrev_b32 v58, 16, v51\n"
    "v_and_b32 v59, s84, v51\nv_pk_mul_f32 v[52:53], v[52:53], v[168:169]\nv_pk_mul_f32 v[54:55], v[54:55], v[170:171]\n"
    "v_pk_mul_f32 v[56:57], v[56:57], v[172:173]\nv_pk_mul_f32 v[58:59], v[58:59], v[174:175]\nv_cvt_pk_bf16_f32 v48, v52, v53\n"
    "v_cvt_pk_bf16_f32 v49, v54, v55\nv_cvt_pk_bf16_f32 v50, v56, v57\nv_cvt_pk_bf16_f32 v51, v58, v59\nv_accvgpr_write_b32 a84, v48\n"
    "v_accvgpr_write_b32 a85, v49\nv_accvgpr_write_b32 a86, v50\nv_accvgpr_write_b32 a87, v51\nv_accvgpr_read_b32 v48, a88\nv_accvgpr_read_b32 v49, a89\n"
    "v_accvgpr_read_b32 v50, a90\nv_accvgpr_read_b32 v51, a91\nv_lshlrev_b32 v52, 16, v48\nv_and_b32 v53, s84, v48\nv_lshlrev_b32 v54, 16, v49\n"
    "v_and_b32 v55, s84, v49\nv_lshlrev_b32 v56, 16, v50\nv_and_b32 v57, s84, v50\nv_lshlrev_b32 v58, 16, v51\nv_and_b32 v59, s84, v51\n"
    "v_pk_mul_f32 v[52:53], v[52:53], v[168:169]\nv_pk_mul_f32 v[54:55], v[54:55], v[170:171]\nv_pk_mul_f32 v[56:57], v[56:57], v[172:173]\n"
    "v_pk_mul_f32 v[58:59], v[58:59], v[174:175]\nv_cvt_pk_bf16_f32 v48, v52, v53\nv_cvt_pk_bf16_f32 v49, v54, v55\nv_cvt_pk_bf16_f32 v50, v56, v57\n"
    "v_cvt_pk_bf16_f32 v51, v58, v59\nv_accvgpr_write_b32 a88, v48\nv_accvgpr_write_b32 a89, v49\nv_accvgpr_write_b32 a90, v50\n"
    "v_accvgpr_write_b32 a91, v51\nv_accvgpr_read_b32 v48, a92\nv_accvgpr_read_b32 v49, a93\nv_accvgpr_read_b32 v50, a94\nv_accvgpr_read_b32 v51, a95\n"
    "v_lshlrev_b32 v52, 16, v48\nv_and_b32 v53, s84, v48\nv_lshlrev_b32 v54, 16, v49\nv_and_b32 v55, s84, v49\nv_lshlrev_b32 v56, 16, v50\n"
    "v_and_b32 v57, s84, v50\nv_lshlrev_b32 v58, 16, v51\nv_and_b32 v59, s84, v51\nv_pk_mul_f32 v[52:53], v[52:53], v[168:169]\n"
    "v_pk_mul_f32 v[54:55], v[54:55], v[170:171]\nv_pk_mul_f32 v[56:57], v[56:57], v[172:173]\nv_pk_mul_f32 v[58:59], v[58:59], v[174:175]\n"
    "v_cvt_pk_bf16_f32 v48, v52, v53\nv_cvt_pk_bf16_f32 v49, v54, v55\nv_cvt_pk_bf16_f32 v50, v56, v57\nv_cvt_pk_bf16_f32 v51, v58, v59\n"
    "v_accvgpr_write_b32 a92, v48\nv_accvgpr_write_b32 a93, v49\nv_accvgpr_write_b32 a94, v50\nv_accvgpr_write_b32 a95, v51\nv_accvgpr_read_b32 v48, a96\n"
    "v_accvgpr_read_b32 v49, a97\nv_accvgpr_read_b32 v50, a98\nv_accvgpr_read_b32 v51, a99\nv_lshlrev_b32 v52, 16, v48\nv_and_b32 v53, s84, v48\n"
    "v_lshlrev_b32 v54, 16, v49\nv_and_b32 v55, s84, v49\nv_lshlrev_b32 v56, 16, v50\nv_and_b32 v57, s84, v50\nv_lshlrev_b32 v58, 16, v51\n"
    "v_and_b32 v59, s84, v51\nv_pk_mul_f32 v[52:53], v[52:53], v[176:177]\nv_pk_mul_f32 v[54:55], v[54:55], v[178:179]\n"
    "v_pk_mul_f32 v[56:57], v[56:57], v[180:181]\nv_pk_mul_f32 v[58:59], v[58:59], v[182:183]\nv_cvt_pk_bf16_f32 v48, v52, v53\n"
    "v_cvt_pk_bf16_f32 v49, v54, v55\nv_cvt_pk_bf16_f32 v50, v56, v57\nv_cvt_pk_bf16_f32 v51, v58, v59\nv_accvgpr_write_b32 a96, v48\n"
    "v_accvgpr_write_b32 a97, v49\nv_accvgpr_write_b32 a98, v50\nv_accvgpr_write_b32 a99, v51\nv_accvgpr_read_b32 v48, a100\nv_accvgpr_read_b32 v49, a101\n"
    "v_accvgpr_read_b32 v50, a102\nv_accvgpr_read_b32 v51, a103\nv_lshlrev_b32 v52, 16, v48\nv_and_b32 v53, s84, v48\nv_lshlrev_b32 v54, 16, v49\n"
    "v_and_b32 v55, s84, v49\nv_lshlrev_b32 v56, 16, v50\nv_and_b32 v57, s84, v50\nv_lshlrev_b32 v58, 16, v51\nv_and_b32 v59, s84, v51\n"
    "v_pk_mul_f32 v[52:53], v[52:53], v[176:177]\nv_pk_mul_f32 v[54:55], v[54:55], v[178:179]\nv_pk_mul_f32 v[56:57], v[56:57], v[180:181]\n"
    "v_pk_mul_f32 v[58:59], v[58:59], v[182:183]\nv_cvt_pk_bf16_f32 v48, v52, v53\nv_cvt_pk_bf16_f32 v49, v54, v55\nv_cvt_pk_bf16_f32 v50, v56, v57\n"
    "v_cvt_pk_bf16_f32 v51, v58, v59\nv_accvgpr_write_b32 a100, v48\nv_accvgpr_write_b32 a101, v49\nv_accvgpr_write_b32 a102, v50\n"
    "v_accvgpr_write_b32 a103, v51\nv_accvgpr_read_b32 v48, a104\nv_accvgpr_read_b32 v49, a105\nv_accvgpr_read_b32 v50, a106\nv_accvgpr_read_b32 v51, a107\n"
    "v_lshlrev_b32 v52, 16, v48\nv_and_b32 v53, s84, v48\nv_lshlrev_b32 v54, 16, v49\nv_and_b32 v55, s84, v49\nv_lshlrev_b32 v56, 16, v50\n"
    "v_and_b32 v57, s84, v50\nv_lshlrev_b32 v58, 16, v51\nv_and_b32 v59, s84, v51\nv_pk_mul_f32 v[52:53], v[52:53], v[176:177]\n"
    "v_pk_mul_f32 v[54:55], v[54:55], v[178:179]\nv_pk_mul_f32 v[56:57], v[56:57], v[180:181]\nv_pk_mul_f32 v[58:59], v[58:59], v[182:183]\n"
    "v_cvt_pk_bf16_f32 v48, v52, v53\nv_cvt_pk_bf16_f32 v49, v54, v55\nv_cvt_pk_bf16_f32 v50, v56, v57\nv_cvt_pk_bf16_f32 v51, v58, v59\n"
    "v_accvgpr_write_b32 a104, v48\nv_accvgpr_write_b32 a105, v49\nv_accvgpr_write_b32 a106, v50\nv_accvgpr_write_b32 a107, v51\n"
    "v_accvgpr_read_b32 v48, a108\nv_accvgpr_read_b32 v49, a109\nv_accvgpr_read_b32 v50, a110\nv_accvgpr_read_b32 v51, a111\nv_lshlrev_b32 v52, 16, v48\n"
    "v_and_b32 v53, s84, v48\nv_lshlrev_b32 v54, 16, v49\nv_and_b32 v55, s84, v49\nv_lshlrev_b32 v56, 16, v50\nv_and_b32 v57, s84, v50\n"
    "v_lshlrev_b32 v58, 16, v51\nv_and_b32 v59, s84, v51\nv_pk_mul_f32 v[52:53], v[52:53], v[176:177]\nv_pk_mul_f32 v[54:55], v[54:55], v[178:179]\n"
    "v_pk_mul_f32 v[56:57], v[56:57], v[180:181]\nv_pk_mul_f32 v[58:59], v[58:59], v[182:183]\nv_cvt_pk_bf16_f32 v48, v52, v53\n"
    "v_cvt_pk_bf16_f32 v49, v54, v55\nv_cvt_pk_bf16_f32 v50, v56, v57\nv_cvt_pk_bf16_f32 v51, v58, v59\nv_accvgpr_write_b32 a108, v48\n"
    "v_accvgpr_write_b32 a109, v49\nv_accvgpr_write_b32 a110, v50\nv_accvgpr_write_b32 a111, v51\nv_accvgpr_read_b32 v48, a112\n"
    "v_accvgpr_read_b32 v49, a113\nv_accvgpr_read_b32 v50, a114\nv_accvgpr_read_b32 v51, a115\nv_lshlrev_b32 v52, 16, v48\nv_and_b32 v53, s84, v48\n"
    "v_lshlrev_b32 v54, 16, v49\nv_and_b32 v55, s84, v49\nv_lshlrev_b32 v56, 16, v50\nv_and_b32 v57, s84, v50\nv_lshlrev_b32 v58, 16, v51\n"
    "v_and_b32 v59, s84, v51\nv_pk_mul_f32 v[52:53], v[52:53], v[184:185]\nv_pk_mul_f32 v[54:55], v[54:55], v[186:187]\n"
    "v_pk_mul_f32 v[56:57], v[56:57], v[188:189]\nv_pk_mul_f32 v[58:59], v[58:59], v[190:191]\nv_cvt_pk_bf16_f32 v48, v52, v53\n"
    "v_cvt_pk_bf16_f32 v49, v54, v55\nv_cvt_pk_bf16_f32 v50, v56, v57\nv_cvt_pk_bf16_f32 v51, v58, v59\nv_accvgpr_write_b32 a112, v48\n"
    "v_accvgpr_write_b32 a113, v49\nv_accvgpr_write_b32 a114, v50\nv_accvgpr_write_b32 a115, v51\nv_accvgpr_read_b32 v48, a116\n"
    "v_accvgpr_read_b32 v49, a117\nv_accvgpr_read_b32 v50, a118\nv_accvgpr_read_b32 v51, a119\nv_lshlrev_b32 v52, 16, v48\nv_and_b32 v53, s84, v48\n"
    "v_lshlrev_b32 v54, 16, v49\nv_and_b32 v55, s84, v49\nv_lshlrev_b32 v56, 16, v50\nv_and_b32 v57, s84, v50\nv_lshlrev_b32 v58, 16, v51\n"
    "v_and_b32 v59, s84, v51\nv_pk_mul_f32 v[52:53], v[52:53], v[184:185]\nv_pk_mul_f32 v[54:55], v[54:55], v[186:187]\n"
    "v_pk_mul_f32 v[56:57], v[56:57], v[188:189]\nv_pk_mul_f32 v[58:59], v[58:59], v[190:191]\nv_cvt_pk_bf16_f32 v48, v52, v53\n"
    "v_cvt_pk_bf16_f32 v49, v54, v55\nv_cvt_pk_bf16_f32 v50, v56, v57\nv_cvt_pk_bf16_f32 v51, v58, v59\nv_accvgpr_write_b32 a116, v48\n"
    "v_accvgpr_write_b32 a117, v49\nv_accvgpr_write_b32 a118, v50\nv_accvgpr_write_b32 a119, v51\nv_accvgpr_read_b32 v48, a120\n"
    "v_accvgpr_read_b32 v49, a121\nv_accvgpr_read_b32 v50, a122\nv_accvgpr_read_b32 v51, a123\nv_lshlrev_b32 v52, 16, v48\nv_and_b32 v53, s84, v48\n"
    "v_lshlrev_b32 v54, 16, v49\nv_and_b32 v55, s84, v49\nv_lshlrev_b32 v56, 16, v50\nv_and_b32 v57, s84, v50\nv_lshlrev_b32 v58, 16, v51\n"
    "v_and_b32 v59, s84, v51\nv_pk_mul_f32 v[52:53], v[52:53], v[184:185]\nv_pk_mul_f32 v[54:55], v[54:55], v[186:187]\n"
    "v_pk_mul_f32 v[56:57], v[56:57], v[188:189]\nv_pk_mul_f32 v[58:59], v[58:59], v[190:191]\nv_cvt_pk_bf16_f32 v48, v52, v53\n"
    "v_cvt_pk_bf16_f32 v49, v54, v55\nv_cvt_pk_bf16_f32 v50, v56, v57\nv_cvt_pk_bf16_f32 v51, v58, v59\nv_accvgpr_write_b32 a120, v48\n"
    "v_accvgpr_write_b32 a121, v49\nv_accvgpr_write_b32 a122, v50\nv_accvgpr_write_b32 a123, v51\nv_accvgpr_read_b32 v48, a124\n"
    "v_accvgpr_read_b32 v49, a125\nv_accvgpr_read_b32 v50, a126\nv_accvgpr_read_b32 v51, a127\nv_lshlrev_b32 v52, 16, v48\nv_and_b32 v53, s84, v48\n"
    "v_lshlrev_b32 v54, 16, v49\nv_and_b32 v55, s84, v49\nv_lshlrev_b32 v56, 16, v50\nv_and_b32 v57, s84, v50\nv_lshlrev_b32 v58, 16, v51\n"
    "v_and_b32 v59, s84, v51\nv_pk_mul_f32 v[52:53], v[52:53], v[184:185]\nv_pk_mul_f32 v[54:55], v[54:55], v[186:187]\n"
    "v_pk_mul_f32 v[56:57], v[56:57], v[188:189]\nv_pk_mul_f32 v[58:59], v[58:59], v[190:191]\nv_cvt_pk_bf16_f32 v48, v52, v53\n"
    "v_cvt_pk_bf16_f32 v49, v54, v55\nv_cvt_pk_bf16_f32 v50, v56, v57\nv_cvt_pk_bf16_f32 v51, v58, v59\nv_accvgpr_write_b32 a124, v48\n"
    "v_accvgpr_write_b32 a125, v49\nv_accvgpr_write_b32 a126, v50\nv_accvgpr_write_b32 a127, v51\ns_waitcnt vmcnt(0)\nv_accvgpr_read_b32 v48, a128\n"
    "v_accvgpr_read_b32 v49, a129\nv_accvgpr_read_b32 v50, a130\nv_accvgpr_read_b32 v51, a131\nv_lshlrev_b32 v52, 16, v48\nv_and_b32 v53, s84, v48\n"
    "v_lshlrev_b32 v54, 16, v49\nv_and_b32 v55, s84, v49\nv_lshlrev_b32 v56, 16, v50\nv_and_b32 v57, s84, v50\nv_lshlrev_b32 v58, 16, v51\n"
    "v_and_b32 v59, s84, v51\nv_pk_mul_f32 v[52:53], v[52:53], v[64:65]\nv_pk_mul_f32 v[54:55], v[54:55], v[66:67]\n"
    "v_pk_mul_f32 v[56:57], v[56:57], v[68:69]\nv_pk_mul_f32 v[58:59], v[58:59], v[70:71]\nv_cvt_pk_bf16_f32 v48, v52, v53\nv_cvt_pk_bf16_f32 v49, v54, v55\n"
    "v_cvt_pk_bf16_f32 v50, v56, v57\nv_cvt_pk_bf16_f32 v51, v58, v59\nv_accvgpr_write_b32 a128, v48\nv_accvgpr_write_b32 a129, v49\n"
    "v_accvgpr_write_b32 a130, v50\nv_accvgpr_write_b32 a131, v51\nv_accvgpr_read_b32 v48, a132\nv_accvgpr_read_b32 v49, a133\nv_accvgpr_read_b32 v50, a134\n"
    "v_accvgpr_read_b32 v51, a135\nv_lshlrev_b32 v52, 16, v48\nv_and_b32 v53, s84, v48\nv_lshlrev_b32 v54, 16, v49\nv_and_b32 v55, s84, v49\n"
    "v_lshlrev_b32 v56, 16, v50\nv_and_b32 v57, s84, v50\nv_lshlrev_b32 v58, 16, v51\nv_and_b32 v59, s84, v51\nv_pk_mul_f32 v[52:53], v[52:53], v[64:65]\n"
    "v_pk_mul_f32 v[54:55], v[54:55], v[66:67]\nv_pk_mul_f32 v[56:57], v[56:57], v[68:69]\nv_pk_mul_f32 v[58:59], v[58:59], v[70:71]\n"
    "v_cvt_pk_bf16_f32 v48, v52, v53\nv_cvt_pk_bf16_f32 v49, v54, v55\nv_cvt_pk_bf16_f32 v50, v56, v57\nv_cvt_pk_bf16_f32 v51, v58, v59\n"
    "v_accvgpr_write_b32 a132, v48\nv_accvgpr_write_b32 a133, v49\nv_accvgpr_write_b32 a134, v50\nv_accvgpr_write_b32 a135, v51\n"
    "v_accvgpr_read_b32 v48, a136\nv_accvgpr_read_b32 v49, a137\nv_accvgpr_read_b32 v50, a138\nv_accvgpr_read_b32 v51, a139\nv_lshlrev_b32 v52, 16, v48\n"
    "v_and_b32 v53, s84, v48\nv_lshlrev_b32 v54, 16, v49\nv_and_b32 v55, s84, v49\nv_lshlrev_b32 v56, 16, v50\nv_and_b32 v57, s84, v50\n"
    "v_lshlrev_b32 v58, 16, v51\nv_and_b32 v59, s84, v51\nv_pk_mul_f32 v[52:53], v[52:53], v[64:65]\nv_pk_mul_f32 v[54:55], v[54:55], v[66:67]\n"
    "v_pk_mul_f32 v[56:57], v[56:57], v[68:69]\nv_pk_mul_f32 v[58:59], v[58:59], v[70:71]\nv_cvt_pk_bf16_f32 v48, v52, v53\nv_cvt_pk_bf16_f32 v49, v54, v55\n"
    "v_cvt_pk_bf16_f32 v50, v56, v57\nv_cvt_pk_bf16_f32 v51, v58, v59\nv_accvgpr_write_b32 a136, v48\nv_accvgpr_write_b32 a137, v49\n"
    "v_accvgpr_write_b32 a138, v50\nv_accvgpr_write_b32 a139, v51\nv_accvgpr_read_b32 v48, a140\nv_accvgpr_read_b32 v49, a141\nv_accvgpr_read_b32 v50, a142\n"
    "v_accvgpr_read_b32 v51, a143\nv_lshlrev_b32 v52, 16, v48\nv_and_b32 v53, s84, v48\nv_lshlrev_b32 v54, 16, v49\nv_and_b32 v55, s84, v49\n"
    "v_lshlrev_b32 v56, 16, v50\nv_and_b32 v57, s84, v50\nv_lshlrev_b32 v58, 16, v51\nv_and_b32 v59, s84, v51\nv_pk_mul_f32 v[52:53], v[52:53], v[64:65]\n"
    "v_pk_mul_f32 v[54:55], v[54:55], v[66:67]\nv_pk_mul_f32 v[56:57], v[56:57], v[68:69]\nv_pk_mul_f32 v[58:59], v[58:59], v[70:71]\n"
    "v_cvt_pk_bf16_f32 v48, v52, v53\nv_cvt_pk_bf16_f32 v49, v54, v55\nv_cvt_pk_bf16_f32 v50, v56, v57\nv_cvt_pk_bf16_f32 v51, v58, v59\n"
    "v_accvgpr_write_b32 a140, v48\nv_accvgpr_write_b32 a141, v49\nv_accvgpr_write_b32 a142, v50\nv_accvgpr_write_b32 a143, v51\n"
    "v_accvgpr_read_b32 v48, a144\nv_accvgpr_read_b32 v49, a145\nv_accvgpr_read_b32 v50, a146\nv_accvgpr_read_b32 v51, a147\nv_lshlrev_b32 v52, 16, v48\n"
    "v_and_b32 v53, s84, v48\nv_lshlrev_b32 v54, 16, v49\nv_and_b32 v55, s84, v49\nv_lshlrev_b32 v56, 16, v50\nv_and_b32 v57, s84, v50\n"
    "v_lshlrev_b32 v58, 16, v51\nv_and_b32 v59, s84, v51\nv_pk_mul_f32 v[52:53], v[52:53], v[72:73]\nv_pk_mul_f32 v[54:55], v[54:55], v[74:75]\n"
    "v_pk_mul_f32 v[56:57], v[56:57], v[76:77]\nv_pk_mul_f32 v[58:59], v[58:59], v[78:79]\nv_cvt_pk_bf16_f32 v48, v52, v53\nv_cvt_pk_bf16_f32 v49, v54, v55\n"
    "v_cvt_pk_bf16_f32 v50, v56, v57\nv_cvt_pk_bf16_f32 v51, v58, v59\nv_accvgpr_write_b32 a144, v48\nv_accvgpr_write_b32 a145, v49\n"
    "v_accvgpr_write_b32 a146, v50\nv_accvgpr_write_b32 a147, v51\nv_accvgpr_read_b32 v48, a148\nv_accvgpr_read_b32 v49, a149\nv_accvgpr_read_b32 v50, a150\n"
    "v_accvgpr_read_b32 v51, a151\nv_lshlrev_b32 v52, 16, v48\nv_and_b32 v53, s84, v48\nv_lshlrev_b32 v54, 16, v49\nv_and_b32 v55, s84, v49\n"
    "v_lshlrev_b32 v56, 16, v50\nv_and_b32 v57, s84, v50\nv_lshlrev_b32 v58, 16, v51\nv_and_b32 v59, s84, v51\nv_pk_mul_f32 v[52:53], v[52:53], v[72:73]\n"
    "v_pk_mul_f32 v[54:55], v[54:55], v[74:75]\nv_pk_mul_f32 v[56:57], v[56:57], v[76:77]\nv_pk_mul_f32 v[58:59], v[58:59], v[78:79]\n"
    "v_cvt_pk_bf16_f32 v48, v52, v53\nv_cvt_pk_bf16_f32 v49, v54, v55\nv_cvt_pk_bf16_f32 v50, v56, v57\nv_cvt_pk_bf16_f32 v51, v58, v59\n"
    "v_accvgpr_write_b32 a148, v48\nv_accvgpr_write_b32 a149, v49\nv_accvgpr_write_b32 a150, v50\nv_accvgpr_write_b32 a151, v51\n"
    "v_accvgpr_read_b32 v48, a152\nv_accvgpr_read_b32 v49, a153\nv_accvgpr_read_b32 v50, a154\nv_accvgpr_read_b32 v51, a155\nv_lshlrev_b32 v52, 16, v48\n"
    "v_and_b32 v53, s84, v48\nv_lshlrev_b32 v54, 16, v49\nv_and_b32 v55, s84, v49\nv_lshlrev_b32 v56, 16, v50\nv_and_b32 v57, s84, v50\n"
    "v_lshlrev_b32 v58, 16, v51\nv_and_b32 v59, s84, v51\nv_pk_mul_f32 v[52:53], v[52:53], v[72:73]\nv_pk_mul_f32 v[54:55], v[54:55], v[74:75]\n"
    "v_pk_mul_f32 v[56:57], v[56:57], v[76:77]\nv_pk_mul_f32 v[58:59], v[58:59], v[78:79]\nv_cvt_pk_bf16_f32 v48, v52, v53\nv_cvt_pk_bf16_f32 v49, v54, v55\n"
    "v_cvt_pk_bf16_f32 v50, v56, v57\nv_cvt_pk_bf16_f32 v51, v58, v59\nv_accvgpr_write_b32 a152, v48\nv_accvgpr_write_b32 a153, v49\n"
    "v_accvgpr_write_b32 a154, v50\nv_accvgpr_write_b32 a155, v51\nv_accvgpr_read_b32 v48, a156\nv_accvgpr_read_b32 v49, a157\nv_accvgpr_read_b32 v50, a158\n"
    "v_accvgpr_read_b32 v51, a159\nv_lshlrev_b32 v52, 16, v48\nv_and_b32 v53, s84, v48\nv_lshlrev_b32 v54, 16, v49\nv_and_b32 v55, s84, v49\n"
    "v_lshlrev_b32 v56, 16, v50\nv_and_b32 v57, s84, v50\nv_lshlrev_b32 v58, 16, v51\nv_and_b32 v59, s84, v51\nv_pk_mul_f32 v[52:53], v[52:53], v[72:73]\n"
    "v_pk_mul_f32 v[54:55], v[54:55], v[74:75]\nv_pk_mul_f32 v[56:57], v[56:57], v[76:77]\nv_pk_mul_f32 v[58:59], v[58:59], v[78:79]\n"
    "v_cvt_pk_bf16_f32 v48, v52, v53\nv_cvt_pk_bf16_f32 v49, v54, v55\nv_cvt_pk_bf16_f32 v50, v56, v57\nv_cvt_pk_bf16_f32 v51, v58, v59\n"
    "v_accvgpr_write_b32 a156, v48\nv_accvgpr_write_b32 a157, v49\nv_accvgpr_write_b32 a158, v50\nv_accvgpr_write_b32 a159, v51\n"
    "v_accvgpr_read_b32 v48, a160\nv_accvgpr_read_b32 v49, a161\nv_accvgpr_read_b32 v50, a162\nv_accvgpr_read_b32 v51, a163\nv_lshlrev_b32 v52, 16, v48\n"
    "v_and_b32 v53, s84, v48\nv_lshlrev_b32 v54, 16, v49\nv_and_b32 v55, s84, v49\nv_lshlrev_b32 v56, 16, v50\nv_and_b32 v57, s84, v50\n"
    "v_lshlrev_b32 v58, 16, v51\nv_and_b32 v59, s84, v51\nv_pk_mul_f32 v[52:53], v[52:53], v[80:81]\nv_pk_mul_f32 v[54:55], v[54:55], v[82:83]\n"
    "v_pk_mul_f32 v[56:57], v[56:57], v[84:85]\nv_pk_mul_f32 v[58:59], v[58:59], v[86:87]\nv_cvt_pk_bf16_f32 v48, v52, v53\nv_cvt_pk_bf16_f32 v49, v54, v55\n"
    "v_cvt_pk_bf16_f32 v50, v56, v57\nv_cvt_pk_bf16_f32 v51, v58, v59\nv_accvgpr_write_b32 a160, v48\nv_accvgpr_write_b32 a161, v49\n"
    "v_accvgpr_write_b32 a162, v50\nv_accvgpr_write_b32 a163, v51\nv_accvgpr_read_b32 v48, a164\nv_accvgpr_read_b32 v49, a165\nv_accvgpr_read_b32 v50, a166\n"
    "v_accvgpr_read_b32 v51, a167\nv_lshlrev_b32 v52, 16, v48\nv_and_b32 v53, s84, v48\nv_lshlrev_b32 v54, 16, v49\nv_and_b32 v55, s84, v49\n"
    "v_lshlrev_b32 v56, 16, v50\nv_and_b32 v57, s84, v50\nv_lshlrev_b32 v58, 16, v51\nv_and_b32 v59, s84, v51\nv_pk_mul_f32 v[52:53], v[52:53], v[80:81]\n"
    "v_pk_mul_f32 v[54:55], v[54:55], v[82:83]\nv_pk_mul_f32 v[56:57], v[56:57], v[84:85]\nv_pk_mul_f32 v[58:59], v[58:59], v[86:87]\n"
    "v_cvt_pk_bf16_f32 v48, v52, v53\nv_cvt_pk_bf16_f32 v49, v54, v55\nv_cvt_pk_bf16_f32 v50, v56, v57\nv_cvt_pk_bf16_f32 v51, v58, v59\n"
    "v_accvgpr_write_b32 a164, v48\nv_accvgpr_write_b32 a165, v49\nv_accvgpr_write_b32 a166, v50\nv_accvgpr_write_b32 a167, v51\n"
    "v_accvgpr_read_b32 v48, a168\nv_accvgpr_read_b32 v49, a169\nv_accvgpr_read_b32 v50, a170\nv_accvgpr_read_b32 v51, a171\nv_lshlrev_b32 v52, 16, v48\n"
    "v_and_b32 v53, s84, v48\nv_lshlrev_b32 v54, 16, v49\nv_and_b32 v55, s84, v49\nv_lshlrev_b32 v56, 16, v50\nv_and_b32 v57, s84, v50\n"
    "v_lshlrev_b32 v58, 16, v51\nv_and_b32 v59, s84, v51\nv_pk_mul_f32 v[52:53], v[52:53], v[80:81]\nv_pk_mul_f32 v[54:55], v[54:55], v[82:83]\n"
    "v_pk_mul_f32 v[56:57], v[56:57], v[84:85]\nv_pk_mul_f32 v[58:59], v[58:59], v[86:87]\nv_cvt_pk_bf16_f32 v48, v52, v53\nv_cvt_pk_bf16_f32 v49, v54, v55\n"
    "v_cvt_pk_bf16_f32 v50, v56, v57\nv_cvt_pk_bf16_f32 v51, v58, v59\nv_accvgpr_write_b32 a168, v48\nv_accvgpr_write_b32 a169, v49\n"
    "v_accvgpr_write_b32 a170, v50\nv_accvgpr_write_b32 a171, v51\nv_accvgpr_read_b32 v48, a172\nv_accvgpr_read_b32 v49, a173\nv_accvgpr_read_b32 v50, a174\n"
    "v_accvgpr_read_b32 v51, a175\nv_lshlrev_b32 v52, 16, v48\nv_and_b32 v53, s84, v48\nv_lshlrev_b32 v54, 16, v49\nv_and_b32 v55, s84, v49\n"
    "v_lshlrev_b32 v56, 16, v50\nv_and_b32 v57, s84, v50\nv_lshlrev_b32 v58, 16, v51\nv_and_b32 v59, s84, v51\nv_pk_mul_f32 v[52:53], v[52:53], v[80:81]\n"
    "v_pk_mul_f32 v[54:55], v[54:55], v[82:83]\nv_pk_mul_f32 v[56:57], v[56:57], v[84:85]\nv_pk_mul_f32 v[58:59], v[58:59], v[86:87]\n"
    "v_cvt_pk_bf16_f32 v48, v52, v53\nv_cvt_pk_bf16_f32 v49, v54, v55\nv_cvt_pk_bf16_f32 v50, v56, v57\nv_cvt_pk_bf16_f32 v51, v58, v59\n"
    "v_accvgpr_write_b32 a172, v48\nv_accvgpr_write_b32 a173, v49\nv_accvgpr_write_b32 a174, v50\nv_accvgpr_write_b32 a175, v51\n"
    "v_accvgpr_read_b32 v48, a176\nv_accvgpr_read_b32 v49, a177\nv_accvgpr_read_b32 v50, a178\nv_accvgpr_read_b32 v51, a179\nv_lshlrev_b32 v52, 16, v48\n"
    "v_and_b32 v53, s84, v48\nv_lshlrev_b32 v54, 16, v49\nv_and_b32 v55, s84, v49\nv_lshlrev_b32 v56, 16, v50\nv_and_b32 v57, s84, v50\n"
    "v_lshlrev_b32 v58, 16, v51\nv_and_b32 v59, s84, v51\nv_pk_mul_f32 v[52:53], v[52:53], v[88:89]\nv_pk_mul_f32 v[54:55], v[54:55], v[90:91]\n"
    "v_pk_mul_f32 v[56:57], v[56:57], v[92:93]\nv_pk_mul_f32 v[58:59], v[58:59], v[94:95]\nv_cvt_pk_bf16_f32 v48, v52, v53\nv_cvt_pk_bf16_f32 v49, v54, v55\n"
    "v_cvt_pk_bf16_f32 v50, v56, v57\nv_cvt_pk_bf16_f32 v51, v58, v59\nv_accvgpr_write_b32 a176, v48\nv_accvgpr_write_b32 a177, v49\n"
    "v_accvgpr_write_b32 a178, v50\nv_accvgpr_write_b32 a179, v51\nv_accvgpr_read_b32 v48, a180\nv_accvgpr_read_b32 v49, a181\nv_accvgpr_read_b32 v50, a182\n"
    "v_accvgpr_read_b32 v51, a183\nv_lshlrev_b32 v52, 16, v48\nv_and_b32 v53, s84, v48\nv_lshlrev_b32 v54, 16, v49\nv_and_b32 v55, s84, v49\n"
    "v_lshlrev_b32 v56, 16, v50\nv_and_b32 v57, s84, v50\nv_lshlrev_b32 v58, 16, v51\nv_and_b32 v59, s84, v51\nv_pk_mul_f32 v[52:53], v[52:53], v[88:89]\n"
    "v_pk_mul_f32 v[54:55], v[54:55], v[90:91]\nv_pk_mul_f32 v[56:57], v[56:57], v[92:93]\nv_pk_mul_f32 v[58:59], v[58:59], v[94:95]\n"
    "v_cvt_pk_bf16_f32 v48, v52, v53\nv_cvt_pk_bf16_f32 v49, v54, v55\nv_cvt_pk_bf16_f32 v50, v56, v57\nv_cvt_pk_bf16_f32 v51, v58, v59\n"
    "v_accvgpr_write_b32 a180, v48\nv_accvgpr_write_b32 a181, v49\nv_accvgpr_write_b32 a182, v50\nv_accvgpr_write_b32 a183, v51\n"
    "v_accvgpr_read_b32 v48, a184\nv_accvgpr_read_b32 v49, a185\nv_accvgpr_read_b32 v50, a186\nv_accvgpr_read_b32 v51, a187\nv_lshlrev_b32 v52, 16, v48\n"
    "v_and_b32 v53, s84, v48\nv_lshlrev_b32 v54, 16, v49\nv_and_b32 v55, s84, v49\nv_lshlrev_b32 v56, 16, v50\nv_and_b32 v57, s84, v50\n"
    "v_lshlrev_b32 v58, 16, v51\nv_and_b32 v59, s84, v51\nv_pk_mul_f32 v[52:53], v[52:53], v[88:89]\nv_pk_mul_f32 v[54:55], v[54:55], v[90:91]\n"
    "v_pk_mul_f32 v[56:57], v[56:57], v[92:93]\nv_pk_mul_f32 v[58:59], v[58:59], v[94:95]\nv_cvt_pk_bf16_f32 v48, v52, v53\nv_cvt_pk_bf16_f32 v49, v54, v55\n"
    "v_cvt_pk_bf16_f32 v50, v56, v57\nv_cvt_pk_bf16_f32 v51, v58, v59\nv_accvgpr_write_b32 a184, v48\nv_accvgpr_write_b32 a185, v49\n"
    "v_accvgpr_write_b32 a186, v50\nv_accvgpr_write_b32 a187, v51\nv_accvgpr_read_b32 v48, a188\nv_accvgpr_read_b32 v49, a189\nv_accvgpr_read_b32 v50, a190\n"
    "v_accvgpr_read_b32 v51, a191\nv_lshlrev_b32 v52, 16, v48\nv_and_b32 v53, s84, v48\nv_lshlrev_b32 v54, 16, v49\nv_and_b32 v55, s84, v49\n"
    "v_lshlrev_b32 v56, 16, v50\nv_and_b32 v57, s84, v50\nv_lshlrev_b32 v58, 16, v51\nv_and_b32 v59, s84, v51\nv_pk_mul_f32 v[52:53], v[52:53], v[88:89]\n"
    "v_pk_mul_f32 v[54:55], v[54:55], v[90:91]\nv_pk_mul_f32 v[56:57], v[56:57], v[92:93]\nv_pk_mul_f32 v[58:59], v[58:59], v[94:95]\n"
    "v_cvt_pk_bf16_f32 v48, v52, v53\nv_cvt_pk_bf16_f32 v49, v54, v55\nv_cvt_pk_bf16_f32 v50, v56, v57\nv_cvt_pk_bf16_f32 v51, v58, v59\n"
    "v_accvgpr_write_b32 a188, v48\nv_accvgpr_write_b32 a189, v49\nv_accvgpr_write_b32 a190, v50\nv_accvgpr_write_b32 a191, v51\n"
    "v_accvgpr_read_b32 v48, a192\nv_accvgpr_read_b32 v49, a193\nv_accvgpr_read_b32 v50, a194\nv_accvgpr_read_b32 v51, a195\nv_lshlrev_b32 v52, 16, v48\n"
    "v_and_b32 v53, s84, v48\nv_lshlrev_b32 v54, 16, v49\nv_and_b32 v55, s84, v49\nv_lshlrev_b32 v56, 16, v50\nv_and_b32 v57, s84, v50\n"
    "v_lshlrev_b32 v58, 16, v51\nv_and_b32 v59, s84, v51\nv_pk_mul_f32 v[52:53], v[52:53], v[96:97]\nv_pk_mul_f32 v[54:55], v[54:55], v[98:99]\n"
    "v_pk_mul_f32 v[56:57], v[56:57], v[100:101]\nv_pk_mul_f32 v[58:59], v[58:59], v[102:103]\nv_cvt_pk_bf16_f32 v48, v52, v53\n"
    "v_cvt_pk_bf16_f32 v49, v54, v55\nv_cvt_pk_bf16_f32 v50, v56, v57\nv_cvt_pk_bf16_f32 v51, v58, v59\nv_accvgpr_write_b32 a192, v48\n"
    "v_accvgpr_write_b32 a193, v49\nv_accvgpr_write_b32 a194, v50\nv_accvgpr_write_b32 a195, v51\nv_accvgpr_read_b32 v48, a196\n"
    "v_accvgpr_read_b32 v49, a197\nv_accvgpr_read_b32 v50, a198\nv_accvgpr_read_b32 v51, a199\nv_lshlrev_b32 v52, 16, v48\nv_and_b32 v53, s84, v48\n"
    "v_lshlrev_b32 v54, 16, v49\nv_and_b32 v55, s84, v49\nv_lshlrev_b32 v56, 16, v50\nv_and_b32 v57, s84, v50\nv_lshlrev_b32 v58, 16, v51\n"
    "v_and_b32 v59, s84, v51\nv_pk_mul_f32 v[52:53], v[52:53], v[96:97]\nv_pk_mul_f32 v[54:55], v[54:55], v[98:99]\n"
    "v_pk_mul_f32 v[56:57], v[56:57], v[100:101]\nv_pk_mul_f32 v[58:59], v[58:59], v[102:103]\nv_cvt_pk_bf16_f32 v48, v52, v53\n"
    "v_cvt_pk_bf16_f32 v49, v54, v55\nv_cvt_pk_bf16_f32 v50, v56, v57\nv_cvt_pk_bf16_f32 v51, v58, v59\nv_accvgpr_write_b32 a196, v48\n"
    "v_accvgpr_write_b32 a197, v49\nv_accvgpr_write_b32 a198, v50\nv_accvgpr_write_b32 a199, v51\nv_accvgpr_read_b32 v48, a200\n"
    "v_accvgpr_read_b32 v49, a201\nv_accvgpr_read_b32 v50, a202\nv_accvgpr_read_b32 v51, a203\nv_lshlrev_b32 v52, 16, v48\nv_and_b32 v53, s84, v48\n"
    "v_lshlrev_b32 v54, 16, v49\nv_and_b32 v55, s84, v49\nv_lshlrev_b32 v56, 16, v50\nv_and_b32 v57, s84, v50\nv_lshlrev_b32 v58, 16, v51\n"
    "v_and_b32 v59, s84, v51\nv_pk_mul_f32 v[52:53], v[52:53], v[96:97]\nv_pk_mul_f32 v[54:55], v[54:55], v[98:99]\n"
    "v_pk_mul_f32 v[56:57], v[56:57], v[100:101]\nv_pk_mul_f32 v[58:59], v[58:59], v[102:103]\nv_cvt_pk_bf16_f32 v48, v52, v53\n"
    "v_cvt_pk_bf16_f32 v49, v54, v55\nv_cvt_pk_bf16_f32 v50, v56, v57\nv_cvt_pk_bf16_f32 v51, v58, v59\nv_accvgpr_write_b32 a200, v48\n"
    "v_accvgpr_write_b32 a201, v49\nv_accvgpr_write_b32 a202, v50\nv_accvgpr_write_b32 a203, v51\nv_accvgpr_read_b32 v48, a204\n"
    "v_accvgpr_read_b32 v49, a205\nv_accvgpr_read_b32 v50, a206\nv_accvgpr_read_b32 v51, a207\nv_lshlrev_b32 v52, 16, v48\nv_and_b32 v53, s84, v48\n"
    "v_lshlrev_b32 v54, 16, v49\nv_and_b32 v55, s84, v49\nv_lshlrev_b32 v56, 16, v50\nv_and_b32 v57, s84, v50\nv_lshlrev_b32 v58, 16, v51\n"
    "v_and_b32 v59, s84, v51\nv_pk_mul_f32 v[52:53], v[52:53], v[96:97]\nv_pk_mul_f32 v[54:55], v[54:55], v[98:99]\n"
    "v_pk_mul_f32 v[56:57], v[56:57], v[100:101]\nv_pk_mul_f32 v[58:59], v[58:59], v[102:103]\nv_cvt_pk_bf16_f32 v48, v52, v53\n"
    "v_cvt_pk_bf16_f32 v49, v54, v55\nv_cvt_pk_bf16_f32 v50, v56, v57\nv_cvt_pk_bf16_f32 v51, v58, v59\nv_accvgpr_write_b32 a204, v48\n"
    "v_accvgpr_write_b32 a205, v49\nv_accvgpr_write_b32 a206, v50\nv_accvgpr_write_b32 a207, v51\nv_accvgpr_read_b32 v48, a208\n"
    "v_accvgpr_read_b32 v49, a209\nv_accvgpr_read_b32 v50, a210\nv_accvgpr_read_b32 v51, a211\nv_lshlrev_b32 v52, 16, v48\nv_and_b32 v53, s84, v48\n"
    "v_lshlrev_b32 v54, 16, v49\nv_and_b32 v55, s84, v49\nv_lshlrev_b32 v56, 16, v50\nv_and_b32 v57, s84, v50\nv_lshlrev_b32 v58, 16, v51\n"
    "v_and_b32 v59, s84, v51\nv_pk_mul_f32 v[52:53], v[52:53], v[104:105]\nv_pk_mul_f32 v[54:55], v[54:55], v[106:107]\n"
    "v_pk_mul_f32 v[56:57], v[56:57], v[108:109]\nv_pk_mul_f32 v[58:59], v[58:59], v[110:111]\nv_cvt_pk_bf16_f32 v48, v52, v53\n"
    "v_cvt_pk_bf16_f32 v49, v54, v55\nv_cvt_pk_bf16_f32 v50, v56, v57\nv_cvt_pk_bf16_f32 v51, v58, v59\nv_accvgpr_write_b32 a208, v48\n"
    "v_accvgpr_write_b32 a209, v49\nv_accvgpr_write_b32 a210, v50\nv_accvgpr_write_b32 a211, v51\nv_accvgpr_read_b32 v48, a212\n"
    "v_accvgpr_read_b32 v49, a213\nv_accvgpr_read_b32 v50, a214\nv_accvgpr_read_b32 v51, a215\nv_lshlrev_b32 v52, 16, v48\nv_and_b32 v53, s84, v48\n"
    "v_lshlrev_b32 v54, 16, v49\nv_and_b32 v55, s84, v49\nv_lshlrev_b32 v56, 16, v50\nv_and_b32 v57, s84, v50\nv_lshlrev_b32 v58, 16, v51\n"
    "v_and_b32 v59, s84, v51\nv_pk_mul_f32 v[52:53], v[52:53], v[104:105]\nv_pk_mul_f32 v[54:55], v[54:55], v[106:107]\n"
    "v_pk_mul_f32 v[56:57], v[56:57], v[108:109]\nv_pk_mul_f32 v[58:59], v[58:59], v[110:111]\nv_cvt_pk_bf16_f32 v48, v52, v53\n"
    "v_cvt_pk_bf16_f32 v49, v54, v55\nv_cvt_pk_bf16_f32 v50, v56, v57\nv_cvt_pk_bf16_f32 v51, v58, v59\nv_accvgpr_write_b32 a212, v48\n"
    "v_accvgpr_write_b32 a213, v49\nv_accvgpr_write_b32 a214, v50\nv_accvgpr_write_b32 a215, v51\nv_accvgpr_read_b32 v48, a216\n"
    "v_accvgpr_read_b32 v49, a217\nv_accvgpr_read_b32 v50, a218\nv_accvgpr_read_b32 v51, a219\nv_lshlrev_b32 v52, 16, v48\nv_and_b32 v53, s84, v48\n"
    "v_lshlrev_b32 v54, 16, v49\nv_and_b32 v55, s84, v49\nv_lshlrev_b32 v56, 16, v50\nv_and_b32 v57, s84, v50\nv_lshlrev_b32 v58, 16, v51\n"
    "v_and_b32 v59, s84, v51\nv_pk_mul_f32 v[52:53], v[52:53], v[104:105]\nv_pk_mul_f32 v[54:55], v[54:55], v[106:107]\n"
    "v_pk_mul_f32 v[56:57], v[56:57], v[108:109]\nv_pk_mul_f32 v[58:59], v[58:59], v[110:111]\nv_cvt_pk_bf16_f32 v48, v52, v53\n"
    "v_cvt_pk_bf16_f32 v49, v54, v55\nv_cvt_pk_bf16_f32 v50, v56, v57\nv_cvt_pk_bf16_f32 v51, v58, v59\nv_accvgpr_write_b32 a216, v48\n"
    "v_accvgpr_write_b32 a217, v49\nv_accvgpr_write_b32 a218, v50\nv_accvgpr_write_b32 a219, v51\nv_accvgpr_read_b32 v48, a220\n"
    "v_accvgpr_read_b32 v49, a221\nv_accvgpr_read_b32 v50, a222\nv_accvgpr_read_b32 v51, a223\nv_lshlrev_b32 v52, 16, v48\nv_and_b32 v53, s84, v48\n"
    "v_lshlrev_b32 v54, 16, v49\nv_and_b32 v55, s84, v49\nv_lshlrev_b32 v56, 16, v50\nv_and_b32 v57, s84, v50\nv_lshlrev_b32 v58, 16, v51\n"
    "v_and_b32 v59, s84, v51\nv_pk_mul_f32 v[52:53], v[52:53], v[104:105]\nv_pk_mul_f32 v[54:55], v[54:55], v[106:107]\n"
    "v_pk_mul_f32 v[56:57], v[56:57], v[108:109]\nv_pk_mul_f32 v[58:59], v[58:59], v[110:111]\nv_cvt_pk_bf16_f32 v48, v52, v53\n"
    "v_cvt_pk_bf16_f32 v49, v54, v55\nv_cvt_pk_bf16_f32 v50, v56, v57\nv_cvt_pk_bf16_f32 v51, v58, v59\nv_accvgpr_write_b32 a220, v48\n"
    "v_accvgpr_write_b32 a221, v49\nv_accvgpr_write_b32 a222, v50\nv_accvgpr_write_b32 a223, v51\nv_accvgpr_read_b32 v48, a224\n"
    "v_accvgpr_read_b32 v49, a225\nv_accvgpr_read_b32 v50, a226\nv_accvgpr_read_b32 v51, a227\nv_lshlrev_b32 v52, 16, v48\nv_and_b32 v53, s84, v48\n"
    "v_lshlrev_b32 v54, 16, v49\nv_and_b32 v55, s84, v49\nv_lshlrev_b32 v56, 16, v50\nv_and_b32 v57, s84, v50\nv_lshlrev_b32 v58, 16, v51\n"
    "v_and_b32 v59, s84, v51\nv_pk_mul_f32 v[52:53], v[52:53], v[112:113]\nv_pk_mul_f32 v[54:55], v[54:55], v[114:115]\n"
    "v_pk_mul_f32 v[56:57], v[56:57], v[116:117]\nv_pk_mul_f32 v[58:59], v[58:59], v[118:119]\nv_cvt_pk_bf16_f32 v48, v52, v53\n"
    "v_cvt_pk_bf16_f32 v49, v54, v55\nv_cvt_pk_bf16_f32 v50, v56, v57\nv_cvt_pk_bf16_f32 v51, v58, v59\nv_accvgpr_write_b32 a224, v48\n"
    "v_accvgpr_write_b32 a225, v49\nv_accvgpr_write_b32 a226, v50\nv_accvgpr_write_b32 a227, v51\nv_accvgpr_read_b32 v48, a228\n"
    "v_accvgpr_read_b32 v49, a229\nv_accvgpr_read_b32 v50, a230\nv_accvgpr_read_b32 v51, a231\nv_lshlrev_b32 v52, 16, v48\nv_and_b32 v53, s84, v48\n"
    "v_lshlrev_b32 v54, 16, v49\nv_and_b32 v55, s84, v49\nv_lshlrev_b32 v56, 16, v50\nv_and_b32 v57, s84, v50\nv_lshlrev_b32 v58, 16, v51\n"
    "v_and_b32 v59, s84, v51\nv_pk_mul_f32 v[52:53], v[52:53], v[112:113]\nv_pk_mul_f32 v[54:55], v[54:55], v[114:115]\n"
    "v_pk_mul_f32 v[56:57], v[56:57], v[116:117]\nv_pk_mul_f32 v[58:59], v[58:59], v[118:119]\nv_cvt_pk_bf16_f32 v48, v52, v53\n"
    "v_cvt_pk_bf16_f32 v49, v54, v55\nv_cvt_pk_bf16_f32 v50, v56, v57\nv_cvt_pk_bf16_f32 v51, v58, v59\nv_accvgpr_write_b32 a228, v48\n"
    "v_accvgpr_write_b32 a229, v49\nv_accvgpr_write_b32 a230, v50\nv_accvgpr_write_b32 a231, v51\nv_accvgpr_read_b32 v48, a232\n"
    "v_accvgpr_read_b32 v49, a233\nv_accvgpr_read_b32 v50, a234\nv_accvgpr_read_b32 v51, a235\nv_lshlrev_b32 v52, 16, v48\nv_and_b32 v53, s84, v48\n"
    "v_lshlrev_b32 v54, 16, v49\nv_and_b32 v55, s84, v49\nv_lshlrev_b32 v56, 16, v50\nv_and_b32 v57, s84, v50\nv_lshlrev_b32 v58, 16, v51\n"
    "v_and_b32 v59, s84, v51\nv_pk_mul_f32 v[52:53], v[52:53], v[112:113]\nv_pk_mul_f32 v[54:55], v[54:55], v[114:115]\n"
    "v_pk_mul_f32 v[56:57], v[56:57], v[116:117]\nv_pk_mul_f32 v[58:59], v[58:59], v[118:119]\nv_cvt_pk_bf16_f32 v48, v52, v53\n"
    "v_cvt_pk_bf16_f32 v49, v54, v55\nv_cvt_pk_bf16_f32 v50, v56, v57\nv_cvt_pk_bf16_f32 v51, v58, v59\nv_accvgpr_write_b32 a232, v48\n"
    "v_accvgpr_write_b32 a233, v49\nv_accvgpr_write_b32 a234, v50\nv_accvgpr_write_b32 a235, v51\nv_accvgpr_read_b32 v48, a236\n"
    "v_accvgpr_read_b32 v49, a237\nv_accvgpr_read_b32 v50, a238\nv_accvgpr_read_b32 v51, a239\nv_lshlrev_b32 v52, 16, v48\nv_and_b32 v53, s84, v48\n"
    "v_lshlrev_b32 v54, 16, v49\nv_and_b32 v55, s84, v49\nv_lshlrev_b32 v56, 16, v50\nv_and_b32 v57, s84, v50\nv_lshlrev_b32 v58, 16, v51\n"
    "v_and_b32 v59, s84, v51\nv_pk_mul_f32 v[52:53], v[52:53], v[112:113]\nv_pk_mul_f32 v[54:55], v[54:55], v[114:115]\n"
    "v_pk_mul_f32 v[56:57], v[56:57], v[116:117]\nv_pk_mul_f32 v[58:59], v[58:59], v[118:119]\nv_cvt_pk_bf16_f32 v48, v52, v53\n"
    "v_cvt_pk_bf16_f32 v49, v54, v55\nv_cvt_pk_bf16_f32 v50, v56, v57\nv_cvt_pk_bf16_f32 v51, v58, v59\nv_accvgpr_write_b32 a236, v48\n"
    "v_accvgpr_write_b32 a237, v49\nv_accvgpr_write_b32 a238, v50\nv_accvgpr_write_b32 a239, v51\nv_accvgpr_read_b32 v48, a240\n"
    "v_accvgpr_read_b32 v49, a241\nv_accvgpr_read_b32 v50, a242\nv_accvgpr_read_b32 v51, a243\nv_lshlrev_b32 v52, 16, v48\nv_and_b32 v53, s84, v48\n"
    "v_lshlrev_b32 v54, 16, v49\nv_and_b32 v55, s84, v49\nv_lshlrev_b32 v56, 16, v50\nv_and_b32 v57, s84, v50\nv_lshlrev_b32 v58, 16, v51\n"
    "v_and_b32 v59, s84, v51\nv_pk_mul_f32 v[52:53], v[52:53], v[120:121]\nv_pk_mul_f32 v[54:55], v[54:55], v[122:123]\n"
    "v_pk_mul_f32 v[56:57], v[56:57], v[124:125]\nv_pk_mul_f32 v[58:59], v[58:59], v[126:127]\nv_cvt_pk_bf16_f32 v48, v52, v53\n"
    "v_cvt_pk_bf16_f32 v49, v54, v55\nv_cvt_pk_bf16_f32 v50, v56, v57\nv_cvt_pk_bf16_f32 v51, v58, v59\nv_accvgpr_write_b32 a240, v48\n"
    "v_accvgpr_write_b32 a241, v49\nv_accvgpr_write_b32 a242, v50\nv_accvgpr_write_b32 a243, v51\nv_accvgpr_read_b32 v48, a244\n"
    "v_accvgpr_read_b32 v49, a245\nv_accvgpr_read_b32 v50, a246\nv_accvgpr_read_b32 v51, a247\nv_lshlrev_b32 v52, 16, v48\nv_and_b32 v53, s84, v48\n"
    "v_lshlrev_b32 v54, 16, v49\nv_and_b32 v55, s84, v49\nv_lshlrev_b32 v56, 16, v50\nv_and_b32 v57, s84, v50\nv_lshlrev_b32 v58, 16, v51\n"
    "v_and_b32 v59, s84, v51\nv_pk_mul_f32 v[52:53], v[52:53], v[120:121]\nv_pk_mul_f32 v[54:55], v[54:55], v[122:123]\n"
    "v_pk_mul_f32 v[56:57], v[56:57], v[124:125]\nv_pk_mul_f32 v[58:59], v[58:59], v[126:127]\nv_cvt_pk_bf16_f32 v48, v52, v53\n"
    "v_cvt_pk_bf16_f32 v49, v54, v55\nv_cvt_pk_bf16_f32 v50, v56, v57\nv_cvt_pk_bf16_f32 v51, v58, v59\nv_accvgpr_write_b32 a244, v48\n"
    "v_accvgpr_write_b32 a245, v49\nv_accvgpr_write_b32 a246, v50\nv_accvgpr_write_b32 a247, v51\nv_accvgpr_read_b32 v48, a248\n"
    "v_accvgpr_read_b32 v49, a249\nv_accvgpr_read_b32 v50, a250\nv_accvgpr_read_b32 v51, a251\nv_lshlrev_b32 v52, 16, v48\nv_and_b32 v53, s84, v48\n"
    "v_lshlrev_b32 v54, 16, v49\nv_and_b32 v55, s84, v49\nv_lshlrev_b32 v56, 16, v50\nv_and_b32 v57, s84, v50\nv_lshlrev_b32 v58, 16, v51\n"
    "v_and_b32 v59, s84, v51\nv_pk_mul_f32 v[52:53], v[52:53], v[120:121]\nv_pk_mul_f32 v[54:55], v[54:55], v[122:123]\n"
    "v_pk_mul_f32 v[56:57], v[56:57], v[124:125]\nv_pk_mul_f32 v[58:59], v[58:59], v[126:127]\nv_cvt_pk_bf16_f32 v48, v52, v53\n"
    "v_cvt_pk_bf16_f32 v49, v54, v55\nv_cvt_pk_bf16_f32 v50, v56, v57\nv_cvt_pk_bf16_f32 v51, v58, v59\nv_accvgpr_write_b32 a248, v48\n"
    "v_accvgpr_write_b32 a249, v49\nv_accvgpr_write_b32 a250, v50\nv_accvgpr_write_b32 a251, v51\nv_accvgpr_read_b32 v48, a252\n"
    "v_accvgpr_read_b32 v49, a253\nv_accvgpr_read_b32 v50, a254\nv_accvgpr_read_b32 v51, a255\nv_lshlrev_b32 v52, 16, v48\nv_and_b32 v53, s84, v48\n"
    "v_lshlrev_b32 v54, 16, v49\nv_and_b32 v55, s84, v49\nv_lshlrev_b32 v56, 16, v50\nv_and_b32 v57, s84, v50\nv_lshlrev_b32 v58, 16, v51\n"
    "v_and_b32 v59, s84, v51\nv_pk_mul_f32 v[52:53], v[52:53], v[120:121]\nv_pk_mul_f32 v[54:55], v[54:55], v[122:123]\n"
    "v_pk_mul_f32 v[56:57], v[56:57], v[124:125]\nv_pk_mul_f32 v[58:59], v[58:59], v[126:127]\nv_cvt_pk_bf16_f32 v48, v52, v53\n"
    "v_cvt_pk_bf16_f32 v49, v54, v55\nv_cvt_pk_bf16_f32 v50, v56, v57\nv_cvt_pk_bf16_f32 v51, v58, v59\nv_accvgpr_write_b32 a252, v48\n"
    "v_accvgpr_write_b32 a253, v49\nv_accvgpr_write_b32 a254, v50\nv_accvgpr_write_b32 a255, v51\nv_mov_b32 v64, 0x3f803f80\nv_mov_b32 v65, 0x3f803f80\n"
    "v_mov_b32 v66, 0x3f803f80\nv_mov_b32 v67, 0x3f803f80\ns_nop 1\nv_mfma_f32_16x16x32_bf16 v[16:19], a[0:3], v[64:67], 0\n"
    "v_mfma_f32_16x16x32_bf16 v[20:23], a[4:7], v[64:67], 0\nv_mfma_f32_16x16x32_bf16 v[24:27], a[8:11], v[64:67], 0\n"
    "v_mfma_f32_16x16x32_bf16 v[28:31], a[12:15], v[64:67], 0\nv_mfma_f32_16x16x32_bf16 v[16:19], a[16:19], v[64:67], v[16:19]\n"
    "v_mfma_f32_16x16x32_bf16 v[20:23], a[20:23], v[64:67], v[20:23]\nv_mfma_f32_16x16x32_bf16 v[24:27], a[24:27], v[64:67], v[24:27]\n"
    "v_mfma_f32_16x16x32_bf16 v[28:31], a[28:31], v[64:67], v[28:31]\nv_mfma_f32_16x16x32_bf16 v[16:19], a[32:35], v[64:67], v[16:19]\n"
    "v_mfma_f32_16x16x32_bf16 v[20:23], a[36:39], v[64:67], v[20:23]\nv_mfma_f32_16x16x32_bf16 v[24:27], a[40:43], v[64:67], v[24:27]\n"
    "v_mfma_f32_16x16x32_bf16 v[28:31], a[44:47], v[64:67], v[28:31]\nv_mfma_f32_16x16x32_bf16 v[16:19], a[48:51], v[64:67], v[16:19]\n"
    "v_mfma_f32_16x16x32_bf16 v[20:23], a[52:55], v[64:67], v[20:23]\nv_mfma_f32_16x16x32_bf16 v[24:27], a[56:59], v[64:67], v[24:27]\n"
    "v_mfma_f32_16x16x32_bf16 v[28:31], a[60:63], v[64:67], v[28:31]\nv_mfma_f32_16x16x32_bf16 v[16:19], a[64:67], v[64:67], v[16:19]\n"
    "v_mfma_f32_16x16x32_bf16 v[20:23], a[68:71], v[64:67], v[20:23]\nv_mfma_f32_16x16x32_bf16 v[24:27], a[72:75], v[64:67], v[24:27]\n"
    "v_mfma_f32_16x16x32_bf16 v[28:31], a[76:79], v[64:67], v[28:31]\nv_mfma_f32_16x16x32_bf16 v[16:19], a[80:83], v[64:67], v[16:19]\n"
    "v_mfma_f32_16x16x32_bf16 v[20:23], a[84:87], v[64:67], v[20:23]\nv_mfma_f32_16x16x32_bf16 v[24:27], a[88:91], v[64:67], v[24:27]\n"
    "v_mfma_f32_16x16x32_bf16 v[28:31], a[92:95], v[64:67], v[28:31]\nv_mfma_f32_16x16x32_bf16 v[16:19], a[96:99], v[64:67], v[16:19]\n"
    "v_mfma_f32_16x16x32_bf16 v[20:23], a[100:103], v[64:67], v[20:23]\nv_mfma_f32_16x16x32_bf16 v[24:27], a[104:107], v[64:67], v[24:27]\n"
    "v_mfma_f32_16x16x32_bf16 v[28:31], a[108:111], v[64:67], v[28:31]\nv_mfma_f32_16x16x32_bf16 v[16:19], a[112:115], v[64:67], v[16:19]\n"
    "v_mfma_f32_16x16x32_bf16 v[20:23], a[116:119], v[64:67], v[20:23]\nv_mfma_f32_16x16x32_bf16 v[24:27], a[120:123], v[64:67], v[24:27]\n"
    "v_mfma_f32_16x16x32_bf16 v[28:31], a[124:127], v[64:67], v[28:31]\nv_mfma_f32_16x16x32_bf16 v[16:19], a[128:131], v[64:67], v[16:19]\n"
    "v_mfma_f32_16x16x32_bf16 v[20:23], a[132:135], v[64:67], v[20:23]\nv_mfma_f32_16x16x32_bf16 v[24:27], a[136:139], v[64:67], v[24:27]\n"
    "v_mfma_f32_16x16x32_bf16 v[28:31], a[140:143], v[64:67], v[28:31]\nv_mfma_f32_16x16x32_bf16 v[16:19], a[144:147], v[64:67], v[16:19]\n"
    "v_mfma_f32_16x16x32_bf16 v[20:23], a[148:151], v[64:67], v[20:23]\nv_mfma_f32_16x16x32_bf16 v[24:27], a[152:155], v[64:67], v[24:27]\n"
    "v_mfma_f32_16x16x32_bf16 v[28:31], a[156:159], v[64:67], v[28:31]\nv_mfma_f32_16x16x32_bf16 v[16:19], a[160:163], v[64:67], v[16:19]\n"
    "v_mfma_f32_16x16x32_bf16 v[20:23], a[164:167], v[64:67], v[20:23]\nv_mfma_f32_16x16x32_bf16 v[24:27], a[168:171], v[64:67], v[24:27]\n"
    "v_mfma_f32_16x16x32_bf16 v[28:31], a[172:175], v[64:67], v[28:31]\nv_mfma_f32_16x16x32_bf16 v[16:19], a[176:179], v[64:67], v[16:19]\n"
    "v_mfma_f32_16x16x32_bf16 v[20:23], a[180:183], v[64:67], v[20:23]\nv_mfma_f32_16x16x32_bf16 v[24:27], a[184:187], v[64:67], v[24:27]\n"
    "v_mfma_f32_16x16x32_bf16 v[28:31], a[188:191], v[64:67], v[28:31]\nv_mfma_f32_16x16x32_bf16 v[16:19], a[192:195], v[64:67], v[16:19]\n"
    "v_mfma_f32_16x16x32_bf16 v[20:23], a[196:199], v[64:67], v[20:23]\nv_mfma_f32_16x16x32_bf16 v[24:27], a[200:203], v[64:67], v[24:27]\n"
    "v_mfma_f32_16x16x32_bf16 v[28:31], a[204:207], v[64:67], v[28:31]\nv_mfma_f32_16x16x32_bf16 v[16:19], a[208:211], v[64:67], v[16:19]\n"
    "v_mfma_f32_16x16x32_bf16 v[20:23], a[212:215], v[64:67], v[20:23]\nv_mfma_f32_16x16x32_bf16 v[24:27], a[216:219], v[64:67], v[24:27]\n"
    "v_mfma_f32_16x16x32_bf16 v[28:31], a[220:223], v[64:67], v[28:31]\nv_mfma_f32_16x16x32_bf16 v[16:19], a[224:227], v[64:67], v[16:19]\n"
    "v_mfma_f32_16x16x32_bf16 v[20:23], a[228:231], v[64:67], v[20:23]\nv_mfma_f32_16x16x32_bf16 v[24:27], a[232:235], v[64:67], v[24:27]\n"
    "v_mfma_f32_16x16x32_bf16 v[28:31], a[236:239], v[64:67], v[28:31]\nv_mfma_f32_16x16x32_bf16 v[16:19], a[240:243], v[64:67], v[16:19]\n"
    "v_mfma_f32_16x16x32_bf16 v[20:23], a[244:247], v[64:67], v[20:23]\nv_mfma_f32_16x16x32_bf16 v[24:27], a[248:251], v[64:67], v[24:27]\n"
    "v_mfma_f32_16x16x32_bf16 v[28:31], a[252:255], v[64:67], v[28:31]\ns_nop 7\n"
    : "=&{v[16:31]}"(cs), "=&{v[32:47]}"(bi) : [rc] "s"(rc), [vsb] "v"(vsb), [vc1] "v"(vc1)
    : "memory", "scc", CLS, CLV_48_191, CLA_0_255);
}
DI void x2_loadb(rsrc_t rc, unsigned sB, unsigned vlc) {
  asm volatile(
    "s_nop 4\nv_add_u32 v64, 0, %[vlc]\nv_and_b32 v64, 0x1fff, v64\nv_add_u32 v65, 1024, %[vlc]\nv_and_b32 v65, 0x1fff, v65\nv_add_u32 v66, 2048, %[vlc]\n"
    "v_and_b32 v66, 0x1fff, v66\nv_add_u32 v67, 3072, %[vlc]\nv_and_b32 v67, 0x1fff, v67\nv_add_u32 v68, 4096, %[vlc]\nv_and_b32 v68, 0x1fff, v68\n"
    "v_add_u32 v69, 5120, %[vlc]\nv_and_b32 v69, 0x1fff, v69\nv_add_u32 v70, 6144, %[vlc]\nv_and_b32 v70, 0x1fff, v70\nv_add_u32 v71, 7168, %[vlc]\n"
    "v_and_b32 v71, 0x1fff, v71\ns_add_u32 s84, %[sB], 65536\ns_add_u32 s85, %[sB], 131072\ns_add_u32 s86, %[sB], 196608\n"
    "buffer_load_dwordx4 a[0:3], v64, %[rc], %[sB] offen sc1\nbuffer_load_dwordx4 a[4:7], v64, %[rc], s84 offen sc1\n"
    "buffer_load_dwordx4 a[8:11], v64, %[rc], s85 offen sc1\nbuffer_load_dwordx4 a[12:15], v64, %[rc], s86 offen sc1\n"
    "buffer_load_dwordx4 a[16:19], v65, %[rc], %[sB] offen sc1\nbuffer_load_dwordx4 a[20:23], v65, %[rc], s84 offen sc1\n"
    "buffer_load_dwordx4 a[24:27], v65, %[rc], s85 offen sc1\nbuffer_load_dwordx4 a[28:31], v65, %[rc], s86 offen sc1\n"
    "buffer_load_dwordx4 a[32:35], v66, %[rc], %[sB] offen sc1\nbuffer_load_dwordx4 a[36:39], v66, %[rc], s84 offen sc1\n"
    "buffer_load_dwordx4 a[40:43], v66, %[rc], s85 offen sc1\nbuffer_load_dwordx4 a[44:47], v66, %[rc], s86 offen sc1\n"
    "buffer_load_dwordx4 a[48:51], v67, %[rc], %[sB] offen sc1\nbuffer_load_dwordx4 a[52:55], v67, %[rc], s84 offen sc1\n"
    "buffer_load_dwordx4 a[56:59], v67, %[rc], s85 offen sc1\nbuffer_load_dwordx4 a[60:63], v67, %[rc], s86 offen sc1\n"
    "buffer_load_dwordx4 a[64:67], v68, %[rc], %[sB] offen sc1\nbuffer_load_dwordx4 a[68:71], v68, %[rc], s84 offen sc1\n"
    "buffer_load_dwordx4 a[72:75], v68, %[rc], s85 offen sc1\nbuffer_load_dwordx4 a[76:79], v68, %[rc], s86 offen sc1\n"
    "buffer_load_dwordx4 a[80:83], v69, %[rc], %[sB] offen sc1\nbuffer_load_dwordx4 a[84:87], v69, %[rc], s84 offen sc1\n"
    "buffer_load_dwordx4 a[88:91], v69, %[rc], s85 offen sc1\nbuffer_load_dwordx4 a[92:95], v69, %[rc], s86 offen sc1\n"
    "buffer_load_dwordx4 a[96:99], v70, %[rc], %[sB] offen sc1\nbuffer_load_dwordx4 a[100:103], v70, %[rc], s84 offen sc1\n"
    "buffer_load_dwordx4 a[104:107], v70, %[rc], s85 offen sc1\nbuffer_load_dwordx4 a[108:111], v70, %[rc], s86 offen sc1\n"
    "buffer_load_dwordx4 a[112:115], v71, %[rc], %[sB] offen sc1\nbuffer_load_dwordx4 a[116:119], v71, %[rc], s84 offen sc1\n"
    "buffer_load_dwordx4 a[120:123], v71, %[rc], s85 offen sc1\nbuffer_load_dwordx4 a[124:127], v71, %[rc], s86 offen sc1\n"
    :: [rc] "s"(rc), [sB] "s"(sB), [vlc] "v"(vlc)
    : "memory", "scc", CLS, CLV_64_71, CLA_0_127);
}
DI void x2_main(rsrc_t rc, unsigned sP, unsigned sQ, unsigned sY, unsigned vl16, unsigned vlc, unsigned vlw) {
  asm volatile(
    "s_nop 4\nv_add_u32 v64, 0, %[vlc]\nv_and_b32 v64, 0x1fff, v64\nv_add_u32 v65, 1024, %[vlc]\nv_and_b32 v65, 0x1fff, v65\nv_add_u32 v66, 2048, %[vlc]\n"
    "v_and_b32 v66, 0x1fff, v66\nv_add_u32 v67, 3072, %[vlc]\nv_and_b32 v67, 0x1fff, v67\nv_add_u32 v68, 4096, %[vlc]\nv_and_b32 v68, 0x1fff, v68\n"
    "v_add_u32 v69, 5120, %[vlc]\nv_and_b32 v69, 0x1fff, v69\nv_add_u32 v70, 6144, %[vlc]\nv_and_b32 v70, 0x1fff, v70\nv_add_u32 v71, 7168, %[vlc]\n"
    "v_and_b32 v71, 0x1fff, v71\ns_add_u32 s85, %[sP], 81920\ns_add_u32 s87, %[sQ], 81920\nbuffer_load_dwordx4 v[96:99], v64, %[rc], %[sP] offen sc1\n"
    "buffer_load_dwordx4 v[100:103], v64, %[rc], s85 offen sc1\nbuffer_load_dwordx4 v[104:107], v65, %[rc], %[sP] offen sc1\n"
    "buffer_load_dwordx4 v[108:111], v65, %[rc], s85 offen sc1\nbuffer_load_dwordx4 v[112:115], v66, %[rc], %[sP] offen sc1\n"
    "buffer_load_dwordx4 v[116:119], v66, %[rc], s85 offen sc1\nbuffer_load_dwordx4 v[120:123], v67, %[rc], %[sP] offen sc1\n"
    "buffer_load_dwordx4 v[124:127], v67, %[rc], s85 offen sc1\nbuffer_load_dwordx4 v[128:131], v68, %[rc], %[sP] offen sc1\n"
    "buffer_load_dwordx4 v[132:135], v68, %[rc], s85 offen sc1\nbuffer_load_dwordx4 v[136:139], v69, %[rc], %[sP] offen sc1\n"
    "buffer_load_dwordx4 v[140:143], v69, %[rc], s85 offen sc1\nbuffer_load_dwordx4 v[144:147], v70, %[rc], %[sP] offen sc1\n"
    "buffer_load_dwordx4 v[148:151], v70, %[rc], s85 offen sc1\nbuffer_load_dwordx4 v[152:155], v71, %[rc], %[sP] offen sc1\n"
    "buffer_load_dwordx4 v[156:159], v71, %[rc], s85 offen sc1\ns_add_u32 s84, %[sY], 0\n"
    "buffer_load_dwordx4 a[128:131], %[vl16], %[rc], s84 offen offset:0 sc1\nbuffer_load_dwordx4 a[144:147], %[vl16], %[rc], s84 offen offset:1024 sc1\n"
    "s_add_u32 s84, %[sY], 16384\nbuffer_load_dwordx4 a[132:135], %[vl16], %[rc], s84 offen offset:0 sc1\n"
    "buffer_load_dwordx4 a[148:151], %[vl16], %[rc], s84 offen offset:1024 sc1\ns_add_u32 s84, %[sY], 32768\n"
    "buffer_load_dwordx4 a[136:139], %[vl16], %[rc], s84 offen offset:0 sc1\nbuffer_load_dwordx4 a[152:155], %[vl16], %[rc], s84 offen offset:1024 sc1\n"
    "s_add_u32 s84, %[sY], 49152\nbuffer_load_dwordx4 a[140:143], %[vl16], %[rc], s84 offen offset:0 sc1\n"
    "buffer_load_dwordx4 a[156:159], %[vl16], %[rc], s84 offen offset:1024 sc1\nbuffer_load_dwordx4 v[160:163], %[vl16], %[rc], %[sQ] offen offset:0 sc1\n"
    "buffer_load_dwordx4 v[164:167], %[vl16], %[rc], s87 offen offset:0 sc1\nbuffer_load_dwordx4 v[168:171], %[vl16], %[rc], %[sQ] offen offset:1024 sc1\n"
    "buffer_load_dwordx4 v[172:175], %[vl16], %[rc], s87 offen offset:1024 sc1\ns_waitcnt vmcnt(27)\n"
    "v_mfma_f32_16x16x32_bf16 v[176:179], a[0:3], v[96:99], 0\nv_mfma_f32_16x16x32_bf16 v[180:183], a[4:7], v[96:99], 0\n"
    "v_mfma_f32_16x16x32_bf16 v[184:187], a[8:11], v[96:99], 0\nv_mfma_f32_16x16x32_bf16 v[188:191], a[12:15], v[96:99], 0\ns_waitcnt vmcnt(26)\n"
    "v_mfma_f32_16x16x32_bf16 v[192:195], a[0:3], v[100:103], 0\nv_mfma_f32_16x16x32_bf16 v[196:199], a[4:7], v[100:103], 0\n"
    "v_mfma_f32_16x16x32_bf16 v[200:203], a[8:11], v[100:103], 0\nv_mfma_f32_16x16x32_bf16 v[204:207], a[12:15], v[100:103], 0\ns_waitcnt vmcnt(25)\n"
    "v_mfma_f32_16x16x32_bf16 v[176:179], a[16:19], v[104:107], v[176:179]\nv_mfma_f32_16x16x32_bf16 v[180:183], a[20:23], v[104:107], v[180:183]\n"
    "v_mfma_f32_16x16x32_bf16 v[184:187], a[24:27], v[104:107], v[184:187]\nv_mfma_f32_16x16x32_bf16 v[188:191], a[28:31], v[104:107], v[188:191]\n"
    "s_waitcnt vmcnt(24)\nv_mfma_f32_16x16x32_bf16 v[192:195], a[16:19], v[108:111], v[192:195]\n"
    "v_mfma_f32_16x16x32_bf16 v[196:199], a[20:23], v[108:111], v[196:199]\nv_mfma_f32_16x16x32_bf16 v[200:203], a[24:27], v[108:111], v[200:203]\n"
    "v_mfma_f32_16x16x32_bf16 v[204:207], a[28:31], v[108:111], v[204:207]\ns_waitcnt vmcnt(23)\n"
    "v_mfma_f32_16x16x32_bf16 v[176:179], a[32:35], v[112:115], v[176:179]\nv_mfma_f32_16x16x32_bf16 v[180:183], a[36:39], v[112:115], v[180:183]\n"
    "v_mfma_f32_16x16x32_bf16 v[184:187], a[40:43], v[112:115], v[184:187]\nv_mfma_f32_16x16x32_bf16 v[188:191], a[44:47], v[112:115], v[188:191]\n"
    "s_waitcnt vmcnt(22)\nv_mfma_f32_16x16x32_bf16 v[192:195], a[32:35], v[116:119], v[192:195]\n"
    "v_mfma_f32_16x16x32_bf16 v[196:199], a[36:39], v[116:119], v[196:199]\nv_mfma_f32_16x16x32_bf16 v[200:203], a[40:43], v[116:119], v[200:203]\n"
    "v_mfma_f32_16x16x32_bf16 v[204:207], a[44:47], v[116:119], v[204:207]\ns_waitcnt vmcnt(21)\n"
    "v_mfma_f32_16x16x32_bf16 v[176:179], a[48:51], v[120:123], v[176:179]\nv_mfma_f32_16x16x32_bf16 v[180:183], a[52:55], v[120:123], v[180:183]\n"
    "v_mfma_f32_16x16x32_bf16 v[184:187], a[56:59], v[120:123], v[184:187]\nv_mfma_f32_16x16x32_bf16 v[188:191], a[60:63], v[120:123], v[188:191]\n"
    "s_waitcnt vmcnt(20)\nv_mfma_f32_16x16x32_bf16 v[192:195], a[48:51], v[124:127], v[192:195]\n"
    "v_mfma_f32_16x16x32_bf16 v[196:199], a[52:55], v[124:127], v[196:199]\nv_mfma_f32_16x16x32_bf16 v[200:203], a[56:59], v[124:127], v[200:203]\n"
    "v_mfma_f32_16x16x32_bf16 v[204:207], a[60:63], v[124:127], v[204:207]\ns_waitcnt vmcnt(19)\n"
    "v_mfma_f32_16x16x32_bf16 v[176:179], a[64:67], v[128:131], v[176:179]\nv_mfma_f32_16x16x32_bf16 v[180:183], a[68:71], v[128:131], v[180:183]\n"
    "v_mfma_f32_16x16x32_bf16 v[184:187], a[72:75], v[128:131], v[184:187]\nv_mfma_f32_16x16x32_bf16 v[188:191], a[76:79], v[128:131], v[188:191]\n"
    "s_waitcnt vmcnt(18)\nv_mfma_f32_16x16x32_bf16 v[192:195], a[64:67], v[132:135], v[192:195]\n"
    "v_mfma_f32_16x16x32_bf16 v[196:199], a[68:71], v[132:135], v[196:199]\nv_mfma_f32_16x16x32_bf16 v[200:203], a[72:75], v[132:135], v[200:203]\n"
    "v_mfma_f32_16x16x32_bf16 v[204:207], a[76:79], v[132:135], v[204:207]\ns_waitcnt vmcnt(17)\n"
    "v_mfma_f32_16x16x32_bf16 v[176:179], a[80:83], v[136:139], v[176:179]\nv_mfma_f32_16x16x32_bf16 v[180:183], a[84:87], v[136:139], v[180:183]\n"
    "v_mfma_f32_16x16x32_bf16 v[184:187], a[88:91], v[136:139], v[184:187]\nv_mfma_f32_16x16x32_bf16 v[188:191], a[92:95], v[136:139], v[188:191]\n"
    "s_waitcnt vmcnt(16)\nv_mfma_f32_16x16x32_bf16 v[192:195], a[80:83], v[140:143], v[192:195]\n"
    "v_mfma_f32_16x16x32_bf16 v[196:199], a[84:87], v[140:143], v[196:199]\nv_mfma_f32_16x16x32_bf16 v[200:203], a[88:91], v[140:143], v[200:203]\n"
    "v_mfma_f32_16x16x32_bf16 v[204:207], a[92:95], v[140:143], v[204:207]\ns_waitcnt vmcnt(15)\n"
    "v_mfma_f32_16x16x32_bf16 v[176:179], a[96:99], v[144:147], v[176:179]\nv_mfma_f32_16x16x32_bf16 v[180:183], a[100:103], v[144:147], v[180:183]\n"
    "v_mfma_f32_16x16x32_bf16 v[184:187], a[104:107], v[144:147], v[184:187]\nv_mfma_f32_16x16x32_bf16 v[188:191], a[108:111], v[144:147], v[188:191]\n"
    "s_waitcnt vmcnt(14)\nv_mfma_f32_16x16x32_bf16 v[192:195], a[96:99], v[148:151], v[192:195]\n"
    "v_mfma_f32_16x16x32_bf16 v[196:199], a[100:103], v[148:151], v[196:199]\nv_mfma_f32_16x16x32_bf16 v[200:203], a[104:107], v[148:151], v[200:203]\n"
    "v_mfma_f32_16x16x32_bf16 v[204:207], a[108:111], v[148:151], v[204:207]\ns_waitcnt vmcnt(13)\n"
    "v_mfma_f32_16x16x32_bf16 v[176:179], a[112:115], v[152:155], v[176:179]\nv_mfma_f32_16x16x32_bf16 v[180:183], a[116:119], v[152:155], v[180:183]\n"
    "v_mfma_f32_16x16x32_bf16 v[184:187], a[120:123], v[152:155], v[184:187]\nv_mfma_f32_16x16x32_bf16 v[188:191], a[124:127], v[152:155], v[188:191]\n"
    "s_waitcnt vmcnt(12)\nv_mfma_f32_16x16x32_bf16 v[192:195], a[112:115], v[156:159], v[192:195]\n"
    "v_mfma_f32_16x16x32_bf16 v[196:199], a[116:119], v[156:159], v[196:199]\nv_mfma_f32_16x16x32_bf16 v[200:203], a[120:123], v[156:159], v[200:203]\n"
    "v_mfma_f32_16x16x32_bf16 v[204:207], a[124:127], v[156:159], v[204:207]\ns_waitcnt vmcnt(3)\n"
    "v_mfma_f32_16x16x32_bf16 v[176:179], a[128:131], v[160:163], v[176:179]\nv_mfma_f32_16x16x32_bf16 v[180:183], a[132:135], v[160:163], v[180:183]\n"
    "v_mfma_f32_16x16x32_bf16 v[184:187], a[136:139], v[160:163], v[184:187]\nv_mfma_f32_16x16x32_bf16 v[188:191], a[140:143], v[160:163], v[188:191]\n"
    "s_waitcnt vmcnt(2)\nv_mfma_f32_16x16x32_bf16 v[192:195], a[128:131], v[164:167], v[192:195]\n"
    "v_mfma_f32_16x16x32_bf16 v[196:199], a[132:135], v[164:167], v[196:199]\nv_mfma_f32_16x16x32_bf16 v[200:203], a[136:139], v[164:167], v[200:203]\n"
    "v_mfma_f32_16x16x32_bf16 v[204:207], a[140:143], v[164:167], v[204:207]\ns_waitcnt vmcnt(1)\n"
    "v_mfma_f32_16x16x32_bf16 v[176:179], a[144:147], v[168:171], v[176:179]\nv_mfma_f32_16x16x32_bf16 v[180:183], a[148:151], v[168:171], v[180:183]\n"
    "v_mfma_f32_16x16x32_bf16 v[184:187], a[152:155], v[168:171], v[184:187]\nv_mfma_f32_16x16x32_bf16 v[188:191], a[156:159], v[168:171], v[188:191]\n"
    "s_waitcnt vmcnt(0)\nv_mfma_f32_16x16x32_bf16 v[192:195], a[144:147], v[172:175], v[192:195]\n"
    "v_mfma_f32_16x16x32_bf16 v[196:199], a[148:151], v[172:175], v[196:199]\nv_mfma_f32_16x16x32_bf16 v[200:203], a[152:155], v[172:175], v[200:203]\n"
    "v_mfma_f32_16x16x32_bf16 v[204:207], a[156:159], v[172:175], v[204:207]\ns_nop 0\nds_write_b128 %[vlw], v[176:179] offset:0\n"
    "ds_write_b128 %[vlw], v[180:183] offset:1024\nds_write_b128 %[vlw], v[184:187] offset:2048\nds_write_b128 %[vlw], v[188:191] offset:3072\n"
    "ds_write_b128 %[vlw], v[192:195] offset:4096\nds_write_b128 %[vlw], v[196:199] offset:5120\nds_write_b128 %[vlw], v[200:203] offset:6144\n"
    "ds_write_b128 %[vlw], v[204:207] offset:7168\n"
    :: [rc] "s"(rc), [sP] "s"(sP), [sQ] "s"(sQ), [sY] "s"(sY), [vl16] "v"(vl16), [vlc] "v"(vlc), [vlw] "v"(vlw)
    : "memory", "scc", CLS, CLV_64_71, CLV_96_207, CLA_128_159);
}
DI void x4_loop(rsrc_t rc, rsrc_t ro, unsigned par, unsigned s0, const v16u& cv) {
  asm volatile(
    "s_nop 4\ns_mov_b32 s87, %[s0]\ns_mov_b32 s86, 0xffff0000\ns_mov_b32 s88, 400000\nv_mov_b32 v200, v54\nv_mov_b32 v201, v55\nv_bfrev_b32 v202, 1\n"
    "1:\nbuffer_load_dword v172, v53, %[rc], 0 offen sc1\ns_waitcnt vmcnt(0)\nv_cmp_ge_u32 vcc, s87, v172\ns_cbranch_vccz 2f\ns_sleep 2\n"
    "s_sub_u32 s88, s88, 1\ns_cmp_gt_i32 s88, 0\ns_cbranch_scc1 1b\n2:\ns_and_b32 s85, s87, 31\ns_bitcmp1_b32 %[par], s85\ns_cselect_b32 s84, 0, 0x2000\n"
    "s_lshl_b32 s85, s85, 21\ns_add_u32 s84, s84, s85\nbuffer_load_dwordx2 v[168:169], v52, %[rc], s84 offen sc1\n"
    "buffer_load_dwordx4 v[96:99], v48, %[rc], s84 offen offset:0 sc1\nbuffer_load_dwordx4 v[100:103], v48, %[rc], s84 offen offset:1024 sc1\n"
    "buffer_load_dwordx4 v[104:107], v48, %[rc], s84 offen offset:2048 sc1\nbuffer_load_dwordx4 v[108:111], v48, %[rc], s84 offen offset:3072 sc1\n"
    "buffer_load_dwordx4 v[112:115], v49, %[rc], s84 offen offset:0 sc1\nbuffer_load_dwordx4 v[116:119], v49, %[rc], s84 offen offset:1024 sc1\n"
    "buffer_load_dwordx4 v[120:123], v49, %[rc], s84 offen offset:2048 sc1\nbuffer_load_dwordx4 v[124:127], v49, %[rc], s84 offen offset:3072 sc1\n"
    "buffer_load_dwordx4 v[128:131], v50, %[rc], s84 offen offset:0 sc1\nbuffer_load_dwordx4 v[132:135], v50, %[rc], s84 offen offset:1024 sc1\n"
    "buffer_load_dwordx4 v[136:139], v50, %[rc], s84 offen offset:2048 sc1\nbuffer_load_dwordx4 v[140:143], v50, %[rc], s84 offen offset:3072 sc1\n"
    "buffer_load_dwordx4 v[144:147], v51, %[rc], s84 offen offset:0 sc1\nbuffer_load_dwordx4 v[148:151], v51, %[rc], s84 offen offset:1024 sc1\n"
    "buffer_store_dword v172, v202, %[rc], 0 offen\n3:\nbuffer_load_dword v172, v53, %[rc], 0 offen sc1\ns_cmp_ge_u32 s87, 58\ns_addc_u32 s89, s87, 1\n"
    "s_and_b32 s85, s89, 31\ns_bitcmp1_b32 %[par], s85\ns_cselect_b32 s84, 0, 0x2000\ns_lshl_b32 s85, s85, 21\ns_add_u32 s84, s84, s85\ns_waitcnt vmcnt(15)\n"
    "v_mfma_f32_16x16x32_bf16 v[152:155], a[0:3], v[96:99], 0\nv_mfma_f32_16x16x32_bf16 v[156:159], a[4:7], v[96:99], 0\n"
    "v_mfma_f32_16x16x32_bf16 v[160:163], a[8:11], v[96:99], 0\nv_mfma_f32_16x16x32_bf16 v[164:167], a[12:15], v[96:99], 0\ns_waitcnt vmcnt(14)\n"
    "v_mfma_f32_16x16x32_bf16 v[152:155], a[16:19], v[100:103], v[152:155]\nv_mfma_f32_16x16x32_bf16 v[156:159], a[20:23], v[100:103], v[156:159]\n"
    "v_mfma_f32_16x16x32_bf16 v[160:163], a[24:27], v[100:103], v[160:163]\nv_mfma_f32_16x16x32_bf16 v[164:167], a[28:31], v[100:103], v[164:167]\n"
    "s_waitcnt vmcnt(13)\nv_mfma_f32_16x16x32_bf16 v[152:155], a[32:35], v[104:107], v[152:155]\n"
    "v_mfma_f32_16x16x32_bf16 v[156:159], a[36:39], v[104:107], v[156:159]\nv_mfma_f32_16x16x32_bf16 v[160:163], a[40:43], v[104:107], v[160:163]\n"
    "v_mfma_f32_16x16x32_bf16 v[164:167], a[44:47], v[104:107], v[164:167]\ns_waitcnt vmcnt(12)\n"
    "v_mfma_f32_16x16x32_bf16 v[152:155], a[48:51], v[108:111], v[152:155]\nv_mfma_f32_16x16x32_bf16 v[156:159], a[52:55], v[108:111], v[156:159]\n"
    "v_mfma_f32_16x16x32_bf16 v[160:163], a[56:59], v[108:111], v[160:163]\nv_mfma_f32_16x16x32_bf16 v[164:167], a[60:63], v[108:111], v[164:167]\n"
    "s_waitcnt vmcnt(11)\nv_mfma_f32_16x16x32_bf16 v[152:155], a[64:67], v[112:115], v[152:155]\n"
    "v_mfma_f32_16x16x32_bf16 v[156:159], a[68:71], v[112:115], v[156:159]\nv_mfma_f32_16x16x32_bf16 v[160:163], a[72:75], v[112:115], v[160:163]\n"
    "v_mfma_f32_16x16x32_bf16 v[164:167], a[76:79], v[112:115], v[164:167]\ns_waitcnt vmcnt(10)\n"
    "v_mfma_f32_16x16x32_bf16 v[152:155], a[80:83], v[116:119], v[152:155]\nv_mfma_f32_16x16x32_bf16 v[156:159], a[84:87], v[116:119], v[156:159]\n"
    "v_mfma_f32_16x16x32_bf16 v[160:163], a[88:91], v[116:119], v[160:163]\nv_mfma_f32_16x16x32_bf16 v[164:167], a[92:95], v[116:119], v[164:167]\n"
    "s_waitcnt vmcnt(9)\nv_mfma_f32_16x16x32_bf16 v[152:155], a[96:99], v[120:123], v[152:155]\n"
    "v_mfma_f32_16x16x32_bf16 v[156:159], a[100:103], v[120:123], v[156:159]\nv_mfma_f32_16x16x32_bf16 v[160:163], a[104:107], v[120:123], v[160:163]\n"
    "v_mfma_f32_16x16x32_bf16 v[164:167], a[108:111], v[120:123], v[164:167]\ns_waitcnt vmcnt(8)\n"
    "v_mfma_f32_16x16x32_bf16 v[152:155], a[112:115], v[124:127], v[152:155]\nv_mfma_f32_16x16x32_bf16 v[156:159], a[116:119], v[124:127], v[156:159]\n"
    "v_mfma_f32_16x16x32_bf16 v[160:163], a[120:123], v[124:127], v[160:163]\nv_mfma_f32_16x16x32_bf16 v[164:167], a[124:127], v[124:127], v[164:167]\n"
    "s_waitcnt vmcnt(7)\nv_mfma_f32_16x16x32_bf16 v[152:155], a[128:131], v[128:131], v[152:155]\n"
    "v_mfma_f32_16x16x32_bf16 v[156:159], a[132:135], v[128:131], v[156:159]\nv_mfma_f32_16x16x32_bf16 v[160:163], a[136:139], v[128:131], v[160:163]\n"
    "v_mfma_f32_16x16x32_bf16 v[164:167], a[140:143], v[128:131], v[164:167]\ns_waitcnt vmcnt(6)\n"
    "v_mfma_f32_16x16x32_bf16 v[152:155], a[144:147], v[132:135], v[152:155]\nv_mfma_f32_16x16x32_bf16 v[156:159], a[148:151], v[132:135], v[156:159]\n"
    "v_mfma_f32_16x16x32_bf16 v[160:163], a[152:155], v[132:135], v[160:163]\nv_mfma_f32_16x16x32_bf16 v[164:167], a[156:159], v[132:135], v[164:167]\n"
    "s_waitcnt vmcnt(5)\nv_mfma_f32_16x16x32_bf16 v[152:155], a[160:163], v[136:139], v[152:155]\n"
    "v_mfma_f32_16x16x32_bf16 v[156:159], a[164:167], v[136:139], v[156:159]\nv_mfma_f32_16x16x32_bf16 v[160:163], a[168:171], v[136:139], v[160:163]\n"
    "v_mfma_f32_16x16x32_bf16 v[164:167], a[172:175], v[136:139], v[164:167]\ns_waitcnt vmcnt(4)\n"
    "v_mfma_f32_16x16x32_bf16 v[152:155], a[176:179], v[140:143], v[152:155]\nv_mfma_f32_16x16x32_bf16 v[156:159], a[180:183], v[140:143], v[156:159]\n"
    "v_mfma_f32_16x16x32_bf16 v[160:163], a[184:187], v[140:143], v[160:163]\nv_mfma_f32_16x16x32_bf16 v[164:167], a[188:191], v[140:143], v[164:167]\n"
    "s_waitcnt vmcnt(3)\nv_mfma_f32_16x16x32_bf16 v[152:155], a[192:195], v[144:147], v[152:155]\n"
    "v_mfma_f32_16x16x32_bf16 v[156:159], a[196:199], v[144:147], v[156:159]\nv_mfma_f32_16x16x32_bf16 v[160:163], a[200:203], v[144:147], v[160:163]\n"
    "v_mfma_f32_16x16x32_bf16 v[164:167], a[204:207], v[144:147], v[164:167]\ns_waitcnt vmcnt(2)\n"
    "v_mfma_f32_16x16x32_bf16 v[152:155], a[208:211], v[148:151], v[152:155]\nv_mfma_f32_16x16x32_bf16 v[156:159], a[212:215], v[148:151], v[156:159]\n"
    "v_mfma_f32_16x16x32_bf16 v[160:163], a[216:219], v[148:151], v[160:163]\nv_mfma_f32_16x16x32_bf16 v[164:167], a[220:223], v[148:151], v[164:167]\n"
    "v_mov_b64 v[170:171], v[168:169]\ns_nop 3\nds_write_b128 v200, v[152:155] offset:0\nds_write_b128 v200, v[156:159] offset:1024\n"
    "ds_write_b128 v200, v[160:163] offset:2048\nds_write_b128 v200, v[164:167] offset:3072\ns_waitcnt vmcnt(0)\nv_cmp_ge_u32 vcc, s89, v172\n"
    "s_mov_b32 s90, 0\ns_cbranch_vccnz 4f\nbuffer_load_dwordx2 v[168:169], v52, %[rc], s84 offen sc1\n"
    "buffer_load_dwordx4 v[96:99], v48, %[rc], s84 offen offset:0 sc1\nbuffer_load_dwordx4 v[100:103], v48, %[rc], s84 offen offset:1024 sc1\n"
    "buffer_load_dwordx4 v[104:107], v48, %[rc], s84 offen offset:2048 sc1\nbuffer_load_dwordx4 v[108:111], v48, %[rc], s84 offen offset:3072 sc1\n"
    "buffer_load_dwordx4 v[112:115], v49, %[rc], s84 offen offset:0 sc1\nbuffer_load_dwordx4 v[116:119], v49, %[rc], s84 offen offset:1024 sc1\n"
    "buffer_load_dwordx4 v[120:123], v49, %[rc], s84 offen offset:2048 sc1\nbuffer_load_dwordx4 v[124:127], v49, %[rc], s84 offen offset:3072 sc1\n"
    "buffer_load_dwordx4 v[128:131], v50, %[rc], s84 offen offset:0 sc1\nbuffer_load_dwordx4 v[132:135], v50, %[rc], s84 offen offset:1024 sc1\n"
    "buffer_load_dwordx4 v[136:139], v50, %[rc], s84 offen offset:2048 sc1\nbuffer_load_dwordx4 v[140:143], v50, %[rc], s84 offen offset:3072 sc1\n"
    "buffer_load_dwordx4 v[144:147], v51, %[rc], s84 offen offset:0 sc1\nbuffer_load_dwordx4 v[148:151], v51, %[rc], s84 offen offset:1024 sc1\n"
    "s_mov_b32 s90, 1\n4:\ns_waitcnt lgkmcnt(0)\ns_barrier\nds_read2st64_b32 v[176:177], v201 offset0:0 offset1:4\n"
    "ds_read2st64_b32 v[178:179], v201 offset0:8 offset1:12\nds_read2st64_b32 v[180:181], v201 offset0:16 offset1:20\n"
    "ds_read2st64_b32 v[182:183], v201 offset0:24 offset1:28\nds_read2st64_b32 v[184:185], v201 offset0:32 offset1:36\n"
    "ds_read2st64_b32 v[186:187], v201 offset0:40 offset1:44\nds_read2st64_b32 v[188:189], v201 offset0:48 offset1:52\n"
    "ds_read2st64_b32 v[190:191], v201 offset0:56 offset1:60\nv_xor_b32 v200, 0x4000, v200\nv_xor_b32 v201, 0x4000, v201\ns_lshl_b32 s91, s87, 15\n"
    "s_waitcnt lgkmcnt(4)\nv_add_f32 v176, v176, v180\nv_add_f32 v177, v177, v181\nv_add_f32 v178, v178, v182\nv_add_f32 v179, v179, v183\n"
    "s_waitcnt lgkmcnt(0)\nv_add_f32 v184, v184, v188\nv_add_f32 v185, v185, v189\nv_add_f32 v186, v186, v190\nv_add_f32 v187, v187, v191\n"
    "v_add_f32 v176, v176, v184\nv_add_f32 v177, v177, v185\nv_add_f32 v178, v178, v186\nv_add_f32 v179, v179, v187\nv_cvt_pk_bf16_f32 v192, v176, v177\n"
    "v_cvt_pk_bf16_f32 v193, v178, v179\nv_lshlrev_b32 v176, 16, v192\nv_and_b32 v177, s86, v192\nv_lshlrev_b32 v178, 16, v193\nv_and_b32 v179, s86, v193\n"
    "v_mul_f32 v176, v176, v58\nv_mul_f32 v177, v177, v59\nv_mul_f32 v178, v178, v60\nv_mul_f32 v179, v179, v61\nv_cvt_pk_bf16_f32 v192, v176, v177\n"
    "v_cvt_pk_bf16_f32 v193, v178, v179\nv_lshlrev_b32 v176, 16, v192\nv_and_b32 v177, s86, v192\nv_lshlrev_b32 v180, 16, v170\nv_and_b32 v181, s86, v170\n"
    "v_lshlrev_b32 v178, 16, v193\nv_and_b32 v179, s86, v193\nv_lshlrev_b32 v182, 16, v171\nv_and_b32 v183, s86, v171\nv_add_f32 v176, v176, v180\n"
    "v_add_f32 v177, v177, v181\nv_add_f32 v178, v178, v182\nv_add_f32 v179, v179, v183\nv_cvt_pk_bf16_f32 v194, v176, v177\n"
    "v_cvt_pk_bf16_f32 v195, v178, v179\ns_and_b32 s85, s87, 7\ns_cmp_lg_u32 s85, 2\ns_cbranch_scc1 5f\nv_mov_b32 v196, s89\n"
    "buffer_store_dword v196, v57, %[rc], 0 offen sc1\n5:\nbuffer_store_dwordx2 v[194:195], v56, %[ro], s91 offen sc1\ns_mov_b32 s87, s89\n"
    "s_cmp_eq_u32 s90, 1\ns_cbranch_scc1 3b\ns_cmp_lt_u32 s87, 64\ns_cbranch_scc1 1b\ns_waitcnt vmcnt(0)\n"
    :
    : [rc] "s"(rc), [ro] "s"(ro), [par] "s"(par), [s0] "s"(s0), "{v[48:63]}"(cv)
    : "memory", "vcc", "scc", CLS, CLV_96_203);
}
DI void load_vo(rsrc_t rw, rsrc_t ro, unsigned ld, unsigned va, unsigned vb, unsigned ra, unsigned rb) {
  asm volatile(
    "s_nop 4\ns_mov_b32 s85, 0x05040100\ns_mov_b32 s86, 0x07060302\ns_add_u32 m0, %[ld], 0\ns_mov_b32 s84, 0\n"
    "buffer_load_dwordx4 %[va], %[rw], s84 offen nt lds\ns_add_u32 m0, m0, 1024\ns_add_u32 s84, s84, 32768\n"
    "buffer_load_dwordx4 %[va], %[rw], s84 offen nt lds\ns_add_u32 m0, m0, 1024\ns_add_u32 s84, s84, 32768\n"
    "buffer_load_dwordx4 %[va], %[rw], s84 offen nt lds\ns_add_u32 m0, m0, 1024\ns_add_u32 s84, s84, 32768\n"
    "buffer_load_dwordx4 %[va], %[rw], s84 offen nt lds\ns_add_u32 m0, m0, 1024\ns_add_u32 s84, s84, 32768\n"
    "buffer_load_dwordx4 %[va], %[rw], s84 offen nt lds\ns_add_u32 m0, m0, 1024\ns_add_u32 s84, s84, 32768\n"
    "buffer_load_dwordx4 %[va], %[rw], s84 offen nt lds\ns_add_u32 m0, m0, 1024\ns_add_u32 s84, s84, 32768\n"
    "buffer_load_dwordx4 %[va], %[rw], s84 offen nt lds\ns_add_u32 m0, m0, 1024\ns_add_u32 s84, s84, 32768\n"
    "buffer_load_dwordx4 %[va], %[rw], s84 offen nt lds\ns_add_u32 m0, %[ld], 32768\ns_mov_b32 s84, 1048576\n"
    "buffer_load_dwordx4 %[va], %[rw], s84 offen nt lds\ns_add_u32 m0, m0, 1024\ns_add_u32 s84, s84, 32768\n"
    "buffer_load_dwordx4 %[va], %[rw], s84 offen nt lds\ns_add_u32 m0, m0, 1024\ns_add_u32 s84, s84, 32768\n"
    "buffer_load_dwordx4 %[va], %[rw], s84 offen nt lds\ns_add_u32 m0, m0, 1024\ns_add_u32 s84, s84, 32768\n"
    "buffer_load_dwordx4 %[va], %[rw], s84 offen nt lds\ns_add_u32 m0, m0, 1024\ns_add_u32 s84, s84, 32768\n"
    "buffer_load_dwordx4 %[va], %[rw], s84 offen nt lds\ns_add_u32 m0, m0, 1024\ns_add_u32 s84, s84, 32768\n"
    "buffer_load_dwordx4 %[va], %[rw], s84 offen nt lds\ns_add_u32 m0, m0, 1024\ns_add_u32 s84, s84, 32768\n"
    "buffer_load_dwordx4 %[va], %[rw], s84 offen nt lds\ns_add_u32 m0, m0, 1024\ns_add_u32 s84, s84, 32768\n"
    "buffer_load_dwordx4 %[va], %[rw], s84 offen nt lds\ns_waitcnt vmcnt(8)\ns_barrier\ns_add_u32 m0, %[ld], 65536\ns_mov_b32 s84, 2097152\n"
    "buffer_load_dwordx4 %[va], %[rw], s84 offen nt lds\ns_add_u32 m0, m0, 1024\ns_add_u32 s84, s84, 32768\n"
    "buffer_load_dwordx4 %[va], %[rw], s84 offen nt lds\ns_add_u32 m0, m0, 1024\ns_add_u32 s84, s84, 32768\n"
    "buffer_load_dwordx4 %[va], %[rw], s84 offen nt lds\ns_add_u32 m0, m0, 1024\ns_add_u32 s84, s84, 32768\n"
    "buffer_load_dwordx4 %[va], %[rw], s84 offen nt lds\ns_add_u32 m0, m0, 1024\ns_add_u32 s84, s84, 32768\n"
    "buffer_load_dwordx4 %[va], %[rw], s84 offen nt lds\ns_add_u32 m0, m0, 1024\ns_add_u32 s84, s84, 32768\n"
    "buffer_load_dwordx4 %[va], %[rw], s84 offen nt lds\ns_add_u32 m0, m0, 1024\ns_add_u32 s84, s84, 32768\n"
    "buffer_load_dwordx4 %[va], %[rw], s84 offen nt lds\ns_add_u32 m0, m0, 1024\ns_add_u32 s84, s84, 32768\n"
    "buffer_load_dwordx4 %[va], %[rw], s84 offen nt lds\nv_add_u32 v96, 0, %[ra]\nds_read2st64_b32 v[64:65], v96 offset0:0 offset1:2\n"
    "ds_read2st64_b32 v[66:67], v96 offset0:4 offset1:6\nds_read2st64_b32 v[68:69], v96 offset0:8 offset1:10\n"
    "ds_read2st64_b32 v[70:71], v96 offset0:12 offset1:14\nv_add_u32 v97, 0, %[ra]\nds_read2st64_b32 v[72:73], v97 offset0:32 offset1:34\n"
    "ds_read2st64_b32 v[74:75], v97 offset0:36 offset1:38\nds_read2st64_b32 v[76:77], v97 offset0:40 offset1:42\n"
    "ds_read2st64_b32 v[78:79], v97 offset0:44 offset1:46\nv_add_u32 v98, 0, %[ra]\nds_read2st64_b32 v[80:81], v98 offset0:64 offset1:66\n"
    "ds_read2st64_b32 v[82:83], v98 offset0:68 offset1:70\nds_read2st64_b32 v[84:85], v98 offset0:72 offset1:74\n"
    "ds_read2st64_b32 v[86:87], v98 offset0:76 offset1:78\nv_add_u32 v99, 0, %[ra]\nds_read2st64_b32 v[88:89], v99 offset0:96 offset1:98\n"
    "ds_read2st64_b32 v[90:91], v99 offset0:100 offset1:102\nds_read2st64_b32 v[92:93], v99 offset0:104 offset1:106\n"
    "ds_read2st64_b32 v[94:95], v99 offset0:108 offset1:110\ns_waitcnt lgkmcnt(12)\nv_perm_b32 v48, v65, v64, s85\nv_perm_b32 v49, v67, v66, s85\n"
    "v_perm_b32 v50, v69, v68, s85\nv_perm_b32 v51, v71, v70, s85\nv_perm_b32 v52, v65, v64, s86\nv_perm_b32 v53, v67, v66, s86\n"
    "v_perm_b32 v54, v69, v68, s86\nv_perm_b32 v55, v71, v70, s86\nv_accvgpr_write_b32 a0, v48\nv_accvgpr_write_b32 a1, v49\nv_accvgpr_write_b32 a2, v50\n"
    "v_accvgpr_write_b32 a3, v51\nv_accvgpr_write_b32 a8, v52\nv_accvgpr_write_b32 a9, v53\nv_accvgpr_write_b32 a10, v54\nv_accvgpr_write_b32 a11, v55\n"
    "s_waitcnt lgkmcnt(8)\nv_perm_b32 v48, v73, v72, s85\nv_perm_b32 v49, v75, v74, s85\nv_perm_b32 v50, v77, v76, s85\nv_perm_b32 v51, v79, v78, s85\n"
    "v_perm_b32 v52, v73, v72, s86\nv_perm_b32 v53, v75, v74, s86\nv_perm_b32 v54, v77, v76, s86\nv_perm_b32 v55, v79, v78, s86\n"
    "v_accvgpr_write_b32 a4, v48\nv_accvgpr_write_b32 a5, v49\nv_accvgpr_write_b32 a6, v50\nv_accvgpr_write_b32 a7, v51\nv_accvgpr_write_b32 a12, v52\n"
    "v_accvgpr_write_b32 a13, v53\nv_accvgpr_write_b32 a14, v54\nv_accvgpr_write_b32 a15, v55\ns_waitcnt lgkmcnt(4)\nv_perm_b32 v48, v81, v80, s85\n"
    "v_perm_b32 v49, v83, v82, s85\nv_perm_b32 v50, v85, v84, s85\nv_perm_b32 v51, v87, v86, s85\nv_perm_b32 v52, v81, v80, s86\n"
    "v_perm_b32 v53, v83, v82, s86\nv_perm_b32 v54, v85, v84, s86\nv_perm_b32 v55, v87, v86, s86\nv_accvgpr_write_b32 a16, v48\n"
    "v_accvgpr_write_b32 a17, v49\nv_accvgpr_write_b32 a18, v50\nv_accvgpr_write_b32 a19, v51\nv_accvgpr_write_b32 a24, v52\nv_accvgpr_write_b32 a25, v53\n"
    "v_accvgpr_write_b32 a26, v54\nv_accvgpr_write_b32 a27, v55\ns_waitcnt lgkmcnt(0)\nv_perm_b32 v48, v89, v88, s85\nv_perm_b32 v49, v91, v90, s85\n"
    "v_perm_b32 v50, v93, v92, s85\nv_perm_b32 v51, v95, v94, s85\nv_perm_b32 v52, v89, v88, s86\nv_perm_b32 v53, v91, v90, s86\n"
    "v_perm_b32 v54, v93, v92, s86\nv_perm_b32 v55, v95, v94, s86\nv_accvgpr_write_b32 a20, v48\nv_accvgpr_write_b32 a21, v49\nv_accvgpr_write_b32 a22, v50\n"
    "v_accvgpr_write_b32 a23, v51\nv_accvgpr_write_b32 a28, v52\nv_accvgpr_write_b32 a29, v53\nv_accvgpr_write_b32 a30, v54\nv_accvgpr_write_b32 a31, v55\n"
    "s_waitcnt vmcnt(8)\ns_barrier\ns_add_u32 m0, %[ld], 0\ns_mov_b32 s84, 3145728\nbuffer_load_dwordx4 %[va], %[rw], s84 offen nt lds\n"
    "s_add_u32 m0, m0, 1024\ns_add_u32 s84, s84, 32768\nbuffer_load_dwordx4 %[va], %[rw], s84 offen nt lds\ns_add_u32 m0, m0, 1024\n"
    "s_add_u32 s84, s84, 32768\nbuffer_load_dwordx4 %[va], %[rw], s84 offen nt lds\ns_add_u32 m0, m0, 1024\ns_add_u32 s84, s84, 32768\n"
    "buffer_load_dwordx4 %[va], %[rw], s84 offen nt lds\ns_add_u32 m0, m0, 1024\ns_add_u32 s84, s84, 32768\n"
    "buffer_load_dwordx4 %[va], %[rw], s84 offen nt lds\ns_add_u32 m0, m0, 1024\ns_add_u32 s84, s84, 32768\n"
    "buffer_load_dwordx4 %[va], %[rw], s84 offen nt lds\ns_add_u32 m0, m0, 1024\ns_add_u32 s84, s84, 32768\n"
    "buffer_load_dwordx4 %[va], %[rw], s84 offen nt lds\ns_add_u32 m0, m0, 1024\ns_add_u32 s84, s84, 32768\n"
    "buffer_load_dwordx4 %[va], %[rw], s84 offen nt lds\nv_add_u32 v96, 32768, %[ra]\nds_read2st64_b32 v[64:65], v96 offset0:0 offset1:2\n"
    "ds_read2st64_b32 v[66:67], v96 offset0:4 offset1:6\nds_read2st64_b32 v[68:69], v96 offset0:8 offset1:10\n"
    "ds_read2st64_b32 v[70:71], v96 offset0:12 offset1:14\nv_add_u32 v97, 32768, %[ra]\nds_read2st64_b32 v[72:73], v97 offset0:32 offset1:34\n"
    "ds_read2st64_b32 v[74:75], v97 offset0:36 offset1:38\nds_read2st64_b32 v[76:77], v97 offset0:40 offset1:42\n"
    "ds_read2st64_b32 v[78:79], v97 offset0:44 offset1:46\nv_add_u32 v98, 32768, %[ra]\nds_read2st64_b32 v[80:81], v98 offset0:64 offset1:66\n"
    "ds_read2st64_b32 v[82:83], v98 offset0:68 offset1:70\nds_read2st64_b32 v[84:85], v98 offset0:72 offset1:74\n"
    "ds_read2st64_b32 v[86:87], v98 offset0:76 offset1:78\nv_add_u32 v99, 32768, %[ra]\nds_read2st64_b32 v[88:89], v99 offset0:96 offset1:98\n"
    "ds_read2st64_b32 v[90:91], v99 offset0:100 offset1:102\nds_read2st64_b32 v[92:93], v99 offset0:104 offset1:106\n"
    "ds_read2st64_b32 v[94:95], v99 offset0:108 offset1:110\ns_waitcnt lgkmcnt(12)\nv_perm_b32 v48, v65, v64, s85\nv_perm_b32 v49, v67, v66, s85\n"
    "v_perm_b32 v50, v69, v68, s85\nv_perm_b32 v51, v71, v70, s85\nv_perm_b32 v52, v65, v64, s86\nv_perm_b32 v53, v67, v66, s86\n"
    "v_perm_b32 v54, v69, v68, s86\nv_perm_b32 v55, v71, v70, s86\nv_accvgpr_write_b32 a32, v48\nv_accvgpr_write_b32 a33, v49\nv_accvgpr_write_b32 a34, v50\n"
    "v_accvgpr_write_b32 a35, v51\nv_accvgpr_write_b32 a40, v52\nv_accvgpr_write_b32 a41, v53\nv_accvgpr_write_b32 a42, v54\nv_accvgpr_write_b32 a43, v55\n"
    "s_waitcnt lgkmcnt(8)\nv_perm_b32 v48, v73, v72, s85\nv_perm_b32 v49, v75, v74, s85\nv_perm_b32 v50, v77, v76, s85\nv_perm_b32 v51, v79, v78, s85\n"
    "v_perm_b32 v52, v73, v72, s86\nv_perm_b32 v53, v75, v74, s86\nv_perm_b32 v54, v77, v76, s86\nv_perm_b32 v55, v79, v78, s86\n"
    "v_accvgpr_write_b32 a36, v48\nv_accvgpr_write_b32 a37, v49\nv_accvgpr_write_b32 a38, v50\nv_accvgpr_write_b32 a39, v51\nv_accvgpr_write_b32 a44, v52\n"
    "v_accvgpr_write_b32 a45, v53\nv_accvgpr_write_b32 a46, v54\nv_accvgpr_write_b32 a47, v55\ns_waitcnt lgkmcnt(4)\nv_perm_b32 v48, v81, v80, s85\n"
    "v_perm_b32 v49, v83, v82, s85\nv_perm_b32 v50, v85, v84, s85\nv_perm_b32 v51, v87, v86, s85\nv_perm_b32 v52, v81, v80, s86\n"
    "v_perm_b32 v53, v83, v82, s86\nv_perm_b32 v54, v85, v84, s86\nv_perm_b32 v55, v87, v86, s86\nv_accvgpr_write_b32 a48, v48\n"
    "v_accvgpr_write_b32 a49, v49\nv_accvgpr_write_b32 a50, v50\nv_accvgpr_write_b32 a51, v51\nv_accvgpr_write_b32 a56, v52\nv_accvgpr_write_b32 a57, v53\n"
    "v_accvgpr_write_b32 a58, v54\nv_accvgpr_write_b32 a59, v55\ns_waitcnt lgkmcnt(0)\nv_perm_b32 v48, v89, v88, s85\nv_perm_b32 v49, v91, v90, s85\n"
    "v_perm_b32 v50, v93, v92, s85\nv_perm_b32 v51, v95, v94, s85\nv_perm_b32 v52, v89, v88, s86\nv_perm_b32 v53, v91, v90, s86\n"
    "v_perm_b32 v54, v93, v92, s86\nv_perm_b32 v55, v95, v94, s86\nv_accvgpr_write_b32 a52, v48\nv_accvgpr_write_b32 a53, v49\nv_accvgpr_write_b32 a54, v50\n"
    "v_accvgpr_write_b32 a55, v51\nv_accvgpr_write_b32 a60, v52\nv_accvgpr_write_b32 a61, v53\nv_accvgpr_write_b32 a62, v54\nv_accvgpr_write_b32 a63, v55\n"
    "s_waitcnt vmcnt(8)\ns_barrier\ns_add_u32 m0, %[ld], 32768\ns_mov_b32 s84, 0\nbuffer_load_dwordx4 %[vb], %[ro], s84 offen nt lds\n"
    "s_add_u32 m0, m0, 1024\ns_add_u32 s84, s84, 1024\nbuffer_load_dwordx4 %[vb], %[ro], s84 offen nt lds\ns_add_u32 m0, m0, 1024\ns_add_u32 s84, s84, 1024\n"
    "buffer_load_dwordx4 %[vb], %[ro], s84 offen nt lds\ns_add_u32 m0, m0, 1024\ns_add_u32 s84, s84, 1024\n"
    "buffer_load_dwordx4 %[vb], %[ro], s84 offen nt lds\ns_add_u32 m0, m0, 1024\ns_add_u32 s84, s84, 1024\n"
    "buffer_load_dwordx4 %[vb], %[ro], s84 offen nt lds\ns_add_u32 m0, m0, 1024\ns_add_u32 s84, s84, 1024\n"
    "buffer_load_dwordx4 %[vb], %[ro], s84 offen nt lds\ns_add_u32 m0, m0, 1024\ns_add_u32 s84, s84, 1024\n"
    "buffer_load_dwordx4 %[vb], %[ro], s84 offen nt lds\ns_add_u32 m0, m0, 1024\ns_add_u32 s84, s84, 1024\n"
    "buffer_load_dwordx4 %[vb], %[ro], s84 offen nt lds\nv_add_u32 v96, 65536, %[ra]\nds_read2st64_b32 v[64:65], v96 offset0:0 offset1:2\n"
    "ds_read2st64_b32 v[66:67], v96 offset0:4 offset1:6\nds_read2st64_b32 v[68:69], v96 offset0:8 offset1:10\n"
    "ds_read2st64_b32 v[70:71], v96 offset0:12 offset1:14\nv_add_u32 v97, 65536, %[ra]\nds_read2st64_b32 v[72:73], v97 offset0:32 offset1:34\n"
    "ds_read2st64_b32 v[74:75], v97 offset0:36 offset1:38\nds_read2st64_b32 v[76:77], v97 offset0:40 offset1:42\n"
    "ds_read2st64_b32 v[78:79], v97 offset0:44 offset1:46\nv_add_u32 v98, 65536, %[ra]\nds_read2st64_b32 v[80:81], v98 offset0:64 offset1:66\n"
    "ds_read2st64_b32 v[82:83], v98 offset0:68 offset1:70\nds_read2st64_b32 v[84:85], v98 offset0:72 offset1:74\n"
    "ds_read2st64_b32 v[86:87], v98 offset0:76 offset1:78\nv_add_u32 v99, 65536, %[ra]\nds_read2st64_b32 v[88:89], v99 offset0:96 offset1:98\n"
    "ds_read2st64_b32 v[90:91], v99 offset0:100 offset1:102\nds_read2st64_b32 v[92:93], v99 offset0:104 offset1:106\n"
    "ds_read2st64_b32 v[94:95], v99 offset0:108 offset1:110\ns_waitcnt lgkmcnt(12)\nv_perm_b32 v48, v65, v64, s85\nv_perm_b32 v49, v67, v66, s85\n"
    "v_perm_b32 v50, v69, v68, s85\nv_perm_b32 v51, v71, v70, s85\nv_perm_b32 v52, v65, v64, s86\nv_perm_b32 v53, v67, v66, s86\n"
    "v_perm_b32 v54, v69, v68, s86\nv_perm_b32 v55, v71, v70, s86\nv_accvgpr_write_b32 a64, v48\nv_accvgpr_write_b32 a65, v49\nv_accvgpr_write_b32 a66, v50\n"
    "v_accvgpr_write_b32 a67, v51\nv_accvgpr_write_b32 a72, v52\nv_accvgpr_write_b32 a73, v53\nv_accvgpr_write_b32 a74, v54\nv_accvgpr_write_b32 a75, v55\n"
    "s_waitcnt lgkmcnt(8)\nv_perm_b32 v48, v73, v72, s85\nv_perm_b32 v49, v75, v74, s85\nv_perm_b32 v50, v77, v76, s85\nv_perm_b32 v51, v79, v78, s85\n"
    "v_perm_b32 v52, v73, v72, s86\nv_perm_b32 v53, v75, v74, s86\nv_perm_b32 v54, v77, v76, s86\nv_perm_b32 v55, v79, v78, s86\n"
    "v_accvgpr_write_b32 a68, v48\nv_accvgpr_write_b32 a69, v49\nv_accvgpr_write_b32 a70, v50\nv_accvgpr_write_b32 a71, v51\nv_accvgpr_write_b32 a76, v52\n"
    "v_accvgpr_write_b32 a77, v53\nv_accvgpr_write_b32 a78, v54\nv_accvgpr_write_b32 a79, v55\ns_waitcnt lgkmcnt(4)\nv_perm_b32 v48, v81, v80, s85\n"
    "v_perm_b32 v49, v83, v82, s85\nv_perm_b32 v50, v85, v84, s85\nv_perm_b32 v51, v87, v86, s85\nv_perm_b32 v52, v81, v80, s86\n"
    "v_perm_b32 v53, v83, v82, s86\nv_perm_b32 v54, v85, v84, s86\nv_perm_b32 v55, v87, v86, s86\nv_accvgpr_write_b32 a80, v48\n"
    "v_accvgpr_write_b32 a81, v49\nv_accvgpr_write_b32 a82, v50\nv_accvgpr_write_b32 a83, v51\nv_accvgpr_write_b32 a88, v52\nv_accvgpr_write_b32 a89, v53\n"
    "v_accvgpr_write_b32 a90, v54\nv_accvgpr_write_b32 a91, v55\ns_waitcnt lgkmcnt(0)\nv_perm_b32 v48, v89, v88, s85\nv_perm_b32 v49, v91, v90, s85\n"
    "v_perm_b32 v50, v93, v92, s85\nv_perm_b32 v51, v95, v94, s85\nv_perm_b32 v52, v89, v88, s86\nv_perm_b32 v53, v91, v90, s86\n"
    "v_perm_b32 v54, v93, v92, s86\nv_perm_b32 v55, v95, v94, s86\nv_accvgpr_write_b32 a84, v48\nv_accvgpr_write_b32 a85, v49\nv_accvgpr_write_b32 a86, v50\n"
    "v_accvgpr_write_b32 a87, v51\nv_accvgpr_write_b32 a92, v52\nv_accvgpr_write_b32 a93, v53\nv_accvgpr_write_b32 a94, v54\nv_accvgpr_write_b32 a95, v55\n"
    "s_waitcnt vmcnt(8)\ns_barrier\ns_add_u32 m0, %[ld], 65536\ns_mov_b32 s84, 32768\nbuffer_load_dwordx4 %[vb], %[ro], s84 offen nt lds\n"
    "s_add_u32 m0, m0, 1024\ns_add_u32 s84, s84, 1024\nbuffer_load_dwordx4 %[vb], %[ro], s84 offen nt lds\ns_add_u32 m0, m0, 1024\ns_add_u32 s84, s84, 1024\n"
    "buffer_load_dwordx4 %[vb], %[ro], s84 offen nt lds\ns_add_u32 m0, m0, 1024\ns_add_u32 s84, s84, 1024\n"
    "buffer_load_dwordx4 %[vb], %[ro], s84 offen nt lds\ns_add_u32 m0, m0, 1024\ns_add_u32 s84, s84, 1024\n"
    "buffer_load_dwordx4 %[vb], %[ro], s84 offen nt lds\ns_add_u32 m0, m0, 1024\ns_add_u32 s84, s84, 1024\n"
    "buffer_load_dwordx4 %[vb], %[ro], s84 offen nt lds\ns_add_u32 m0, m0, 1024\ns_add_u32 s84, s84, 1024\n"
    "buffer_load_dwordx4 %[vb], %[ro], s84 offen nt lds\ns_add_u32 m0, m0, 1024\ns_add_u32 s84, s84, 1024\n"
    "buffer_load_dwordx4 %[vb], %[ro], s84 offen nt lds\nv_add_u32 v96, 0, %[ra]\nds_read2st64_b32 v[64:65], v96 offset0:0 offset1:2\n"
    "ds_read2st64_b32 v[66:67], v96 offset0:4 offset1:6\nds_read2st64_b32 v[68:69], v96 offset0:8 offset1:10\n"
    "ds_read2st64_b32 v[70:71], v96 offset0:12 offset1:14\nv_add_u32 v97, 0, %[ra]\nds_read2st64_b32 v[72:73], v97 offset0:32 offset1:34\n"
    "ds_read2st64_b32 v[74:75], v97 offset0:36 offset1:38\nds_read2st64_b32 v[76:77], v97 offset0:40 offset1:42\n"
    "ds_read2st64_b32 v[78:79], v97 offset0:44 offset1:46\nv_add_u32 v98, 0, %[ra]\nds_read2st64_b32 v[80:81], v98 offset0:64 offset1:66\n"
    "ds_read2st64_b32 v[82:83], v98 offset0:68 offset1:70\nds_read2st64_b32 v[84:85], v98 offset0:72 offset1:74\n"
    "ds_read2st64_b32 v[86:87], v98 offset0:76 offset1:78\nv_add_u32 v99, 0, %[ra]\nds_read2st64_b32 v[88:89], v99 offset0:96 offset1:98\n"
    "ds_read2st64_b32 v[90:91], v99 offset0:100 offset1:102\nds_read2st64_b32 v[92:93], v99 offset0:104 offset1:106\n"
    "ds_read2st64_b32 v[94:95], v99 offset0:108 offset1:110\ns_waitcnt lgkmcnt(12)\nv_perm_b32 v48, v65, v64, s85\nv_perm_b32 v49, v67, v66, s85\n"
    "v_perm_b32 v50, v69, v68, s85\nv_perm_b32 v51, v71, v70, s85\nv_perm_b32 v52, v65, v64, s86\nv_perm_b32 v53, v67, v66, s86\n"
    "v_perm_b32 v54, v69, v68, s86\nv_perm_b32 v55, v71, v70, s86\nv_accvgpr_write_b32 a96, v48\nv_accvgpr_write_b32 a97, v49\nv_accvgpr_write_b32 a98, v50\n"
    "v_accvgpr_write_b32 a99, v51\nv_accvgpr_write_b32 a104, v52\nv_accvgpr_write_b32 a105, v53\nv_accvgpr_write_b32 a106, v54\n"
    "v_accvgpr_write_b32 a107, v55\ns_waitcnt lgkmcnt(8)\nv_perm_b32 v48, v73, v72, s85\nv_perm_b32 v49, v75, v74, s85\nv_perm_b32 v50, v77, v76, s85\n"
    "v_perm_b32 v51, v79, v78, s85\nv_perm_b32 v52, v73, v72, s86\nv_perm_b32 v53, v75, v74, s86\nv_perm_b32 v54, v77, v76, s86\n"
    "v_perm_b32 v55, v79, v78, s86\nv_accvgpr_write_b32 a100, v48\nv_accvgpr_write_b32 a101, v49\nv_accvgpr_write_b32 a102, v50\n"
    "v_accvgpr_write_b32 a103, v51\nv_accvgpr_write_b32 a108, v52\nv_accvgpr_write_b32 a109, v53\nv_accvgpr_write_b32 a110, v54\n"
    "v_accvgpr_write_b32 a111, v55\ns_waitcnt lgkmcnt(4)\nv_perm_b32 v48, v81, v80, s85\nv_perm_b32 v49, v83, v82, s85\nv_perm_b32 v50, v85, v84, s85\n"
    "v_perm_b32 v51, v87, v86, s85\nv_perm_b32 v52, v81, v80, s86\nv_perm_b32 v53, v83, v82, s86\nv_perm_b32 v54, v85, v84, s86\n"
    "v_perm_b32 v55, v87, v86, s86\nv_accvgpr_write_b32 a112, v48\nv_accvgpr_write_b32 a113, v49\nv_accvgpr_write_b32 a114, v50\n"
    "v_accvgpr_write_b32 a115, v51\nv_accvgpr_write_b32 a120, v52\nv_accvgpr_write_b32 a121, v53\nv_accvgpr_write_b32 a122, v54\n"
    "v_accvgpr_write_b32 a123, v55\ns_waitcnt lgkmcnt(0)\nv_perm_b32 v48, v89, v88, s85\nv_perm_b32 v49, v91, v90, s85\nv_perm_b32 v50, v93, v92, s85\n"
    "v_perm_b32 v51, v95, v94, s85\nv_perm_b32 v52, v89, v88, s86\nv_perm_b32 v53, v91, v90, s86\nv_perm_b32 v54, v93, v92, s86\n"
    "v_perm_b32 v55, v95, v94, s86\nv_accvgpr_write_b32 a116, v48\nv_accvgpr_write_b32 a117, v49\nv_accvgpr_write_b32 a118, v50\n"
    "v_accvgpr_write_b32 a119, v51\nv_accvgpr_write_b32 a124, v52\nv_accvgpr_write_b32 a125, v53\nv_accvgpr_write_b32 a126, v54\n"
    "v_accvgpr_write_b32 a127, v55\ns_waitcnt vmcnt(8)\ns_barrier\ns_add_u32 m0, %[ld], 0\ns_mov_b32 s84, 65536\n"
    "buffer_load_dwordx4 %[vb], %[ro], s84 offen nt lds\ns_add_u32 m0, m0, 1024\ns_add_u32 s84, s84, 1024\n"
    "buffer_load_dwordx4 %[vb], %[ro], s84 offen nt lds\ns_add_u32 m0, m0, 1024\ns_add_u32 s84, s84, 1024\n"
    "buffer_load_dwordx4 %[vb], %[ro], s84 offen nt lds\ns_add_u32 m0, m0, 1024\ns_add_u32 s84, s84, 1024\n"
    "buffer_load_dwordx4 %[vb], %[ro], s84 offen nt lds\ns_add_u32 m0, m0, 1024\ns_add_u32 s84, s84, 1024\n"
    "buffer_load_dwordx4 %[vb], %[ro], s84 offen nt lds\ns_add_u32 m0, m0, 1024\ns_add_u32 s84, s84, 1024\n"
    "buffer_load_dwordx4 %[vb], %[ro], s84 offen nt lds\ns_add_u32 m0, m0, 1024\ns_add_u32 s84, s84, 1024\n"
    "buffer_load_dwordx4 %[vb], %[ro], s84 offen nt lds\ns_add_u32 m0, m0, 1024\ns_add_u32 s84, s84, 1024\n"
    "buffer_load_dwordx4 %[vb], %[ro], s84 offen nt lds\nv_add_u32 v96, 32768, %[rb]\nds_read2st64_b32 v[64:65], v96 offset0:0 offset1:4\n"
    "ds_read2st64_b32 v[66:67], v96 offset0:8 offset1:12\nds_read2st64_b32 v[68:69], v96 offset0:16 offset1:20\n"
    "ds_read2st64_b32 v[70:71], v96 offset0:24 offset1:28\nv_add_u32 v97, 32832, %[rb]\nds_read2st64_b32 v[72:73], v97 offset0:0 offset1:4\n"
    "ds_read2st64_b32 v[74:75], v97 offset0:8 offset1:12\nds_read2st64_b32 v[76:77], v97 offset0:16 offset1:20\n"
    "ds_read2st64_b32 v[78:79], v97 offset0:24 offset1:28\nv_add_u32 v98, 32896, %[rb]\nds_read2st64_b32 v[80:81], v98 offset0:0 offset1:4\n"
    "ds_read2st64_b32 v[82:83], v98 offset0:8 offset1:12\nds_read2st64_b32 v[84:85], v98 offset0:16 offset1:20\n"
    "ds_read2st64_b32 v[86:87], v98 offset0:24 offset1:28\nv_add_u32 v99, 32960, %[rb]\nds_read2st64_b32 v[88:89], v99 offset0:0 offset1:4\n"
    "ds_read2st64_b32 v[90:91], v99 offset0:8 offset1:12\nds_read2st64_b32 v[92:93], v99 offset0:16 offset1:20\n"
    "ds_read2st64_b32 v[94:95], v99 offset0:24 offset1:28\ns_waitcnt lgkmcnt(12)\nv_perm_b32 v48, v65, v64, s85\nv_perm_b32 v49, v67, v66, s85\n"
    "v_perm_b32 v50, v69, v68, s85\nv_perm_b32 v51, v71, v70, s85\nv_perm_b32 v52, v65, v64, s86\nv_perm_b32 v53, v67, v66, s86\n"
    "v_perm_b32 v54, v69, v68, s86\nv_perm_b32 v55, v71, v70, s86\nv_accvgpr_write_b32 a128, v48\nv_accvgpr_write_b32 a129, v49\n"
    "v_accvgpr_write_b32 a130, v50\nv_accvgpr_write_b32 a131, v51\nv_accvgpr_write_b32 a144, v52\nv_accvgpr_write_b32 a145, v53\n"
    "v_accvgpr_write_b32 a146, v54\nv_accvgpr_write_b32 a147, v55\ns_waitcnt lgkmcnt(8)\nv_perm_b32 v48, v73, v72, s85\nv_perm_b32 v49, v75, v74, s85\n"
    "v_perm_b32 v50, v77, v76, s85\nv_perm_b32 v51, v79, v78, s85\nv_perm_b32 v52, v73, v72, s86\nv_perm_b32 v53, v75, v74, s86\n"
    "v_perm_b32 v54, v77, v76, s86\nv_perm_b32 v55, v79, v78, s86\nv_accvgpr_write_b32 a160, v48\nv_accvgpr_write_b32 a161, v49\n"
    "v_accvgpr_write_b32 a162, v50\nv_accvgpr_write_b32 a163, v51\nv_accvgpr_write_b32 a176, v52\nv_accvgpr_write_b32 a177, v53\n"
    "v_accvgpr_write_b32 a178, v54\nv_accvgpr_write_b32 a179, v55\ns_waitcnt lgkmcnt(4)\nv_perm_b32 v48, v81, v80, s85\nv_perm_b32 v49, v83, v82, s85\n"
    "v_perm_b32 v50, v85, v84, s85\nv_perm_b32 v51, v87, v86, s85\nv_perm_b32 v52, v81, v80, s86\nv_perm_b32 v53, v83, v82, s86\n"
    "v_perm_b32 v54, v85, v84, s86\nv_perm_b32 v55, v87, v86, s86\nv_accvgpr_write_b32 a192, v48\nv_accvgpr_write_b32 a193, v49\n"
    "v_accvgpr_write_b32 a194, v50\nv_accvgpr_write_b32 a195, v51\nv_accvgpr_write_b32 a208, v52\nv_accvgpr_write_b32 a209, v53\n"
    "v_accvgpr_write_b32 a210, v54\nv_accvgpr_write_b32 a211, v55\ns_waitcnt lgkmcnt(0)\nv_perm_b32 v48, v89, v88, s85\nv_perm_b32 v49, v91, v90, s85\n"
    "v_perm_b32 v50, v93, v92, s85\nv_perm_b32 v51, v95, v94, s85\nv_perm_b32 v52, v89, v88, s86\nv_perm_b32 v53, v91, v90, s86\n"
    "v_perm_b32 v54, v93, v92, s86\nv_perm_b32 v55, v95, v94, s86\nv_accvgpr_write_b32 a224, v48\nv_accvgpr_write_b32 a225, v49\n"
    "v_accvgpr_write_b32 a226, v50\nv_accvgpr_write_b32 a227, v51\nv_accvgpr_write_b32 a240, v52\nv_accvgpr_write_b32 a241, v53\n"
    "v_accvgpr_write_b32 a242, v54\nv_accvgpr_write_b32 a243, v55\ns_waitcnt vmcnt(8)\ns_barrier\ns_add_u32 m0, %[ld], 32768\ns_mov_b32 s84, 98304\n"
    "buffer_load_dwordx4 %[vb], %[ro], s84 offen nt lds\ns_add_u32 m0, m0, 1024\ns_add_u32 s84, s84, 1024\n"
    "buffer_load_dwordx4 %[vb], %[ro], s84 offen nt lds\ns_add_u32 m0, m0, 1024\ns_add_u32 s84, s84, 1024\n"
    "buffer_load_dwordx4 %[vb], %[ro], s84 offen nt lds\ns_add_u32 m0, m0, 1024\ns_add_u32 s84, s84, 1024\n"
    "buffer_load_dwordx4 %[vb], %[ro], s84 offen nt lds\ns_add_u32 m0, m0, 1024\ns_add_u32 s84, s84, 1024\n"
    "buffer_load_dwordx4 %[vb], %[ro], s84 offen nt lds\ns_add_u32 m0, m0, 1024\ns_add_u32 s84, s84, 1024\n"
    "buffer_load_dwordx4 %[vb], %[ro], s84 offen nt lds\ns_add_u32 m0, m0, 1024\ns_add_u32 s84, s84, 1024\n"
    "buffer_load_dwordx4 %[vb], %[ro], s84 offen nt lds\ns_add_u32 m0, m0, 1024\ns_add_u32 s84, s84, 1024\n"
    "buffer_load_dwordx4 %[vb], %[ro], s84 offen nt lds\nv_add_u32 v96, 65536, %[rb]\nds_read2st64_b32 v[64:65], v96 offset0:0 offset1:4\n"
    "ds_read2st64_b32 v[66:67], v96 offset0:8 offset1:12\nds_read2st64_b32 v[68:69], v96 offset0:16 offset1:20\n"
    "ds_read2st64_b32 v[70:71], v96 offset0:24 offset1:28\nv_add_u32 v97, 65600, %[rb]\nds_read2st64_b32 v[72:73], v97 offset0:0 offset1:4\n"
    "ds_read2st64_b32 v[74:75], v97 offset0:8 offset1:12\nds_read2st64_b32 v[76:77], v97 offset0:16 offset1:20\n"
    "ds_read2st64_b32 v[78:79], v97 offset0:24 offset1:28\nv_add_u32 v98, 65664, %[rb]\nds_read2st64_b32 v[80:81], v98 offset0:0 offset1:4\n"
    "ds_read2st64_b32 v[82:83], v98 offset0:8 offset1:12\nds_read2st64_b32 v[84:85], v98 offset0:16 offset1:20\n"
    "ds_read2st64_b32 v[86:87], v98 offset0:24 offset1:28\nv_add_u32 v99, 65728, %[rb]\nds_read2st64_b32 v[88:89], v99 offset0:0 offset1:4\n"
    "ds_read2st64_b32 v[90:91], v99 offset0:8 offset1:12\nds_read2st64_b32 v[92:93], v99 offset0:16 offset1:20\n"
    "ds_read2st64_b32 v[94:95], v99 offset0:24 offset1:28\ns_waitcnt lgkmcnt(12)\nv_perm_b32 v48, v65, v64, s85\nv_perm_b32 v49, v67, v66, s85\n"
    "v_perm_b32 v50, v69, v68, s85\nv_perm_b32 v51, v71, v70, s85\nv_perm_b32 v52, v65, v64, s86\nv_perm_b32 v53, v67, v66, s86\n"
    "v_perm_b32 v54, v69, v68, s86\nv_perm_b32 v55, v71, v70, s86\nv_accvgpr_write_b32 a132, v48\nv_accvgpr_write_b32 a133, v49\n"
    "v_accvgpr_write_b32 a134, v50\nv_accvgpr_write_b32 a135, v51\nv_accvgpr_write_b32 a148, v52\nv_accvgpr_write_b32 a149, v53\n"
    "v_accvgpr_write_b32 a150, v54\nv_accvgpr_write_b32 a151, v55\ns_waitcnt lgkmcnt(8)\nv_perm_b32 v48, v73, v72, s85\nv_perm_b32 v49, v75, v74, s85\n"
    "v_perm_b32 v50, v77, v76, s85\nv_perm_b32 v51, v79, v78, s85\nv_perm_b32 v52, v73, v72, s86\nv_perm_b32 v53, v75, v74, s86\n"
    "v_perm_b32 v54, v77, v76, s86\nv_perm_b32 v55, v79, v78, s86\nv_accvgpr_write_b32 a164, v48\nv_accvgpr_write_b32 a165, v49\n"
    "v_accvgpr_write_b32 a166, v50\nv_accvgpr_write_b32 a167, v51\nv_accvgpr_write_b32 a180, v52\nv_accvgpr_write_b32 a181, v53\n"
    "v_accvgpr_write_b32 a182, v54\nv_accvgpr_write_b32 a183, v55\ns_waitcnt lgkmcnt(4)\nv_perm_b32 v48, v81, v80, s85\nv_perm_b32 v49, v83, v82, s85\n"
    "v_perm_b32 v50, v85, v84, s85\nv_perm_b32 v51, v87, v86, s85\nv_perm_b32 v52, v81, v80, s86\nv_perm_b32 v53, v83, v82, s86\n"
    "v_perm_b32 v54, v85, v84, s86\nv_perm_b32 v55, v87, v86, s86\nv_accvgpr_write_b32 a196, v48\nv_accvgpr_write_b32 a197, v49\n"
    "v_accvgpr_write_b32 a198, v50\nv_accvgpr_write_b32 a199, v51\nv_accvgpr_write_b32 a212, v52\nv_accvgpr_write_b32 a213, v53\n"
    "v_accvgpr_write_b32 a214, v54\nv_accvgpr_write_b32 a215, v55\ns_waitcnt lgkmcnt(0)\nv_perm_b32 v48, v89, v88, s85\nv_perm_b32 v49, v91, v90, s85\n"
    "v_perm_b32 v50, v93, v92, s85\nv_perm_b32 v51, v95, v94, s85\nv_perm_b32 v52, v89, v88, s86\nv_perm_b32 v53, v91, v90, s86\n"
    "v_perm_b32 v54, v93, v92, s86\nv_perm_b32 v55, v95, v94, s86\nv_accvgpr_write_b32 a228, v48\nv_accvgpr_write_b32 a229, v49\n"
    "v_accvgpr_write_b32 a230, v50\nv_accvgpr_write_b32 a231, v51\nv_accvgpr_write_b32 a244, v52\nv_accvgpr_write_b32 a245, v53\n"
    "v_accvgpr_write_b32 a246, v54\nv_accvgpr_write_b32 a247, v55\ns_waitcnt vmcnt(8)\ns_barrier\nv_add_u32 v96, 0, %[rb]\n"
    "ds_read2st64_b32 v[64:65], v96 offset0:0 offset1:4\nds_read2st64_b32 v[66:67], v96 offset0:8 offset1:12\n"
    "ds_read2st64_b32 v[68:69], v96 offset0:16 offset1:20\nds_read2st64_b32 v[70:71], v96 offset0:24 offset1:28\nv_add_u32 v97, 64, %[rb]\n"
    "ds_read2st64_b32 v[72:73], v97 offset0:0 offset1:4\nds_read2st64_b32 v[74:75], v97 offset0:8 offset1:12\n"
    "ds_read2st64_b32 v[76:77], v97 offset0:16 offset1:20\nds_read2st64_b32 v[78:79], v97 offset0:24 offset1:28\nv_add_u32 v98, 128, %[rb]\n"
    "ds_read2st64_b32 v[80:81], v98 offset0:0 offset1:4\nds_read2st64_b32 v[82:83], v98 offset0:8 offset1:12\n"
    "ds_read2st64_b32 v[84:85], v98 offset0:16 offset1:20\nds_read2st64_b32 v[86:87], v98 offset0:24 offset1:28\nv_add_u32 v99, 192, %[rb]\n"
    "ds_read2st64_b32 v[88:89], v99 offset0:0 offset1:4\nds_read2st64_b32 v[90:91], v99 offset0:8 offset1:12\n"
    "ds_read2st64_b32 v[92:93], v99 offset0:16 offset1:20\nds_read2st64_b32 v[94:95], v99 offset0:24 offset1:28\ns_waitcnt lgkmcnt(12)\n"
    "v_perm_b32 v48, v65, v64, s85\nv_perm_b32 v49, v67, v66, s85\nv_perm_b32 v50, v69, v68, s85\nv_perm_b32 v51, v71, v70, s85\n"
    "v_perm_b32 v52, v65, v64, s86\nv_perm_b32 v53, v67, v66, s86\nv_perm_b32 v54, v69, v68, s86\nv_perm_b32 v55, v71, v70, s86\n"
    "v_accvgpr_write_b32 a136, v48\nv_accvgpr_write_b32 a137, v49\nv_accvgpr_write_b32 a138, v50\nv_accvgpr_write_b32 a139, v51\n"
    "v_accvgpr_write_b32 a152, v52\nv_accvgpr_write_b32 a153, v53\nv_accvgpr_write_b32 a154, v54\nv_accvgpr_write_b32 a155, v55\ns_waitcnt lgkmcnt(8)\n"
    "v_perm_b32 v48, v73, v72, s85\nv_perm_b32 v49, v75, v74, s85\nv_perm_b32 v50, v77, v76, s85\nv_perm_b32 v51, v79, v78, s85\n"
    "v_perm_b32 v52, v73, v72, s86\nv_perm_b32 v53, v75, v74, s86\nv_perm_b32 v54, v77, v76, s86\nv_perm_b32 v55, v79, v78, s86\n"
    "v_accvgpr_write_b32 a168, v48\nv_accvgpr_write_b32 a169, v49\nv_accvgpr_write_b32 a170, v50\nv_accvgpr_write_b32 a171, v51\n"
    "v_accvgpr_write_b32 a184, v52\nv_accvgpr_write_b32 a185, v53\nv_accvgpr_write_b32 a186, v54\nv_accvgpr_write_b32 a187, v55\ns_waitcnt lgkmcnt(4)\n"
    "v_perm_b32 v48, v81, v80, s85\nv_perm_b32 v49, v83, v82, s85\nv_perm_b32 v50, v85, v84, s85\nv_perm_b32 v51, v87, v86, s85\n"
    "v_perm_b32 v52, v81, v80, s86\nv_perm_b32 v53, v83, v82, s86\nv_perm_b32 v54, v85, v84, s86\nv_perm_b32 v55, v87, v86, s86\n"
    "v_accvgpr_write_b32 a200, v48\nv_accvgpr_write_b32 a201, v49\nv_accvgpr_write_b32 a202, v50\nv_accvgpr_write_b32 a203, v51\n"
    "v_accvgpr_write_b32 a216, v52\nv_accvgpr_write_b32 a217, v53\nv_accvgpr_write_b32 a218, v54\nv_accvgpr_write_b32 a219, v55\ns_waitcnt lgkmcnt(0)\n"
    "v_perm_b32 v48, v89, v88, s85\nv_perm_b32 v49, v91, v90, s85\nv_perm_b32 v50, v93, v92, s85\nv_perm_b32 v51, v95, v94, s85\n"
    "v_perm_b32 v52, v89, v88, s86\nv_perm_b32 v53, v91, v90, s86\nv_perm_b32 v54, v93, v92, s86\nv_perm_b32 v55, v95, v94, s86\n"
    "v_accvgpr_write_b32 a232, v48\nv_accvgpr_write_b32 a233, v49\nv_accvgpr_write_b32 a234, v50\nv_accvgpr_write_b32 a235, v51\n"
    "v_accvgpr_write_b32 a248, v52\nv_accvgpr_write_b32 a249, v53\nv_accvgpr_write_b32 a250, v54\nv_accvgpr_write_b32 a251, v55\ns_waitcnt vmcnt(0)\n"
    "s_barrier\nv_add_u32 v96, 32768, %[rb]\nds_read2st64_b32 v[64:65], v96 offset0:0 offset1:4\nds_read2st64_b32 v[66:67], v96 offset0:8 offset1:12\n"
    "ds_read2st64_b32 v[68:69], v96 offset0:16 offset1:20\nds_read2st64_b32 v[70:71], v96 offset0:24 offset1:28\nv_add_u32 v97, 32832, %[rb]\n"
    "ds_read2st64_b32 v[72:73], v97 offset0:0 offset1:4\nds_read2st64_b32 v[74:75], v97 offset0:8 offset1:12\n"
    "ds_read2st64_b32 v[76:77], v97 offset0:16 offset1:20\nds_read2st64_b32 v[78:79], v97 offset0:24 offset1:28\nv_add_u32 v98, 32896, %[rb]\n"
    "ds_read2st64_b32 v[80:81], v98 offset0:0 offset1:4\nds_read2st64_b32 v[82:83], v98 offset0:8 offset1:12\n"
    "ds_read2st64_b32 v[84:85], v98 offset0:16 offset1:20\nds_read2st64_b32 v[86:87], v98 offset0:24 offset1:28\nv_add_u32 v99, 32960, %[rb]\n"
    "ds_read2st64_b32 v[88:89], v99 offset0:0 offset1:4\nds_read2st64_b32 v[90:91], v99 offset0:8 offset1:12\n"
    "ds_read2st64_b32 v[92:93], v99 offset0:16 offset1:20\nds_read2st64_b32 v[94:95], v99 offset0:24 offset1:28\ns_waitcnt lgkmcnt(12)\n"
    "v_perm_b32 v48, v65, v64, s85\nv_perm_b32 v49, v67, v66, s85\nv_perm_b32 v50, v69, v68, s85\nv_perm_b32 v51, v71, v70, s85\n"
    "v_perm_b32 v52, v65, v64, s86\nv_perm_b32 v53, v67, v66, s86\nv_perm_b32 v54, v69, v68, s86\nv_perm_b32 v55, v71, v70, s86\n"
    "v_accvgpr_write_b32 a140, v48\nv_accvgpr_write_b32 a141, v49\nv_accvgpr_write_b32 a142, v50\nv_accvgpr_write_b32 a143, v51\n"
    "v_accvgpr_write_b32 a156, v52\nv_accvgpr_write_b32 a157, v53\nv_accvgpr_write_b32 a158, v54\nv_accvgpr_write_b32 a159, v55\ns_waitcnt lgkmcnt(8)\n"
    "v_perm_b32 v48, v73, v72, s85\nv_perm_b32 v49, v75, v74, s85\nv_perm_b32 v50, v77, v76, s85\nv_perm_b32 v51, v79, v78, s85\n"
    "v_perm_b32 v52, v73, v72, s86\nv_perm_b32 v53, v75, v74, s86\nv_perm_b32 v54, v77, v76, s86\nv_perm_b32 v55, v79, v78, s86\n"
    "v_accvgpr_write_b32 a172, v48\nv_accvgpr_write_b32 a173, v49\nv_accvgpr_write_b32 a174, v50\nv_accvgpr_write_b32 a175, v51\n"
    "v_accvgpr_write_b32 a188, v52\nv_accvgpr_write_b32 a189, v53\nv_accvgpr_write_b32 a190, v54\nv_accvgpr_write_b32 a191, v55\ns_waitcnt lgkmcnt(4)\n"
    "v_perm_b32 v48, v81, v80, s85\nv_perm_b32 v49, v83, v82, s85\nv_perm_b32 v50, v85, v84, s85\nv_perm_b32 v51, v87, v86, s85\n"
    "v_perm_b32 v52, v81, v80, s86\nv_perm_b32 v53, v83, v82, s86\nv_perm_b32 v54, v85, v84, s86\nv_perm_b32 v55, v87, v86, s86\n"
    "v_accvgpr_write_b32 a204, v48\nv_accvgpr_write_b32 a205, v49\nv_accvgpr_write_b32 a206, v50\nv_accvgpr_write_b32 a207, v51\n"
    "v_accvgpr_write_b32 a220, v52\nv_accvgpr_write_b32 a221, v53\nv_accvgpr_write_b32 a222, v54\nv_accvgpr_write_b32 a223, v55\ns_waitcnt lgkmcnt(0)\n"
    "v_perm_b32 v48, v89, v88, s85\nv_perm_b32 v49, v91, v90, s85\nv_perm_b32 v50, v93, v92, s85\nv_perm_b32 v51, v95, v94, s85\n"
    "v_perm_b32 v52, v89, v88, s86\nv_perm_b32 v53, v91, v90, s86\nv_perm_b32 v54, v93, v92, s86\nv_perm_b32 v55, v95, v94, s86\n"
    "v_accvgpr_write_b32 a236, v48\nv_accvgpr_write_b32 a237, v49\nv_accvgpr_write_b32 a238, v50\nv_accvgpr_write_b32 a239, v51\n"
    "v_accvgpr_write_b32 a252, v52\nv_accvgpr_write_b32 a253, v53\nv_accvgpr_write_b32 a254, v54\nv_accvgpr_write_b32 a255, v55\ns_barrier\n"
    :: [rw] "s"(rw), [ro] "s"(ro), [ld] "s"(ld), [va] "v"(va), [vb] "v"(vb), [ra] "v"(ra), [rb] "v"(rb)
    : "memory", "scc", CLS, CLV_48_99, CLA_0_255);
}
DI void load_qk(rsrc_t rw, unsigned ld, unsigned va, unsigned ra) {
  asm volatile(
    "s_nop 4\ns_mov_b32 s85, 0x05040100\ns_mov_b32 s86, 0x07060302\ns_add_u32 m0, %[ld], 0\ns_mov_b32 s84, 0\n"
    "buffer_load_dwordx4 %[va], %[rw], s84 offen nt lds\ns_add_u32 m0, m0, 1024\ns_add_u32 s84, s84, 32768\n"
    "buffer_load_dwordx4 %[va], %[rw], s84 offen nt lds\ns_add_u32 m0, m0, 1024\ns_add_u32 s84, s84, 32768\n"
    "buffer_load_dwordx4 %[va], %[rw], s84 offen nt lds\ns_add_u32 m0, m0, 1024\ns_add_u32 s84, s84, 32768\n"
    "buffer_load_dwordx4 %[va], %[rw], s84 offen nt lds\ns_add_u32 m0, m0, 1024\ns_add_u32 s84, s84, 32768\n"
    "buffer_load_dwordx4 %[va], %[rw], s84 offen nt lds\ns_add_u32 m0, m0, 1024\ns_add_u32 s84, s84, 32768\n"
    "buffer_load_dwordx4 %[va], %[rw], s84 offen nt lds\ns_add_u32 m0, m0, 1024\ns_add_u32 s84, s84, 32768\n"
    "buffer_load_dwordx4 %[va], %[rw], s84 offen nt lds\ns_add_u32 m0, m0, 1024\ns_add_u32 s84, s84, 32768\n"
    "buffer_load_dwordx4 %[va], %[rw], s84 offen nt lds\ns_add_u32 m0, %[ld], 32768\ns_mov_b32 s84, 524288\n"
    "buffer_load_dwordx4 %[va], %[rw], s84 offen nt lds\ns_add_u32 m0, m0, 1024\ns_add_u32 s84, s84, 32768\n"
    "buffer_load_dwordx4 %[va], %[rw], s84 offen nt lds\ns_add_u32 m0, m0, 1024\ns_add_u32 s84, s84, 32768\n"
    "buffer_load_dwordx4 %[va], %[rw], s84 offen nt lds\ns_add_u32 m0, m0, 1024\ns_add_u32 s84, s84, 32768\n"
    "buffer_load_dwordx4 %[va], %[rw], s84 offen nt lds\ns_add_u32 m0, m0, 1024\ns_add_u32 s84, s84, 32768\n"
    "buffer_load_dwordx4 %[va], %[rw], s84 offen nt lds\ns_add_u32 m0, m0, 1024\ns_add_u32 s84, s84, 32768\n"
    "buffer_load_dwordx4 %[va], %[rw], s84 offen nt lds\ns_add_u32 m0, m0, 1024\ns_add_u32 s84, s84, 32768\n"
    "buffer_load_dwordx4 %[va], %[rw], s84 offen nt lds\ns_add_u32 m0, m0, 1024\ns_add_u32 s84, s84, 32768\n"
    "buffer_load_dwordx4 %[va], %[rw], s84 offen nt lds\ns_waitcnt vmcnt(8)\ns_barrier\ns_add_u32 m0, %[ld], 65536\ns_mov_b32 s84, 1048576\n"
    "buffer_load_dwordx4 %[va], %[rw], s84 offen nt lds\ns_add_u32 m0, m0, 1024\ns_add_u32 s84, s84, 32768\n"
    "buffer_load_dwordx4 %[va], %[rw], s84 offen nt lds\ns_add_u32 m0, m0, 1024\ns_add_u32 s84, s84, 32768\n"
    "buffer_load_dwordx4 %[va], %[rw], s84 offen nt lds\ns_add_u32 m0, m0, 1024\ns_add_u32 s84, s84, 32768\n"
    "buffer_load_dwordx4 %[va], %[rw], s84 offen nt lds\ns_add_u32 m0, m0, 1024\ns_add_u32 s84, s84, 32768\n"
    "buffer_load_dwordx4 %[va], %[rw], s84 offen nt lds\ns_add_u32 m0, m0, 1024\ns_add_u32 s84, s84, 32768\n"
    "buffer_load_dwordx4 %[va], %[rw], s84 offen nt lds\ns_add_u32 m0, m0, 1024\ns_add_u32 s84, s84, 32768\n"
    "buffer_load_dwordx4 %[va], %[rw], s84 offen nt lds\ns_add_u32 m0, m0, 1024\ns_add_u32 s84, s84, 32768\n"
    "buffer_load_dwordx4 %[va], %[rw], s84 offen nt lds\nv_add_u32 v96, 0, %[ra]\nds_read2st64_b64 v[64:67], v96 offset0:0 offset1:1\n"
    "ds_read2st64_b64 v[68:71], v96 offset0:2 offset1:3\nds_read2st64_b64 v[72:75], v96 offset0:4 offset1:5\n"
    "ds_read2st64_b64 v[76:79], v96 offset0:6 offset1:7\nv_add_u32 v97, 0, %[ra]\nds_read2st64_b64 v[80:83], v97 offset0:16 offset1:17\n"
    "ds_read2st64_b64 v[84:87], v97 offset0:18 offset1:19\nds_read2st64_b64 v[88:91], v97 offset0:20 offset1:21\n"
    "ds_read2st64_b64 v[92:95], v97 offset0:22 offset1:23\ns_waitcnt lgkmcnt(4)\nv_perm_b32 v48, v66, v64, s85\nv_perm_b32 v49, v70, v68, s85\n"
    "v_perm_b32 v50, v74, v72, s85\nv_perm_b32 v51, v78, v76, s85\nv_perm_b32 v52, v66, v64, s86\nv_perm_b32 v53, v70, v68, s86\n"
    "v_perm_b32 v54, v74, v72, s86\nv_perm_b32 v55, v78, v76, s86\nv_perm_b32 v56, v67, v65, s85\nv_perm_b32 v57, v71, v69, s85\n"
    "v_perm_b32 v58, v75, v73, s85\nv_perm_b32 v59, v79, v77, s85\nv_perm_b32 v60, v67, v65, s86\nv_perm_b32 v61, v71, v69, s86\n"
    "v_perm_b32 v62, v75, v73, s86\nv_perm_b32 v63, v79, v77, s86\nv_accvgpr_write_b32 a0, v48\nv_accvgpr_write_b32 a1, v49\nv_accvgpr_write_b32 a2, v50\n"
    "v_accvgpr_write_b32 a3, v51\nv_accvgpr_write_b32 a8, v52\nv_accvgpr_write_b32 a9, v53\nv_accvgpr_write_b32 a10, v54\nv_accvgpr_write_b32 a11, v55\n"
    "v_accvgpr_write_b32 a16, v56\nv_accvgpr_write_b32 a17, v57\nv_accvgpr_write_b32 a18, v58\nv_accvgpr_write_b32 a19, v59\nv_accvgpr_write_b32 a24, v60\n"
    "v_accvgpr_write_b32 a25, v61\nv_accvgpr_write_b32 a26, v62\nv_accvgpr_write_b32 a27, v63\ns_waitcnt lgkmcnt(0)\nv_perm_b32 v48, v82, v80, s85\n"
    "v_perm_b32 v49, v86, v84, s85\nv_perm_b32 v50, v90, v88, s85\nv_perm_b32 v51, v94, v92, s85\nv_perm_b32 v52, v82, v80, s86\n"
    "v_perm_b32 v53, v86, v84, s86\nv_perm_b32 v54, v90, v88, s86\nv_perm_b32 v55, v94, v92, s86\nv_perm_b32 v56, v83, v81, s85\n"
    "v_perm_b32 v57, v87, v85, s85\nv_perm_b32 v58, v91, v89, s85\nv_perm_b32 v59, v95, v93, s85\nv_perm_b32 v60, v83, v81, s86\n"
    "v_perm_b32 v61, v87, v85, s86\nv_perm_b32 v62, v91, v89, s86\nv_perm_b32 v63, v95, v93, s86\nv_accvgpr_write_b32 a4, v48\nv_accvgpr_write_b32 a5, v49\n"
    "v_accvgpr_write_b32 a6, v50\nv_accvgpr_write_b32 a7, v51\nv_accvgpr_write_b32 a12, v52\nv_accvgpr_write_b32 a13, v53\nv_accvgpr_write_b32 a14, v54\n"
    "v_accvgpr_write_b32 a15, v55\nv_accvgpr_write_b32 a20, v56\nv_accvgpr_write_b32 a21, v57\nv_accvgpr_write_b32 a22, v58\nv_accvgpr_write_b32 a23, v59\n"
    "v_accvgpr_write_b32 a28, v60\nv_accvgpr_write_b32 a29, v61\nv_accvgpr_write_b32 a30, v62\nv_accvgpr_write_b32 a31, v63\ns_waitcnt vmcnt(8)\ns_barrier\n"
    "s_add_u32 m0, %[ld], 0\ns_mov_b32 s84, 1572864\nbuffer_load_dwordx4 %[va], %[rw], s84 offen nt lds\ns_add_u32 m0, m0, 1024\ns_add_u32 s84, s84, 32768\n"
    "buffer_load_dwordx4 %[va], %[rw], s84 offen nt lds\ns_add_u32 m0, m0, 1024\ns_add_u32 s84, s84, 32768\n"
    "buffer_load_dwordx4 %[va], %[rw], s84 offen nt lds\ns_add_u32 m0, m0, 1024\ns_add_u32 s84, s84, 32768\n"
    "buffer_load_dwordx4 %[va], %[rw], s84 offen nt lds\ns_add_u32 m0, m0, 1024\ns_add_u32 s84, s84, 32768\n"
    "buffer_load_dwordx4 %[va], %[rw], s84 offen nt lds\ns_add_u32 m0, m0, 1024\ns_add_u32 s84, s84, 32768\n"
    "buffer_load_dwordx4 %[va], %[rw], s84 offen nt lds\ns_add_u32 m0, m0, 1024\ns_add_u32 s84, s84, 32768\n"
    "buffer_load_dwordx4 %[va], %[rw], s84 offen nt lds\ns_add_u32 m0, m0, 1024\ns_add_u32 s84, s84, 32768\n"
    "buffer_load_dwordx4 %[va], %[rw], s84 offen nt lds\nv_add_u32 v96, 32768, %[ra]\nds_read2st64_b64 v[64:67], v96 offset0:0 offset1:1\n"
    "ds_read2st64_b64 v[68:71], v96 offset0:2 offset1:3\nds_read2st64_b64 v[72:75], v96 offset0:4 offset1:5\n"
    "ds_read2st64_b64 v[76:79], v96 offset0:6 offset1:7\nv_add_u32 v97, 32768, %[ra]\nds_read2st64_b64 v[80:83], v97 offset0:16 offset1:17\n"
    "ds_read2st64_b64 v[84:87], v97 offset0:18 offset1:19\nds_read2st64_b64 v[88:91], v97 offset0:20 offset1:21\n"
    "ds_read2st64_b64 v[92:95], v97 offset0:22 offset1:23\ns_waitcnt lgkmcnt(4)\nv_perm_b32 v48, v66, v64, s85\nv_perm_b32 v49, v70, v68, s85\n"
    "v_perm_b32 v50, v74, v72, s85\nv_perm_b32 v51, v78, v76, s85\nv_perm_b32 v52, v66, v64, s86\nv_perm_b32 v53, v70, v68, s86\n"
    "v_perm_b32 v54, v74, v72, s86\nv_perm_b32 v55, v78, v76, s86\nv_perm_b32 v56, v67, v65, s85\nv_perm_b32 v57, v71, v69, s85\n"
    "v_perm_b32 v58, v75, v73, s85\nv_perm_b32 v59, v79, v77, s85\nv_perm_b32 v60, v67, v65, s86\nv_perm_b32 v61, v71, v69, s86\n"
    "v_perm_b32 v62, v75, v73, s86\nv_perm_b32 v63, v79, v77, s86\nv_accvgpr_write_b32 a32, v48\nv_accvgpr_write_b32 a33, v49\nv_accvgpr_write_b32 a34, v50\n"
    "v_accvgpr_write_b32 a35, v51\nv_accvgpr_write_b32 a40, v52\nv_accvgpr_write_b32 a41, v53\nv_accvgpr_write_b32 a42, v54\nv_accvgpr_write_b32 a43, v55\n"
    "v_accvgpr_write_b32 a48, v56\nv_accvgpr_write_b32 a49, v57\nv_accvgpr_write_b32 a50, v58\nv_accvgpr_write_b32 a51, v59\nv_accvgpr_write_b32 a56, v60\n"
    "v_accvgpr_write_b32 a57, v61\nv_accvgpr_write_b32 a58, v62\nv_accvgpr_write_b32 a59, v63\ns_waitcnt lgkmcnt(0)\nv_perm_b32 v48, v82, v80, s85\n"
    "v_perm_b32 v49, v86, v84, s85\nv_perm_b32 v50, v90, v88, s85\nv_perm_b32 v51, v94, v92, s85\nv_perm_b32 v52, v82, v80, s86\n"
    "v_perm_b32 v53, v86, v84, s86\nv_perm_b32 v54, v90, v88, s86\nv_perm_b32 v55, v94, v92, s86\nv_perm_b32 v56, v83, v81, s85\n"
    "v_perm_b32 v57, v87, v85, s85\nv_perm_b32 v58, v91, v89, s85\nv_perm_b32 v59, v95, v93, s85\nv_perm_b32 v60, v83, v81, s86\n"
    "v_perm_b32 v61, v87, v85, s86\nv_perm_b32 v62, v91, v89, s86\nv_perm_b32 v63, v95, v93, s86\nv_accvgpr_write_b32 a36, v48\n"
    "v_accvgpr_write_b32 a37, v49\nv_accvgpr_write_b32 a38, v50\nv_accvgpr_write_b32 a39, v51\nv_accvgpr_write_b32 a44, v52\nv_accvgpr_write_b32 a45, v53\n"
    "v_accvgpr_write_b32 a46, v54\nv_accvgpr_write_b32 a47, v55\nv_accvgpr_write_b32 a52, v56\nv_accvgpr_write_b32 a53, v57\nv_accvgpr_write_b32 a54, v58\n"
    "v_accvgpr_write_b32 a55, v59\nv_accvgpr_write_b32 a60, v60\nv_accvgpr_write_b32 a61, v61\nv_accvgpr_write_b32 a62, v62\nv_accvgpr_write_b32 a63, v63\n"
    "s_waitcnt vmcnt(8)\ns_barrier\ns_add_u32 m0, %[ld], 32768\ns_mov_b32 s84, 2097152\nbuffer_load_dwordx4 %[va], %[rw], s84 offen nt lds\n"
    "s_add_u32 m0, m0, 1024\ns_add_u32 s84, s84, 32768\nbuffer_load_dwordx4 %[va], %[rw], s84 offen nt lds\ns_add_u32 m0, m0, 1024\n"
    "s_add_u32 s84, s84, 32768\nbuffer_load_dwordx4 %[va], %[rw], s84 offen nt lds\ns_add_u32 m0, m0, 1024\ns_add_u32 s84, s84, 32768\n"
    "buffer_load_dwordx4 %[va], %[rw], s84 offen nt lds\ns_add_u32 m0, m0, 1024\ns_add_u32 s84, s84, 32768\n"
    "buffer_load_dwordx4 %[va], %[rw], s84 offen nt lds\ns_add_u32 m0, m0, 1024\ns_add_u32 s84, s84, 32768\n"
    "buffer_load_dwordx4 %[va], %[rw], s84 offen nt lds\ns_add_u32 m0, m0, 1024\ns_add_u32 s84, s84, 32768\n"
    "buffer_load_dwordx4 %[va], %[rw], s84 offen nt lds\ns_add_u32 m0, m0, 1024\ns_add_u32 s84, s84, 32768\n"
    "buffer_load_dwordx4 %[va], %[rw], s84 offen nt lds\nv_add_u32 v96, 65536, %[ra]\nds_read2st64_b64 v[64:67], v96 offset0:0 offset1:1\n"
    "ds_read2st64_b64 v[68:71], v96 offset0:2 offset1:3\nds_read2st64_b64 v[72:75], v96 offset0:4 offset1:5\n"
    "ds_read2st64_b64 v[76:79], v96 offset0:6 offset1:7\nv_add_u32 v97, 65536, %[ra]\nds_read2st64_b64 v[80:83], v97 offset0:16 offset1:17\n"
    "ds_read2st64_b64 v[84:87], v97 offset0:18 offset1:19\nds_read2st64_b64 v[88:91], v97 offset0:20 offset1:21\n"
    "ds_read2st64_b64 v[92:95], v97 offset0:22 offset1:23\ns_waitcnt lgkmcnt(4)\nv_perm_b32 v48, v66, v64, s85\nv_perm_b32 v49, v70, v68, s85\n"
    "v_perm_b32 v50, v74, v72, s85\nv_perm_b32 v51, v78, v76, s85\nv_perm_b32 v52, v66, v64, s86\nv_perm_b32 v53, v70, v68, s86\n"
    "v_perm_b32 v54, v74, v72, s86\nv_perm_b32 v55, v78, v76, s86\nv_perm_b32 v56, v67, v65, s85\nv_perm_b32 v57, v71, v69, s85\n"
    "v_perm_b32 v58, v75, v73, s85\nv_perm_b32 v59, v79, v77, s85\nv_perm_b32 v60, v67, v65, s86\nv_perm_b32 v61, v71, v69, s86\n"
    "v_perm_b32 v62, v75, v73, s86\nv_perm_b32 v63, v79, v77, s86\nv_accvgpr_write_b32 a64, v48\nv_accvgpr_write_b32 a65, v49\nv_accvgpr_write_b32 a66, v50\n"
    "v_accvgpr_write_b32 a67, v51\nv_accvgpr_write_b32 a72, v52\nv_accvgpr_write_b32 a73, v53\nv_accvgpr_write_b32 a74, v54\nv_accvgpr_write_b32 a75, v55\n"
    "v_accvgpr_write_b32 a80, v56\nv_accvgpr_write_b32 a81, v57\nv_accvgpr_write_b32 a82, v58\nv_accvgpr_write_b32 a83, v59\nv_accvgpr_write_b32 a88, v60\n"
    "v_accvgpr_write_b32 a89, v61\nv_accvgpr_write_b32 a90, v62\nv_accvgpr_write_b32 a91, v63\ns_waitcnt lgkmcnt(0)\nv_perm_b32 v48, v82, v80, s85\n"
    "v_perm_b32 v49, v86, v84, s85\nv_perm_b32 v50, v90, v88, s85\nv_perm_b32 v51, v94, v92, s85\nv_perm_b32 v52, v82, v80, s86\n"
    "v_perm_b32 v53, v86, v84, s86\nv_perm_b32 v54, v90, v88, s86\nv_perm_b32 v55, v94, v92, s86\nv_perm_b32 v56, v83, v81, s85\n"
    "v_perm_b32 v57, v87, v85, s85\nv_perm_b32 v58, v91, v89, s85\nv_perm_b32 v59, v95, v93, s85\nv_perm_b32 v60, v83, v81, s86\n"
    "v_perm_b32 v61, v87, v85, s86\nv_perm_b32 v62, v91, v89, s86\nv_perm_b32 v63, v95, v93, s86\nv_accvgpr_write_b32 a68, v48\n"
    "v_accvgpr_write_b32 a69, v49\nv_accvgpr_write_b32 a70, v50\nv_accvgpr_write_b32 a71, v51\nv_accvgpr_write_b32 a76, v52\nv_accvgpr_write_b32 a77, v53\n"
    "v_accvgpr_write_b32 a78, v54\nv_accvgpr_write_b32 a79, v55\nv_accvgpr_write_b32 a84, v56\nv_accvgpr_write_b32 a85, v57\nv_accvgpr_write_b32 a86, v58\n"
    "v_accvgpr_write_b32 a87, v59\nv_accvgpr_write_b32 a92, v60\nv_accvgpr_write_b32 a93, v61\nv_accvgpr_write_b32 a94, v62\nv_accvgpr_write_b32 a95, v63\n"
    "s_waitcnt vmcnt(8)\ns_barrier\ns_add_u32 m0, %[ld], 65536\ns_mov_b32 s84, 2621440\nbuffer_load_dwordx4 %[va], %[rw], s84 offen nt lds\n"
    "s_add_u32 m0, m0, 1024\ns_add_u32 s84, s84, 32768\nbuffer_load_dwordx4 %[va], %[rw], s84 offen nt lds\ns_add_u32 m0, m0, 1024\n"
    "s_add_u32 s84, s84, 32768\nbuffer_load_dwordx4 %[va], %[rw], s84 offen nt lds\ns_add_u32 m0, m0, 1024\ns_add_u32 s84, s84, 32768\n"
    "buffer_load_dwordx4 %[va], %[rw], s84 offen nt lds\ns_add_u32 m0, m0, 1024\ns_add_u32 s84, s84, 32768\n"
    "buffer_load_dwordx4 %[va], %[rw], s84 offen nt lds\ns_add_u32 m0, m0, 1024\ns_add_u32 s84, s84, 32768\n"
    "buffer_load_dwordx4 %[va], %[rw], s84 offen nt lds\ns_add_u32 m0, m0, 1024\ns_add_u32 s84, s84, 32768\n"
    "buffer_load_dwordx4 %[va], %[rw], s84 offen nt lds\ns_add_u32 m0, m0, 1024\ns_add_u32 s84, s84, 32768\n"
    "buffer_load_dwordx4 %[va], %[rw], s84 offen nt lds\nv_add_u32 v96, 0, %[ra]\nds_read2st64_b64 v[64:67], v96 offset0:0 offset1:1\n"
    "ds_read2st64_b64 v[68:71], v96 offset0:2 offset1:3\nds_read2st64_b64 v[72:75], v96 offset0:4 offset1:5\n"
    "ds_read2st64_b64 v[76:79], v96 offset0:6 offset1:7\nv_add_u32 v97, 0, %[ra]\nds_read2st64_b64 v[80:83], v97 offset0:16 offset1:17\n"
    "ds_read2st64_b64 v[84:87], v97 offset0:18 offset1:19\nds_read2st64_b64 v[88:91], v97 offset0:20 offset1:21\n"
    "ds_read2st64_b64 v[92:95], v97 offset0:22 offset1:23\ns_waitcnt lgkmcnt(4)\nv_perm_b32 v48, v66, v64, s85\nv_perm_b32 v49, v70, v68, s85\n"
    "v_perm_b32 v50, v74, v72, s85\nv_perm_b32 v51, v78, v76, s85\nv_perm_b32 v52, v66, v64, s86\nv_perm_b32 v53, v70, v68, s86\n"
    "v_perm_b32 v54, v74, v72, s86\nv_perm_b32 v55, v78, v76, s86\nv_perm_b32 v56, v67, v65, s85\nv_perm_b32 v57, v71, v69, s85\n"
    "v_perm_b32 v58, v75, v73, s85\nv_perm_b32 v59, v79, v77, s85\nv_perm_b32 v60, v67, v65, s86\nv_perm_b32 v61, v71, v69, s86\n"
    "v_perm_b32 v62, v75, v73, s86\nv_perm_b32 v63, v79, v77, s86\nv_accvgpr_write_b32 a96, v48\nv_accvgpr_write_b32 a97, v49\nv_accvgpr_write_b32 a98, v50\n"
    "v_accvgpr_write_b32 a99, v51\nv_accvgpr_write_b32 a104, v52\nv_accvgpr_write_b32 a105, v53\nv_accvgpr_write_b32 a106, v54\n"
    "v_accvgpr_write_b32 a107, v55\nv_accvgpr_write_b32 a112, v56\nv_accvgpr_write_b32 a113, v57\nv_accvgpr_write_b32 a114, v58\n"
    "v_accvgpr_write_b32 a115, v59\nv_accvgpr_write_b32 a120, v60\nv_accvgpr_write_b32 a121, v61\nv_accvgpr_write_b32 a122, v62\n"
    "v_accvgpr_write_b32 a123, v63\ns_waitcnt lgkmcnt(0)\nv_perm_b32 v48, v82, v80, s85\nv_perm_b32 v49, v86, v84, s85\nv_perm_b32 v50, v90, v88, s85\n"
    "v_perm_b32 v51, v94, v92, s85\nv_perm_b32 v52, v82, v80, s86\nv_perm_b32 v53, v86, v84, s86\nv_perm_b32 v54, v90, v88, s86\n"
    "v_perm_b32 v55, v94, v92, s86\nv_perm_b32 v56, v83, v81, s85\nv_perm_b32 v57, v87, v85, s85\nv_perm_b32 v58, v91, v89, s85\n"
    "v_perm_b32 v59, v95, v93, s85\nv_perm_b32 v60, v83, v81, s86\nv_perm_b32 v61, v87, v85, s86\nv_perm_b32 v62, v91, v89, s86\n"
    "v_perm_b32 v63, v95, v93, s86\nv_accvgpr_write_b32 a100, v48\nv_accvgpr_write_b32 a101, v49\nv_accvgpr_write_b32 a102, v50\n"
    "v_accvgpr_write_b32 a103, v51\nv_accvgpr_write_b32 a108, v52\nv_accvgpr_write_b32 a109, v53\nv_accvgpr_write_b32 a110, v54\n"
    "v_accvgpr_write_b32 a111, v55\nv_accvgpr_write_b32 a116, v56\nv_accvgpr_write_b32 a117, v57\nv_accvgpr_write_b32 a118, v58\n"
    "v_accvgpr_write_b32 a119, v59\nv_accvgpr_write_b32 a124, v60\nv_accvgpr_write_b32 a125, v61\nv_accvgpr_write_b32 a126, v62\n"
    "v_accvgpr_write_b32 a127, v63\ns_waitcnt vmcnt(8)\ns_barrier\ns_add_u32 m0, %[ld], 0\ns_mov_b32 s84, 3145728\n"
    "buffer_load_dwordx4 %[va], %[rw], s84 offen nt lds\ns_add_u32 m0, m0, 1024\ns_add_u32 s84, s84, 32768\n"
    "buffer_load_dwordx4 %[va], %[rw], s84 offen nt lds\ns_add_u32 m0, m0, 1024\ns_add_u32 s84, s84, 32768\n"
    "buffer_load_dwordx4 %[va], %[rw], s84 offen nt lds\ns_add_u32 m0, m0, 1024\ns_add_u32 s84, s84, 32768\n"
    "buffer_load_dwordx4 %[va], %[rw], s84 offen nt lds\ns_add_u32 m0, m0, 1024\ns_add_u32 s84, s84, 32768\n"
    "buffer_load_dwordx4 %[va], %[rw], s84 offen nt lds\ns_add_u32 m0, m0, 1024\ns_add_u32 s84, s84, 32768\n"
    "buffer_load_dwordx4 %[va], %[rw], s84 offen nt lds\ns_add_u32 m0, m0, 1024\ns_add_u32 s84, s84, 32768\n"
    "buffer_load_dwordx4 %[va], %[rw], s84 offen nt lds\ns_add_u32 m0, m0, 1024\ns_add_u32 s84, s84, 32768\n"
    "buffer_load_dwordx4 %[va], %[rw], s84 offen nt lds\nv_add_u32 v96, 32768, %[ra]\nds_read2st64_b64 v[64:67], v96 offset0:0 offset1:1\n"
    "ds_read2st64_b64 v[68:71], v96 offset0:2 offset1:3\nds_read2st64_b64 v[72:75], v96 offset0:4 offset1:5\n"
    "ds_read2st64_b64 v[76:79], v96 offset0:6 offset1:7\nv_add_u32 v97, 32768, %[ra]\nds_read2st64_b64 v[80:83], v97 offset0:16 offset1:17\n"
    "ds_read2st64_b64 v[84:87], v97 offset0:18 offset1:19\nds_read2st64_b64 v[88:91], v97 offset0:20 offset1:21\n"
    "ds_read2st64_b64 v[92:95], v97 offset0:22 offset1:23\ns_waitcnt lgkmcnt(4)\nv_perm_b32 v48, v66, v64, s85\nv_perm_b32 v49, v70, v68, s85\n"
    "v_perm_b32 v50, v74, v72, s85\nv_perm_b32 v51, v78, v76, s85\nv_perm_b32 v52, v66, v64, s86\nv_perm_b32 v53, v70, v68, s86\n"
    "v_perm_b32 v54, v74, v72, s86\nv_perm_b32 v55, v78, v76, s86\nv_perm_b32 v56, v67, v65, s85\nv_perm_b32 v57, v71, v69, s85\n"
    "v_perm_b32 v58, v75, v73, s85\nv_perm_b32 v59, v79, v77, s85\nv_perm_b32 v60, v67, v65, s86\nv_perm_b32 v61, v71, v69, s86\n"
    "v_perm_b32 v62, v75, v73, s86\nv_perm_b32 v63, v79, v77, s86\nv_accvgpr_write_b32 a128, v48\nv_accvgpr_write_b32 a129, v49\n"
    "v_accvgpr_write_b32 a130, v50\nv_accvgpr_write_b32 a131, v51\nv_accvgpr_write_b32 a136, v52\nv_accvgpr_write_b32 a137, v53\n"
    "v_accvgpr_write_b32 a138, v54\nv_accvgpr_write_b32 a139, v55\nv_accvgpr_write_b32 a144, v56\nv_accvgpr_write_b32 a145, v57\n"
    "v_accvgpr_write_b32 a146, v58\nv_accvgpr_write_b32 a147, v59\nv_accvgpr_write_b32 a152, v60\nv_accvgpr_write_b32 a153, v61\n"
    "v_accvgpr_write_b32 a154, v62\nv_accvgpr_write_b32 a155, v63\ns_waitcnt lgkmcnt(0)\nv_perm_b32 v48, v82, v80, s85\nv_perm_b32 v49, v86, v84, s85\n"
    "v_perm_b32 v50, v90, v88, s85\nv_perm_b32 v51, v94, v92, s85\nv_perm_b32 v52, v82, v80, s86\nv_perm_b32 v53, v86, v84, s86\n"
    "v_perm_b32 v54, v90, v88, s86\nv_perm_b32 v55, v94, v92, s86\nv_perm_b32 v56, v83, v81, s85\nv_perm_b32 v57, v87, v85, s85\n"
    "v_perm_b32 v58, v91, v89, s85\nv_perm_b32 v59, v95, v93, s85\nv_perm_b32 v60, v83, v81, s86\nv_perm_b32 v61, v87, v85, s86\n"
    "v_perm_b32 v62, v91, v89, s86\nv_perm_b32 v63, v95, v93, s86\nv_accvgpr_write_b32 a132, v48\nv_accvgpr_write_b32 a133, v49\n"
    "v_accvgpr_write_b32 a134, v50\nv_accvgpr_write_b32 a135, v51\nv_accvgpr_write_b32 a140, v52\nv_accvgpr_write_b32 a141, v53\n"
    "v_accvgpr_write_b32 a142, v54\nv_accvgpr_write_b32 a143, v55\nv_accvgpr_write_b32 a148, v56\nv_accvgpr_write_b32 a149, v57\n"
    "v_accvgpr_write_b32 a150, v58\nv_accvgpr_write_b32 a151, v59\nv_accvgpr_write_b32 a156, v60\nv_accvgpr_write_b32 a157, v61\n"
    "v_accvgpr_write_b32 a158, v62\nv_accvgpr_write_b32 a159, v63\ns_waitcnt vmcnt(8)\ns_barrier\ns_add_u32 m0, %[ld], 32768\ns_mov_b32 s84, 3670016\n"
    "buffer_load_dwordx4 %[va], %[rw], s84 offen nt lds\ns_add_u32 m0, m0, 1024\ns_add_u32 s84, s84, 32768\n"
    "buffer_load_dwordx4 %[va], %[rw], s84 offen nt lds\ns_add_u32 m0, m0, 1024\ns_add_u32 s84, s84, 32768\n"
    "buffer_load_dwordx4 %[va], %[rw], s84 offen nt lds\ns_add_u32 m0, m0, 1024\ns_add_u32 s84, s84, 32768\n"
    "buffer_load_dwordx4 %[va], %[rw], s84 offen nt lds\ns_add_u32 m0, m0, 1024\ns_add_u32 s84, s84, 32768\n"
    "buffer_load_dwordx4 %[va], %[rw], s84 offen nt lds\ns_add_u32 m0, m0, 1024\ns_add_u32 s84, s84, 32768\n"
    "buffer_load_dwordx4 %[va], %[rw], s84 offen nt lds\ns_add_u32 m0, m0, 1024\ns_add_u32 s84, s84, 32768\n"
    "buffer_load_dwordx4 %[va], %[rw], s84 offen nt lds\ns_add_u32 m0, m0, 1024\ns_add_u32 s84, s84, 32768\n"
    "buffer_load_dwordx4 %[va], %[rw], s84 offen nt lds\nv_add_u32 v96, 65536, %[ra]\nds_read2st64_b64 v[64:67], v96 offset0:0 offset1:1\n"
    "ds_read2st64_b64 v[68:71], v96 offset0:2 offset1:3\nds_read2st64_b64 v[72:75], v96 offset0:4 offset1:5\n"
    "ds_read2st64_b64 v[76:79], v96 offset0:6 offset1:7\nv_add_u32 v97, 65536, %[ra]\nds_read2st64_b64 v[80:83], v97 offset0:16 offset1:17\n"
    "ds_read2st64_b64 v[84:87], v97 offset0:18 offset1:19\nds_read2st64_b64 v[88:91], v97 offset0:20 offset1:21\n"
    "ds_read2st64_b64 v[92:95], v97 offset0:22 offset1:23\ns_waitcnt lgkmcnt(4)\nv_perm_b32 v48, v66, v64, s85\nv_perm_b32 v49, v70, v68, s85\n"
    "v_perm_b32 v50, v74, v72, s85\nv_perm_b32 v51, v78, v76, s85\nv_perm_b32 v52, v66, v64, s86\nv_perm_b32 v53, v70, v68, s86\n"
    "v_perm_b32 v54, v74, v72, s86\nv_perm_b32 v55, v78, v76, s86\nv_perm_b32 v56, v67, v65, s85\nv_perm_b32 v57, v71, v69, s85\n"
    "v_perm_b32 v58, v75, v73, s85\nv_perm_b32 v59, v79, v77, s85\nv_perm_b32 v60, v67, v65, s86\nv_perm_b32 v61, v71, v69, s86\n"
    "v_perm_b32 v62, v75, v73, s86\nv_perm_b32 v63, v79, v77, s86\nv_accvgpr_write_b32 a160, v48\nv_accvgpr_write_b32 a161, v49\n"
    "v_accvgpr_write_b32 a162, v50\nv_accvgpr_write_b32 a163, v51\nv_accvgpr_write_b32 a168, v52\nv_accvgpr_write_b32 a169, v53\n"
    "v_accvgpr_write_b32 a170, v54\nv_accvgpr_write_b32 a171, v55\nv_accvgpr_write_b32 a176, v56\nv_accvgpr_write_b32 a177, v57\n"
    "v_accvgpr_write_b32 a178, v58\nv_accvgpr_write_b32 a179, v59\nv_accvgpr_write_b32 a184, v60\nv_accvgpr_write_b32 a185, v61\n"
    "v_accvgpr_write_b32 a186, v62\nv_accvgpr_write_b32 a187, v63\ns_waitcnt lgkmcnt(0)\nv_perm_b32 v48, v82, v80, s85\nv_perm_b32 v49, v86, v84, s85\n"
    "v_perm_b32 v50, v90, v88, s85\nv_perm_b32 v51, v94, v92, s85\nv_perm_b32 v52, v82, v80, s86\nv_perm_b32 v53, v86, v84, s86\n"
    "v_perm_b32 v54, v90, v88, s86\nv_perm_b32 v55, v94, v92, s86\nv_perm_b32 v56, v83, v81, s85\nv_perm_b32 v57, v87, v85, s85\n"
    "v_perm_b32 v58, v91, v89, s85\nv_perm_b32 v59, v95, v93, s85\nv_perm_b32 v60, v83, v81, s86\nv_perm_b32 v61, v87, v85, s86\n"
    "v_perm_b32 v62, v91, v89, s86\nv_perm_b32 v63, v95, v93, s86\nv_accvgpr_write_b32 a164, v48\nv_accvgpr_write_b32 a165, v49\n"
    "v_accvgpr_write_b32 a166, v50\nv_accvgpr_write_b32 a167, v51\nv_accvgpr_write_b32 a172, v52\nv_accvgpr_write_b32 a173, v53\n"
    "v_accvgpr_write_b32 a174, v54\nv_accvgpr_write_b32 a175, v55\nv_accvgpr_write_b32 a180, v56\nv_accvgpr_write_b32 a181, v57\n"
    "v_accvgpr_write_b32 a182, v58\nv_accvgpr_write_b32 a183, v59\nv_accvgpr_write_b32 a188, v60\nv_accvgpr_write_b32 a189, v61\n"
    "v_accvgpr_write_b32 a190, v62\nv_accvgpr_write_b32 a191, v63\ns_waitcnt vmcnt(8)\ns_barrier\nv_add_u32 v96, 0, %[ra]\n"
    "ds_read2st64_b64 v[64:67], v96 offset0:0 offset1:1\nds_read2st64_b64 v[68:71], v96 offset0:2 offset1:3\n"
    "ds_read2st64_b64 v[72:75], v96 offset0:4 offset1:5\nds_read2st64_b64 v[76:79], v96 offset0:6 offset1:7\nv_add_u32 v97, 0, %[ra]\n"
    "ds_read2st64_b64 v[80:83], v97 offset0:16 offset1:17\nds_read2st64_b64 v[84:87], v97 offset0:18 offset1:19\n"
    "ds_read2st64_b64 v[88:91], v97 offset0:20 offset1:21\nds_read2st64_b64 v[92:95], v97 offset0:22 offset1:23\ns_waitcnt lgkmcnt(4)\n"
    "v_perm_b32 v48, v66, v64, s85\nv_perm_b32 v49, v70, v68, s85\nv_perm_b32 v50, v74, v72, s85\nv_perm_b32 v51, v78, v76, s85\n"
    "v_perm_b32 v52, v66, v64, s86\nv_perm_b32 v53, v70, v68, s86\nv_perm_b32 v54, v74, v72, s86\nv_perm_b32 v55, v78, v76, s86\n"
    "v_perm_b32 v56, v67, v65, s85\nv_perm_b32 v57, v71, v69, s85\nv_perm_b32 v58, v75, v73, s85\nv_perm_b32 v59, v79, v77, s85\n"
    "v_perm_b32 v60, v67, v65, s86\nv_perm_b32 v61, v71, v69, s86\nv_perm_b32 v62, v75, v73, s86\nv_perm_b32 v63, v79, v77, s86\n"
    "v_accvgpr_write_b32 a192, v48\nv_accvgpr_write_b32 a193, v49\nv_accvgpr_write_b32 a194, v50\nv_accvgpr_write_b32 a195, v51\n"
    "v_accvgpr_write_b32 a200, v52\nv_accvgpr_write_b32 a201, v53\nv_accvgpr_write_b32 a202, v54\nv_accvgpr_write_b32 a203, v55\n"
    "v_accvgpr_write_b32 a208, v56\nv_accvgpr_write_b32 a209, v57\nv_accvgpr_write_b32 a210, v58\nv_accvgpr_write_b32 a211, v59\n"
    "v_accvgpr_write_b32 a216, v60\nv_accvgpr_write_b32 a217, v61\nv_accvgpr_write_b32 a218, v62\nv_accvgpr_write_b32 a219, v63\ns_waitcnt lgkmcnt(0)\n"
    "v_perm_b32 v48, v82, v80, s85\nv_perm_b32 v49, v86, v84, s85\nv_perm_b32 v50, v90, v88, s85\nv_perm_b32 v51, v94, v92, s85\n"
    "v_perm_b32 v52, v82, v80, s86\nv_perm_b32 v53, v86, v84, s86\nv_perm_b32 v54, v90, v88, s86\nv_perm_b32 v55, v94, v92, s86\n"
    "v_perm_b32 v56, v83, v81, s85\nv_perm_b32 v57, v87, v85, s85\nv_perm_b32 v58, v91, v89, s85\nv_perm_b32 v59, v95, v93, s85\n"
    "v_perm_b32 v60, v83, v81, s86\nv_perm_b32 v61, v87, v85, s86\nv_perm_b32 v62, v91, v89, s86\nv_perm_b32 v63, v95, v93, s86\n"
    "v_accvgpr_write_b32 a196, v48\nv_accvgpr_write_b32 a197, v49\nv_accvgpr_write_b32 a198, v50\nv_accvgpr_write_b32 a199, v51\n"
    "v_accvgpr_write_b32 a204, v52\nv_accvgpr_write_b32 a205, v53\nv_accvgpr_write_b32 a206, v54\nv_accvgpr_write_b32 a207, v55\n"
    "v_accvgpr_write_b32 a212, v56\nv_accvgpr_write_b32 a213, v57\nv_accvgpr_write_b32 a214, v58\nv_accvgpr_write_b32 a215, v59\n"
    "v_accvgpr_write_b32 a220, v60\nv_accvgpr_write_b32 a221, v61\nv_accvgpr_write_b32 a222, v62\nv_accvgpr_write_b32 a223, v63\ns_waitcnt vmcnt(0)\n"
    "s_barrier\nv_add_u32 v96, 32768, %[ra]\nds_read2st64_b64 v[64:67], v96 offset0:0 offset1:1\nds_read2st64_b64 v[68:71], v96 offset0:2 offset1:3\n"
    "ds_read2st64_b64 v[72:75], v96 offset0:4 offset1:5\nds_read2st64_b64 v[76:79], v96 offset0:6 offset1:7\nv_add_u32 v97, 32768, %[ra]\n"
    "ds_read2st64_b64 v[80:83], v97 offset0:16 offset1:17\nds_read2st64_b64 v[84:87], v97 offset0:18 offset1:19\n"
    "ds_read2st64_b64 v[88:91], v97 offset0:20 offset1:21\nds_read2st64_b64 v[92:95], v97 offset0:22 offset1:23\ns_waitcnt lgkmcnt(4)\n"
    "v_perm_b32 v48, v66, v64, s85\nv_perm_b32 v49, v70, v68, s85\nv_perm_b32 v50, v74, v72, s85\nv_perm_b32 v51, v78, v76, s85\n"
    "v_perm_b32 v52, v66, v64, s86\nv_perm_b32 v53, v70, v68, s86\nv_perm_b32 v54, v74, v72, s86\nv_perm_b32 v55, v78, v76, s86\n"
    "v_perm_b32 v56, v67, v65, s85\nv_perm_b32 v57, v71, v69, s85\nv_perm_b32 v58, v75, v73, s85\nv_perm_b32 v59, v79, v77, s85\n"
    "v_perm_b32 v60, v67, v65, s86\nv_perm_b32 v61, v71, v69, s86\nv_perm_b32 v62, v75, v73, s86\nv_perm_b32 v63, v79, v77, s86\n"
    "v_accvgpr_write_b32 a224, v48\nv_accvgpr_write_b32 a225, v49\nv_accvgpr_write_b32 a226, v50\nv_accvgpr_write_b32 a227, v51\n"
    "v_accvgpr_write_b32 a232, v52\nv_accvgpr_write_b32 a233, v53\nv_accvgpr_write_b32 a234, v54\nv_accvgpr_write_b32 a235, v55\n"
    "v_accvgpr_write_b32 a240, v56\nv_accvgpr_write_b32 a241, v57\nv_accvgpr_write_b32 a242, v58\nv_accvgpr_write_b32 a243, v59\n"
    "v_accvgpr_write_b32 a248, v60\nv_accvgpr_write_b32 a249, v61\nv_accvgpr_write_b32 a250, v62\nv_accvgpr_write_b32 a251, v63\ns_waitcnt lgkmcnt(0)\n"
    "v_perm_b32 v48, v82, v80, s85\nv_perm_b32 v49, v86, v84, s85\nv_perm_b32 v50, v90, v88, s85\nv_perm_b32 v51, v94, v92, s85\n"
    "v_perm_b32 v52, v82, v80, s86\nv_perm_b32 v53, v86, v84, s86\nv_perm_b32 v54, v90, v88, s86\nv_perm_b32 v55, v94, v92, s86\n"
    "v_perm_b32 v56, v83, v81, s85\nv_perm_b32 v57, v87, v85, s85\nv_perm_b32 v58, v91, v89, s85\nv_perm_b32 v59, v95, v93, s85\n"
    "v_perm_b32 v60, v83, v81, s86\nv_perm_b32 v61, v87, v85, s86\nv_perm_b32 v62, v91, v89, s86\nv_perm_b32 v63, v95, v93, s86\n"
    "v_accvgpr_write_b32 a228, v48\nv_accvgpr_write_b32 a229, v49\nv_accvgpr_write_b32 a230, v50\nv_accvgpr_write_b32 a231, v51\n"
    "v_accvgpr_write_b32 a236, v52\nv_accvgpr_write_b32 a237, v53\nv_accvgpr_write_b32 a238, v54\nv_accvgpr_write_b32 a239, v55\n"
    "v_accvgpr_write_b32 a244, v56\nv_accvgpr_write_b32 a245, v57\nv_accvgpr_write_b32 a246, v58\nv_accvgpr_write_b32 a247, v59\n"
    "v_accvgpr_write_b32 a252, v60\nv_accvgpr_write_b32 a253, v61\nv_accvgpr_write_b32 a254, v62\nv_accvgpr_write_b32 a255, v63\ns_barrier\n"
    :: [rw] "s"(rw), [ld] "s"(ld), [va] "v"(va), [ra] "v"(ra)
    : "memory", "scc", CLS, CLV_48_99, CLA_0_255);
}
DI void load_gu(rsrc_t rw, rsrc_t ro, unsigned ld, unsigned va, unsigned ra) {
  asm volatile(
    "s_nop 4\ns_mov_b32 s85, 0x05040100\ns_mov_b32 s86, 0x07060302\ns_add_u32 m0, %[ld], 0\ns_mov_b32 s84, 0\n"
    "buffer_load_dwordx4 %[va], %[rw], s84 offen nt lds\ns_add_u32 m0, m0, 1024\ns_add_u32 s84, s84, 14336\n"
    "buffer_load_dwordx4 %[va], %[rw], s84 offen nt lds\ns_add_u32 m0, m0, 1024\ns_add_u32 s84, s84, 14336\n"
    "buffer_load_dwordx4 %[va], %[rw], s84 offen nt lds\ns_add_u32 m0, m0, 1024\ns_add_u32 s84, s84, 14336\n"
    "buffer_load_dwordx4 %[va], %[rw], s84 offen nt lds\ns_add_u32 m0, m0, 1024\ns_add_u32 s84, s84, 14336\n"
    "buffer_load_dwordx4 %[va], %[rw], s84 offen nt lds\ns_add_u32 m0, m0, 1024\ns_add_u32 s84, s84, 14336\n"
    "buffer_load_dwordx4 %[va], %[rw], s84 offen nt lds\ns_add_u32 m0, m0, 1024\ns_add_u32 s84, s84, 14336\n"
    "buffer_load_dwordx4 %[va], %[rw], s84 offen nt lds\ns_add_u32 m0, m0, 1024\ns_add_u32 s84, s84, 14336\n"
    "buffer_load_dwordx4 %[va], %[rw], s84 offen nt lds\ns_add_u32 m0, %[ld], 32768\ns_mov_b32 s84, 0\nbuffer_load_dwordx4 %[va], %[ro], s84 offen nt lds\n"
    "s_add_u32 m0, m0, 1024\ns_add_u32 s84, s84, 14336\nbuffer_load_dwordx4 %[va], %[ro], s84 offen nt lds\ns_add_u32 m0, m0, 1024\n"
    "s_add_u32 s84, s84, 14336\nbuffer_load_dwordx4 %[va], %[ro], s84 offen nt lds\ns_add_u32 m0, m0, 1024\ns_add_u32 s84, s84, 14336\n"
    "buffer_load_dwordx4 %[va], %[ro], s84 offen nt lds\ns_add_u32 m0, m0, 1024\ns_add_u32 s84, s84, 14336\n"
    "buffer_load_dwordx4 %[va], %[ro], s84 offen nt lds\ns_add_u32 m0, m0, 1024\ns_add_u32 s84, s84, 14336\n"
    "buffer_load_dwordx4 %[va], %[ro], s84 offen nt lds\ns_add_u32 m0, m0, 1024\ns_add_u32 s84, s84, 14336\n"
    "buffer_load_dwordx4 %[va], %[ro], s84 offen nt lds\ns_add_u32 m0, m0, 1024\ns_add_u32 s84, s84, 14336\n"
    "buffer_load_dwordx4 %[va], %[ro], s84 offen nt lds\ns_waitcnt vmcnt(8)\ns_barrier\ns_add_u32 m0, %[ld], 65536\ns_mov_b32 s84, 458752\n"
    "buffer_load_dwordx4 %[va], %[rw], s84 offen nt lds\ns_add_u32 m0, m0, 1024\ns_add_u32 s84, s84, 14336\n"
    "buffer_load_dwordx4 %[va], %[rw], s84 offen nt lds\ns_add_u32 m0, m0, 1024\ns_add_u32 s84, s84, 14336\n"
    "buffer_load_dwordx4 %[va], %[rw], s84 offen nt lds\ns_add_u32 m0, m0, 1024\ns_add_u32 s84, s84, 14336\n"
    "buffer_load_dwordx4 %[va], %[rw], s84 offen nt lds\ns_add_u32 m0, m0, 1024\ns_add_u32 s84, s84, 14336\n"
    "buffer_load_dwordx4 %[va], %[rw], s84 offen nt lds\ns_add_u32 m0, m0, 1024\ns_add_u32 s84, s84, 14336\n"
    "buffer_load_dwordx4 %[va], %[rw], s84 offen nt lds\ns_add_u32 m0, m0, 1024\ns_add_u32 s84, s84, 14336\n"
    "buffer_load_dwordx4 %[va], %[rw], s84 offen nt lds\ns_add_u32 m0, m0, 1024\ns_add_u32 s84, s84, 14336\n"
    "buffer_load_dwordx4 %[va], %[rw], s84 offen nt lds\nv_add_u32 v96, 0, %[ra]\nds_read2st64_b32 v[64:65], v96 offset0:0 offset1:1\n"
    "ds_read2st64_b32 v[66:67], v96 offset0:2 offset1:3\nds_read2st64_b32 v[68:69], v96 offset0:4 offset1:5\n"
    "ds_read2st64_b32 v[70:71], v96 offset0:6 offset1:7\nv_add_u32 v97, 0, %[ra]\nds_read2st64_b32 v[72:73], v97 offset0:32 offset1:33\n"
    "ds_read2st64_b32 v[74:75], v97 offset0:34 offset1:35\nds_read2st64_b32 v[76:77], v97 offset0:36 offset1:37\n"
    "ds_read2st64_b32 v[78:79], v97 offset0:38 offset1:39\nv_add_u32 v98, 0, %[ra]\nds_read2st64_b32 v[80:81], v98 offset0:64 offset1:65\n"
    "ds_read2st64_b32 v[82:83], v98 offset0:66 offset1:67\nds_read2st64_b32 v[84:85], v98 offset0:68 offset1:69\n"
    "ds_read2st64_b32 v[86:87], v98 offset0:70 offset1:71\nv_add_u32 v99, 0, %[ra]\nds_read2st64_b32 v[88:89], v99 offset0:96 offset1:97\n"
    "ds_read2st64_b32 v[90:91], v99 offset0:98 offset1:99\nds_read2st64_b32 v[92:93], v99 offset0:100 offset1:101\n"
    "ds_read2st64_b32 v[94:95], v99 offset0:102 offset1:103\ns_waitcnt lgkmcnt(12)\nv_perm_b32 v48, v65, v64, s85\nv_perm_b32 v49, v67, v66, s85\n"
    "v_perm_b32 v50, v69, v68, s85\nv_perm_b32 v51, v71, v70, s85\nv_perm_b32 v52, v65, v64, s86\nv_perm_b32 v53, v67, v66, s86\n"
    "v_perm_b32 v54, v69, v68, s86\nv_perm_b32 v55, v71, v70, s86\nv_accvgpr_write_b32 a0, v48\nv_accvgpr_write_b32 a1, v49\nv_accvgpr_write_b32 a2, v50\n"
    "v_accvgpr_write_b32 a3, v51\nv_accvgpr_write_b32 a4, v52\nv_accvgpr_write_b32 a5, v53\nv_accvgpr_write_b32 a6, v54\nv_accvgpr_write_b32 a7, v55\n"
    "s_waitcnt lgkmcnt(8)\nv_perm_b32 v48, v73, v72, s85\nv_perm_b32 v49, v75, v74, s85\nv_perm_b32 v50, v77, v76, s85\nv_perm_b32 v51, v79, v78, s85\n"
    "v_perm_b32 v52, v73, v72, s86\nv_perm_b32 v53, v75, v74, s86\nv_perm_b32 v54, v77, v76, s86\nv_perm_b32 v55, v79, v78, s86\n"
    "v_accvgpr_write_b32 a16, v48\nv_accvgpr_write_b32 a17, v49\nv_accvgpr_write_b32 a18, v50\nv_accvgpr_write_b32 a19, v51\nv_accvgpr_write_b32 a20, v52\n"
    "v_accvgpr_write_b32 a21, v53\nv_accvgpr_write_b32 a22, v54\nv_accvgpr_write_b32 a23, v55\ns_waitcnt lgkmcnt(4)\nv_perm_b32 v48, v81, v80, s85\n"
    "v_perm_b32 v49, v83, v82, s85\nv_perm_b32 v50, v85, v84, s85\nv_perm_b32 v51, v87, v86, s85\nv_perm_b32 v52, v81, v80, s86\n"
    "v_perm_b32 v53, v83, v82, s86\nv_perm_b32 v54, v85, v84, s86\nv_perm_b32 v55, v87, v86, s86\nv_accvgpr_write_b32 a32, v48\n"
    "v_accvgpr_write_b32 a33, v49\nv_accvgpr_write_b32 a34, v50\nv_accvgpr_write_b32 a35, v51\nv_accvgpr_write_b32 a36, v52\nv_accvgpr_write_b32 a37, v53\n"
    "v_accvgpr_write_b32 a38, v54\nv_accvgpr_write_b32 a39, v55\ns_waitcnt lgkmcnt(0)\nv_perm_b32 v48, v89, v88, s85\nv_perm_b32 v49, v91, v90, s85\n"
    "v_perm_b32 v50, v93, v92, s85\nv_perm_b32 v51, v95, v94, s85\nv_perm_b32 v52, v89, v88, s86\nv_perm_b32 v53, v91, v90, s86\n"
    "v_perm_b32 v54, v93, v92, s86\nv_perm_b32 v55, v95, v94, s86\nv_accvgpr_write_b32 a48, v48\nv_accvgpr_write_b32 a49, v49\nv_accvgpr_write_b32 a50, v50\n"
    "v_accvgpr_write_b32 a51, v51\nv_accvgpr_write_b32 a52, v52\nv_accvgpr_write_b32 a53, v53\nv_accvgpr_write_b32 a54, v54\nv_accvgpr_write_b32 a55, v55\n"
    "s_waitcnt vmcnt(8)\ns_barrier\ns_add_u32 m0, %[ld], 0\ns_mov_b32 s84, 458752\nbuffer_load_dwordx4 %[va], %[ro], s84 offen nt lds\n"
    "s_add_u32 m0, m0, 1024\ns_add_u32 s84, s84, 14336\nbuffer_load_dwordx4 %[va], %[ro], s84 offen nt lds\ns_add_u32 m0, m0, 1024\n"
    "s_add_u32 s84, s84, 14336\nbuffer_load_dwordx4 %[va], %[ro], s84 offen nt lds\ns_add_u32 m0, m0, 1024\ns_add_u32 s84, s84, 14336\n"
    "buffer_load_dwordx4 %[va], %[ro], s84 offen nt lds\ns_add_u32 m0, m0, 1024\ns_add_u32 s84, s84, 14336\n"
    "buffer_load_dwordx4 %[va], %[ro], s84 offen nt lds\ns_add_u32 m0, m0, 1024\ns_add_u32 s84, s84, 14336\n"
    "buffer_load_dwordx4 %[va], %[ro], s84 offen nt lds\ns_add_u32 m0, m0, 1024\ns_add_u32 s84, s84, 14336\n"
    "buffer_load_dwordx4 %[va], %[ro], s84 offen nt lds\ns_add_u32 m0, m0, 1024\ns_add_u32 s84, s84, 14336\n"
    "buffer_load_dwordx4 %[va], %[ro], s84 offen nt lds\nv_add_u32 v96, 32768, %[ra]\nds_read2st64_b32 v[64:65], v96 offset0:0 offset1:1\n"
    "ds_read2st64_b32 v[66:67], v96 offset0:2 offset1:3\nds_read2st64_b32 v[68:69], v96 offset0:4 offset1:5\n"
    "ds_read2st64_b32 v[70:71], v96 offset0:6 offset1:7\nv_add_u32 v97, 32768, %[ra]\nds_read2st64_b32 v[72:73], v97 offset0:32 offset1:33\n"
    "ds_read2st64_b32 v[74:75], v97 offset0:34 offset1:35\nds_read2st64_b32 v[76:77], v97 offset0:36 offset1:37\n"
    "ds_read2st64_b32 v[78:79], v97 offset0:38 offset1:39\nv_add_u32 v98, 32768, %[ra]\nds_read2st64_b32 v[80:81], v98 offset0:64 offset1:65\n"
    "ds_read2st64_b32 v[82:83], v98 offset0:66 offset1:67\nds_read2st64_b32 v[84:85], v98 offset0:68 offset1:69\n"
    "ds_read2st64_b32 v[86:87], v98 offset0:70 offset1:71\nv_add_u32 v99, 32768, %[ra]\nds_read2st64_b32 v[88:89], v99 offset0:96 offset1:97\n"
    "ds_read2st64_b32 v[90:91], v99 offset0:98 offset1:99\nds_read2st64_b32 v[92:93], v99 offset0:100 offset1:101\n"
    "ds_read2st64_b32 v[94:95], v99 offset0:102 offset1:103\ns_waitcnt lgkmcnt(12)\nv_perm_b32 v48, v65, v64, s85\nv_perm_b32 v49, v67, v66, s85\n"
    "v_perm_b32 v50, v69, v68, s85\nv_perm_b32 v51, v71, v70, s85\nv_perm_b32 v52, v65, v64, s86\nv_perm_b32 v53, v67, v66, s86\n"
    "v_perm_b32 v54, v69, v68, s86\nv_perm_b32 v55, v71, v70, s86\nv_accvgpr_write_b32 a8, v48\nv_accvgpr_write_b32 a9, v49\nv_accvgpr_write_b32 a10, v50\n"
    "v_accvgpr_write_b32 a11, v51\nv_accvgpr_write_b32 a12, v52\nv_accvgpr_write_b32 a13, v53\nv_accvgpr_write_b32 a14, v54\nv_accvgpr_write_b32 a15, v55\n"
    "s_waitcnt lgkmcnt(8)\nv_perm_b32 v48, v73, v72, s85\nv_perm_b32 v49, v75, v74, s85\nv_perm_b32 v50, v77, v76, s85\nv_perm_b32 v51, v79, v78, s85\n"
    "v_perm_b32 v52, v73, v72, s86\nv_perm_b32 v53, v75, v74, s86\nv_perm_b32 v54, v77, v76, s86\nv_perm_b32 v55, v79, v78, s86\n"
    "v_accvgpr_write_b32 a24, v48\nv_accvgpr_write_b32 a25, v49\nv_accvgpr_write_b32 a26, v50\nv_accvgpr_write_b32 a27, v51\nv_accvgpr_write_b32 a28, v52\n"
    "v_accvgpr_write_b32 a29, v53\nv_accvgpr_write_b32 a30, v54\nv_accvgpr_write_b32 a31, v55\ns_waitcnt lgkmcnt(4)\nv_perm_b32 v48, v81, v80, s85\n"
    "v_perm_b32 v49, v83, v82, s85\nv_perm_b32 v50, v85, v84, s85\nv_perm_b32 v51, v87, v86, s85\nv_perm_b32 v52, v81, v80, s86\n"
    "v_perm_b32 v53, v83, v82, s86\nv_perm_b32 v54, v85, v84, s86\nv_perm_b32 v55, v87, v86, s86\nv_accvgpr_write_b32 a40, v48\n"
    "v_accvgpr_write_b32 a41, v49\nv_accvgpr_write_b32 a42, v50\nv_accvgpr_write_b32 a43, v51\nv_accvgpr_write_b32 a44, v52\nv_accvgpr_write_b32 a45, v53\n"
    "v_accvgpr_write_b32 a46, v54\nv_accvgpr_write_b32 a47, v55\ns_waitcnt lgkmcnt(0)\nv_perm_b32 v48, v89, v88, s85\nv_perm_b32 v49, v91, v90, s85\n"
    "v_perm_b32 v50, v93, v92, s85\nv_perm_b32 v51, v95, v94, s85\nv_perm_b32 v52, v89, v88, s86\nv_perm_b32 v53, v91, v90, s86\n"
    "v_perm_b32 v54, v93, v92, s86\nv_perm_b32 v55, v95, v94, s86\nv_accvgpr_write_b32 a56, v48\nv_accvgpr_write_b32 a57, v49\nv_accvgpr_write_b32 a58, v50\n"
    "v_accvgpr_write_b32 a59, v51\nv_accvgpr_write_b32 a60, v52\nv_accvgpr_write_b32 a61, v53\nv_accvgpr_write_b32 a62, v54\nv_accvgpr_write_b32 a63, v55\n"
    "s_waitcnt vmcnt(8)\ns_barrier\ns_add_u32 m0, %[ld], 32768\ns_mov_b32 s84, 917504\nbuffer_load_dwordx4 %[va], %[rw], s84 offen nt lds\n"
    "s_add_u32 m0, m0, 1024\ns_add_u32 s84, s84, 14336\nbuffer_load_dwordx4 %[va], %[rw], s84 offen nt lds\ns_add_u32 m0, m0, 1024\n"
    "s_add_u32 s84, s84, 14336\nbuffer_load_dwordx4 %[va], %[rw], s84 offen nt lds\ns_add_u32 m0, m0, 1024\ns_add_u32 s84, s84, 14336\n"
    "buffer_load_dwordx4 %[va], %[rw], s84 offen nt lds\ns_add_u32 m0, m0, 1024\ns_add_u32 s84, s84, 14336\n"
    "buffer_load_dwordx4 %[va], %[rw], s84 offen nt lds\ns_add_u32 m0, m0, 1024\ns_add_u32 s84, s84, 14336\n"
    "buffer_load_dwordx4 %[va], %[rw], s84 offen nt lds\ns_add_u32 m0, m0, 1024\ns_add_u32 s84, s84, 14336\n"
    "buffer_load_dwordx4 %[va], %[rw], s84 offen nt lds\ns_add_u32 m0, m0, 1024\ns_add_u32 s84, s84, 14336\n"
    "buffer_load_dwordx4 %[va], %[rw], s84 offen nt lds\nv_add_u32 v96, 65536, %[ra]\nds_read2st64_b32 v[64:65], v96 offset0:0 offset1:1\n"
    "ds_read2st64_b32 v[66:67], v96 offset0:2 offset1:3\nds_read2st64_b32 v[68:69], v96 offset0:4 offset1:5\n"
    "ds_read2st64_b32 v[70:71], v96 offset0:6 offset1:7\nv_add_u32 v97, 65536, %[ra]\nds_read2st64_b32 v[72:73], v97 offset0:32 offset1:33\n"
    "ds_read2st64_b32 v[74:75], v97 offset0:34 offset1:35\nds_read2st64_b32 v[76:77], v97 offset0:36 offset1:37\n"
    "ds_read2st64_b32 v[78:79], v97 offset0:38 offset1:39\nv_add_u32 v98, 65536, %[ra]\nds_read2st64_b32 v[80:81], v98 offset0:64 offset1:65\n"
    "ds_read2st64_b32 v[82:83], v98 offset0:66 offset1:67\nds_read2st64_b32 v[84:85], v98 offset0:68 offset1:69\n"
    "ds_read2st64_b32 v[86:87], v98 offset0:70 offset1:71\nv_add_u32 v99, 65536, %[ra]\nds_read2st64_b32 v[88:89], v99 offset0:96 offset1:97\n"
    "ds_read2st64_b32 v[90:91], v99 offset0:98 offset1:99\nds_read2st64_b32 v[92:93], v99 offset0:100 offset1:101\n"
    "ds_read2st64_b32 v[94:95], v99 offset0:102 offset1:103\ns_waitcnt lgkmcnt(12)\nv_perm_b32 v48, v65, v64, s85\nv_perm_b32 v49, v67, v66, s85\n"
    "v_perm_b32 v50, v69, v68, s85\nv_perm_b32 v51, v71, v70, s85\nv_perm_b32 v52, v65, v64, s86\nv_perm_b32 v53, v67, v66, s86\n"
    "v_perm_b32 v54, v69, v68, s86\nv_perm_b32 v55, v71, v70, s86\nv_accvgpr_write_b32 a64, v48\nv_accvgpr_write_b32 a65, v49\nv_accvgpr_write_b32 a66, v50\n"
    "v_accvgpr_write_b32 a67, v51\nv_accvgpr_write_b32 a68, v52\nv_accvgpr_write_b32 a69, v53\nv_accvgpr_write_b32 a70, v54\nv_accvgpr_write_b32 a71, v55\n"
    "s_waitcnt lgkmcnt(8)\nv_perm_b32 v48, v73, v72, s85\nv_perm_b32 v49, v75, v74, s85\nv_perm_b32 v50, v77, v76, s85\nv_perm_b32 v51, v79, v78, s85\n"
    "v_perm_b32 v52, v73, v72, s86\nv_perm_b32 v53, v75, v74, s86\nv_perm_b32 v54, v77, v76, s86\nv_perm_b32 v55, v79, v78, s86\n"
    "v_accvgpr_write_b32 a80, v48\nv_accvgpr_write_b32 a81, v49\nv_accvgpr_write_b32 a82, v50\nv_accvgpr_write_b32 a83, v51\nv_accvgpr_write_b32 a84, v52\n"
    "v_accvgpr_write_b32 a85, v53\nv_accvgpr_write_b32 a86, v54\nv_accvgpr_write_b32 a87, v55\ns_waitcnt lgkmcnt(4)\nv_perm_b32 v48, v81, v80, s85\n"
    "v_perm_b32 v49, v83, v82, s85\nv_perm_b32 v50, v85, v84, s85\nv_perm_b32 v51, v87, v86, s85\nv_perm_b32 v52, v81, v80, s86\n"
    "v_perm_b32 v53, v83, v82, s86\nv_perm_b32 v54, v85, v84, s86\nv_perm_b32 v55, v87, v86, s86\nv_accvgpr_write_b32 a96, v48\n"
    "v_accvgpr_write_b32 a97, v49\nv_accvgpr_write_b32 a98, v50\nv_accvgpr_write_b32 a99, v51\nv_accvgpr_write_b32 a100, v52\nv_accvgpr_write_b32 a101, v53\n"
    "v_accvgpr_write_b32 a102, v54\nv_accvgpr_write_b32 a103, v55\ns_waitcnt lgkmcnt(0)\nv_perm_b32 v48, v89, v88, s85\nv_perm_b32 v49, v91, v90, s85\n"
    "v_perm_b32 v50, v93, v92, s85\nv_perm_b32 v51, v95, v94, s85\nv_perm_b32 v52, v89, v88, s86\nv_perm_b32 v53, v91, v90, s86\n"
    "v_perm_b32 v54, v93, v92, s86\nv_perm_b32 v55, v95, v94, s86\nv_accvgpr_write_b32 a112, v48\nv_accvgpr_write_b32 a113, v49\n"
    "v_accvgpr_write_b32 a114, v50\nv_accvgpr_write_b32 a115, v51\nv_accvgpr_write_b32 a116, v52\nv_accvgpr_write_b32 a117, v53\n"
    "v_accvgpr_write_b32 a118, v54\nv_accvgpr_write_b32 a119, v55\ns_waitcnt vmcnt(8)\ns_barrier\ns_add_u32 m0, %[ld], 65536\ns_mov_b32 s84, 917504\n"
    "buffer_load_dwordx4 %[va], %[ro], s84 offen nt lds\ns_add_u32 m0, m0, 1024\ns_add_u32 s84, s84, 14336\n"
    "buffer_load_dwordx4 %[va], %[ro], s84 offen nt lds\ns_add_u32 m0, m0, 1024\ns_add_u32 s84, s84, 14336\n"
    "buffer_load_dwordx4 %[va], %[ro], s84 offen nt lds\ns_add_u32 m0, m0, 1024\ns_add_u32 s84, s84, 14336\n"
    "buffer_load_dwordx4 %[va], %[ro], s84 offen nt lds\ns_add_u32 m0, m0, 1024\ns_add_u32 s84, s84, 14336\n"
    "buffer_load_dwordx4 %[va], %[ro], s84 offen nt lds\ns_add_u32 m0, m0, 1024\ns_add_u32 s84, s84, 14336\n"
    "buffer_load_dwordx4 %[va], %[ro], s84 offen nt lds\ns_add_u32 m0, m0, 1024\ns_add_u32 s84, s84, 14336\n"
    "buffer_load_dwordx4 %[va], %[ro], s84 offen nt lds\ns_add_u32 m0, m0, 1024\ns_add_u32 s84, s84, 14336\n"
    "buffer_load_dwordx4 %[va], %[ro], s84 offen nt lds\nv_add_u32 v96, 0, %[ra]\nds_read2st64_b32 v[64:65], v96 offset0:0 offset1:1\n"
    "ds_read2st64_b32 v[66:67], v96 offset0:2 offset1:3\nds_read2st64_b32 v[68:69], v96 offset0:4 offset1:5\n"
    "ds_read2st64_b32 v[70:71], v96 offset0:6 offset1:7\nv_add_u32 v97, 0, %[ra]\nds_read2st64_b32 v[72:73], v97 offset0:32 offset1:33\n"
    "ds_read2st64_b32 v[74:75], v97 offset0:34 offset1:35\nds_read2st64_b32 v[76:77], v97 offset0:36 offset1:37\n"
    "ds_read2st64_b32 v[78:79], v97 offset0:38 offset1:39\nv_add_u32 v98, 0, %[ra]\nds_read2st64_b32 v[80:81], v98 offset0:64 offset1:65\n"
    "ds_read2st64_b32 v[82:83], v98 offset0:66 offset1:67\nds_read2st64_b32 v[84:85], v98 offset0:68 offset1:69\n"
    "ds_read2st64_b32 v[86:87], v98 offset0:70 offset1:71\nv_add_u32 v99, 0, %[ra]\nds_read2st64_b32 v[88:89], v99 offset0:96 offset1:97\n"
    "ds_read2st64_b32 v[90:91], v99 offset0:98 offset1:99\nds_read2st64_b32 v[92:93], v99 offset0:100 offset1:101\n"
    "ds_read2st64_b32 v[94:95], v99 offset0:102 offset1:103\ns_waitcnt lgkmcnt(12)\nv_perm_b32 v48, v65, v64, s85\nv_perm_b32 v49, v67, v66, s85\n"
    "v_perm_b32 v50, v69, v68, s85\nv_perm_b32 v51, v71, v70, s85\nv_perm_b32 v52, v65, v64, s86\nv_perm_b32 v53, v67, v66, s86\n"
    "v_perm_b32 v54, v69, v68, s86\nv_perm_b32 v55, v71, v70, s86\nv_accvgpr_write_b32 a72, v48\nv_accvgpr_write_b32 a73, v49\nv_accvgpr_write_b32 a74, v50\n"
    "v_accvgpr_write_b32 a75, v51\nv_accvgpr_write_b32 a76, v52\nv_accvgpr_write_b32 a77, v53\nv_accvgpr_write_b32 a78, v54\nv_accvgpr_write_b32 a79, v55\n"
    "s_waitcnt lgkmcnt(8)\nv_perm_b32 v48, v73, v72, s85\nv_perm_b32 v49, v75, v74, s85\nv_perm_b32 v50, v77, v76, s85\nv_perm_b32 v51, v79, v78, s85\n"
    "v_perm_b32 v52, v73, v72, s86\nv_perm_b32 v53, v75, v74, s86\nv_perm_b32 v54, v77, v76, s86\nv_perm_b32 v55, v79, v78, s86\n"
    "v_accvgpr_write_b32 a88, v48\nv_accvgpr_write_b32 a89, v49\nv_accvgpr_write_b32 a90, v50\nv_accvgpr_write_b32 a91, v51\nv_accvgpr_write_b32 a92, v52\n"
    "v_accvgpr_write_b32 a93, v53\nv_accvgpr_write_b32 a94, v54\nv_accvgpr_write_b32 a95, v55\ns_waitcnt lgkmcnt(4)\nv_perm_b32 v48, v81, v80, s85\n"
    "v_perm_b32 v49, v83, v82, s85\nv_perm_b32 v50, v85, v84, s85\nv_perm_b32 v51, v87, v86, s85\nv_perm_b32 v52, v81, v80, s86\n"
    "v_perm_b32 v53, v83, v82, s86\nv_perm_b32 v54, v85, v84, s86\nv_perm_b32 v55, v87, v86, s86\nv_accvgpr_write_b32 a104, v48\n"
    "v_accvgpr_write_b32 a105, v49\nv_accvgpr_write_b32 a106, v50\nv_accvgpr_write_b32 a107, v51\nv_accvgpr_write_b32 a108, v52\n"
    "v_accvgpr_write_b32 a109, v53\nv_accvgpr_write_b32 a110, v54\nv_accvgpr_write_b32 a111, v55\ns_waitcnt lgkmcnt(0)\nv_perm_b32 v48, v89, v88, s85\n"
    "v_perm_b32 v49, v91, v90, s85\nv_perm_b32 v50, v93, v92, s85\nv_perm_b32 v51, v95, v94, s85\nv_perm_b32 v52, v89, v88, s86\n"
    "v_perm_b32 v53, v91, v90, s86\nv_perm_b32 v54, v93, v92, s86\nv_perm_b32 v55, v95, v94, s86\nv_accvgpr_write_b32 a120, v48\n"
    "v_accvgpr_write_b32 a121, v49\nv_accvgpr_write_b32 a122, v50\nv_accvgpr_write_b32 a123, v51\nv_accvgpr_write_b32 a124, v52\n"
    "v_accvgpr_write_b32 a125, v53\nv_accvgpr_write_b32 a126, v54\nv_accvgpr_write_b32 a127, v55\ns_waitcnt vmcnt(8)\ns_barrier\ns_add_u32 m0, %[ld], 0\n"
    "s_mov_b32 s84, 1376256\nbuffer_load_dwordx4 %[va], %[rw], s84 offen nt lds\ns_add_u32 m0, m0, 1024\ns_add_u32 s84, s84, 14336\n"
    "buffer_load_dwordx4 %[va], %[rw], s84 offen nt lds\ns_add_u32 m0, m0, 1024\ns_add_u32 s84, s84, 14336\n"
    "buffer_load_dwordx4 %[va], %[rw], s84 offen nt lds\ns_add_u32 m0, m0, 1024\ns_add_u32 s84, s84, 14336\n"
    "buffer_load_dwordx4 %[va], %[rw], s84 offen nt lds\ns_add_u32 m0, m0, 1024\ns_add_u32 s84, s84, 14336\n"
    "buffer_load_dwordx4 %[va], %[rw], s84 offen nt lds\ns_add_u32 m0, m0, 1024\ns_add_u32 s84, s84, 14336\n"
    "buffer_load_dwordx4 %[va], %[rw], s84 offen nt lds\ns_add_u32 m0, m0, 1024\ns_add_u32 s84, s84, 14336\n"
    "buffer_load_dwordx4 %[va], %[rw], s84 offen nt lds\ns_add_u32 m0, m0, 1024\ns_add_u32 s84, s84, 14336\n"
    "buffer_load_dwordx4 %[va], %[rw], s84 offen nt lds\nv_add_u32 v96, 32768, %[ra]\nds_read2st64_b32 v[64:65], v96 offset0:0 offset1:1\n"
    "ds_read2st64_b32 v[66:67], v96 offset0:2 offset1:3\nds_read2st64_b32 v[68:69], v96 offset0:4 offset1:5\n"
    "ds_read2st64_b32 v[70:71], v96 offset0:6 offset1:7\nv_add_u32 v97, 32768, %[ra]\nds_read2st64_b32 v[72:73], v97 offset0:32 offset1:33\n"
    "ds_read2st64_b32 v[74:75], v97 offset0:34 offset1:35\nds_read2st64_b32 v[76:77], v97 offset0:36 offset1:37\n"
    "ds_read2st64_b32 v[78:79], v97 offset0:38 offset1:39\nv_add_u32 v98, 32768, %[ra]\nds_read2st64_b32 v[80:81], v98 offset0:64 offset1:65\n"
    "ds_read2st64_b32 v[82:83], v98 offset0:66 offset1:67\nds_read2st64_b32 v[84:85], v98 offset0:68 offset1:69\n"
    "ds_read2st64_b32 v[86:87], v98 offset0:70 offset1:71\nv_add_u32 v99, 32768, %[ra]\nds_read2st64_b32 v[88:89], v99 offset0:96 offset1:97\n"
    "ds_read2st64_b32 v[90:91], v99 offset0:98 offset1:99\nds_read2st64_b32 v[92:93], v99 offset0:100 offset1:101\n"
    "ds_read2st64_b32 v[94:95], v99 offset0:102 offset1:103\ns_waitcnt lgkmcnt(12)\nv_perm_b32 v48, v65, v64, s85\nv_perm_b32 v49, v67, v66, s85\n"
    "v_perm_b32 v50, v69, v68, s85\nv_perm_b32 v51, v71, v70, s85\nv_perm_b32 v52, v65, v64, s86\nv_perm_b32 v53, v67, v66, s86\n"
    "v_perm_b32 v54, v69, v68, s86\nv_perm_b32 v55, v71, v70, s86\nv_accvgpr_write_b32 a128, v48\nv_accvgpr_write_b32 a129, v49\n"
    "v_accvgpr_write_b32 a130, v50\nv_accvgpr_write_b32 a131, v51\nv_accvgpr_write_b32 a132, v52\nv_accvgpr_write_b32 a133, v53\n"
    "v_accvgpr_write_b32 a134, v54\nv_accvgpr_write_b32 a135, v55\ns_waitcnt lgkmcnt(8)\nv_perm_b32 v48, v73, v72, s85\nv_perm_b32 v49, v75, v74, s85\n"
    "v_perm_b32 v50, v77, v76, s85\nv_perm_b32 v51, v79, v78, s85\nv_perm_b32 v52, v73, v72, s86\nv_perm_b32 v53, v75, v74, s86\n"
    "v_perm_b32 v54, v77, v76, s86\nv_perm_b32 v55, v79, v78, s86\nv_accvgpr_write_b32 a144, v48\nv_accvgpr_write_b32 a145, v49\n"
    "v_accvgpr_write_b32 a146, v50\nv_accvgpr_write_b32 a147, v51\nv_accvgpr_write_b32 a148, v52\nv_accvgpr_write_b32 a149, v53\n"
    "v_accvgpr_write_b32 a150, v54\nv_accvgpr_write_b32 a151, v55\ns_waitcnt lgkmcnt(4)\nv_perm_b32 v48, v81, v80, s85\nv_perm_b32 v49, v83, v82, s85\n"
    "v_perm_b32 v50, v85, v84, s85\nv_perm_b32 v51, v87, v86, s85\nv_perm_b32 v52, v81, v80, s86\nv_perm_b32 v53, v83, v82, s86\n"
    "v_perm_b32 v54, v85, v84, s86\nv_perm_b32 v55, v87, v86, s86\nv_accvgpr_write_b32 a160, v48\nv_accvgpr_write_b32 a161, v49\n"
    "v_accvgpr_write_b32 a162, v50\nv_accvgpr_write_b32 a163, v51\nv_accvgpr_write_b32 a164, v52\nv_accvgpr_write_b32 a165, v53\n"
    "v_accvgpr_write_b32 a166, v54\nv_accvgpr_write_b32 a167, v55\ns_waitcnt lgkmcnt(0)\nv_perm_b32 v48, v89, v88, s85\nv_perm_b32 v49, v91, v90, s85\n"
    "v_perm_b32 v50, v93, v92, s85\nv_perm_b32 v51, v95, v94, s85\nv_perm_b32 v52, v89, v88, s86\nv_perm_b32 v53, v91, v90, s86\n"
    "v_perm_b32 v54, v93, v92, s86\nv_perm_b32 v55, v95, v94, s86\nv_accvgpr_write_b32 a176, v48\nv_accvgpr_write_b32 a177, v49\n"
    "v_accvgpr_write_b32 a178, v50\nv_accvgpr_write_b32 a179, v51\nv_accvgpr_write_b32 a180, v52\nv_accvgpr_write_b32 a181, v53\n"
    "v_accvgpr_write_b32 a182, v54\nv_accvgpr_write_b32 a183, v55\ns_waitcnt vmcnt(8)\ns_barrier\ns_add_u32 m0, %[ld], 32768\ns_mov_b32 s84, 1376256\n"
    "buffer_load_dwordx4 %[va], %[ro], s84 offen nt lds\ns_add_u32 m0, m0, 1024\ns_add_u32 s84, s84, 14336\n"
    "buffer_load_dwordx4 %[va], %[ro], s84 offen nt lds\ns_add_u32 m0, m0, 1024\ns_add_u32 s84, s84, 14336\n"
    "buffer_load_dwordx4 %[va], %[ro], s84 offen nt lds\ns_add_u32 m0, m0, 1024\ns_add_u32 s84, s84, 14336\n"
    "buffer_load_dwordx4 %[va], %[ro], s84 offen nt lds\ns_add_u32 m0, m0, 1024\ns_add_u32 s84, s84, 14336\n"
    "buffer_load_dwordx4 %[va], %[ro], s84 offen nt lds\ns_add_u32 m0, m0, 1024\ns_add_u32 s84, s84, 14336\n"
    "buffer_load_dwordx4 %[va], %[ro], s84 offen nt lds\ns_add_u32 m0, m0, 1024\ns_add_u32 s84, s84, 14336\n"
    "buffer_load_dwordx4 %[va], %[ro], s84 offen nt lds\ns_add_u32 m0, m0, 1024\ns_add_u32 s84, s84, 14336\n"
    "buffer_load_dwordx4 %[va], %[ro], s84 offen nt lds\nv_add_u32 v96, 65536, %[ra]\nds_read2st64_b32 v[64:65], v96 offset0:0 offset1:1\n"
    "ds_read2st64_b32 v[66:67], v96 offset0:2 offset1:3\nds_read2st64_b32 v[68:69], v96 offset0:4 offset1:5\n"
    "ds_read2st64_b32 v[70:71], v96 offset0:6 offset1:7\nv_add_u32 v97, 65536, %[ra]\nds_read2st64_b32 v[72:73], v97 offset0:32 offset1:33\n"
    "ds_read2st64_b32 v[74:75], v97 offset0:34 offset1:35\nds_read2st64_b32 v[76:77], v97 offset0:36 offset1:37\n"
    "ds_read2st64_b32 v[78:79], v97 offset0:38 offset1:39\nv_add_u32 v98, 65536, %[ra]\nds_read2st64_b32 v[80:81], v98 offset0:64 offset1:65\n"
    "ds_read2st64_b32 v[82:83], v98 offset0:66 offset1:67\nds_read2st64_b32 v[84:85], v98 offset0:68 offset1:69\n"
    "ds_read2st64_b32 v[86:87], v98 offset0:70 offset1:71\nv_add_u32 v99, 65536, %[ra]\nds_read2st64_b32 v[88:89], v99 offset0:96 offset1:97\n"
    "ds_read2st64_b32 v[90:91], v99 offset0:98 offset1:99\nds_read2st64_b32 v[92:93], v99 offset0:100 offset1:101\n"
    "ds_read2st64_b32 v[94:95], v99 offset0:102 offset1:103\ns_waitcnt lgkmcnt(12)\nv_perm_b32 v48, v65, v64, s85\nv_perm_b32 v49, v67, v66, s85\n"
    "v_perm_b32 v50, v69, v68, s85\nv_perm_b32 v51, v71, v70, s85\nv_perm_b32 v52, v65, v64, s86\nv_perm_b32 v53, v67, v66, s86\n"
    "v_perm_b32 v54, v69, v68, s86\nv_perm_b32 v55, v71, v70, s86\nv_accvgpr_write_b32 a136, v48\nv_accvgpr_write_b32 a137, v49\n"
    "v_accvgpr_write_b32 a138, v50\nv_accvgpr_write_b32 a139, v51\nv_accvgpr_write_b32 a140, v52\nv_accvgpr_write_b32 a141, v53\n"
    "v_accvgpr_write_b32 a142, v54\nv_accvgpr_write_b32 a143, v55\ns_waitcnt lgkmcnt(8)\nv_perm_b32 v48, v73, v72, s85\nv_perm_b32 v49, v75, v74, s85\n"
    "v_perm_b32 v50, v77, v76, s85\nv_perm_b32 v51, v79, v78, s85\nv_perm_b32 v52, v73, v72, s86\nv_perm_b32 v53, v75, v74, s86\n"
    "v_perm_b32 v54, v77, v76, s86\nv_perm_b32 v55, v79, v78, s86\nv_accvgpr_write_b32 a152, v48\nv_accvgpr_write_b32 a153, v49\n"
    "v_accvgpr_write_b32 a154, v50\nv_accvgpr_write_b32 a155, v51\nv_accvgpr_write_b32 a156, v52\nv_accvgpr_write_b32 a157, v53\n"
    "v_accvgpr_write_b32 a158, v54\nv_accvgpr_write_b32 a159, v55\ns_waitcnt lgkmcnt(4)\nv_perm_b32 v48, v81, v80, s85\nv_perm_b32 v49, v83, v82, s85\n"
    "v_perm_b32 v50, v85, v84, s85\nv_perm_b32 v51, v87, v86, s85\nv_perm_b32 v52, v81, v80, s86\nv_perm_b32 v53, v83, v82, s86\n"
    "v_perm_b32 v54, v85, v84, s86\nv_perm_b32 v55, v87, v86, s86\nv_accvgpr_write_b32 a168, v48\nv_accvgpr_write_b32 a169, v49\n"
    "v_accvgpr_write_b32 a170, v50\nv_accvgpr_write_b32 a171, v51\nv_accvgpr_write_b32 a172, v52\nv_accvgpr_write_b32 a173, v53\n"
    "v_accvgpr_write_b32 a174, v54\nv_accvgpr_write_b32 a175, v55\ns_waitcnt lgkmcnt(0)\nv_perm_b32 v48, v89, v88, s85\nv_perm_b32 v49, v91, v90, s85\n"
    "v_perm_b32 v50, v93, v92, s85\nv_perm_b32 v51, v95, v94, s85\nv_perm_b32 v52, v89, v88, s86\nv_perm_b32 v53, v91, v90, s86\n"
    "v_perm_b32 v54, v93, v92, s86\nv_perm_b32 v55, v95, v94, s86\nv_accvgpr_write_b32 a184, v48\nv_accvgpr_write_b32 a185, v49\n"
    "v_accvgpr_write_b32 a186, v50\nv_accvgpr_write_b32 a187, v51\nv_accvgpr_write_b32 a188, v52\nv_accvgpr_write_b32 a189, v53\n"
    "v_accvgpr_write_b32 a190, v54\nv_accvgpr_write_b32 a191, v55\ns_waitcnt vmcnt(8)\ns_barrier\nv_add_u32 v96, 0, %[ra]\n"
    "ds_read2st64_b32 v[64:65], v96 offset0:0 offset1:1\nds_read2st64_b32 v[66:67], v96 offset0:2 offset1:3\n"
    "ds_read2st64_b32 v[68:69], v96 offset0:4 offset1:5\nds_read2st64_b32 v[70:71], v96 offset0:6 offset1:7\nv_add_u32 v97, 0, %[ra]\n"
    "ds_read2st64_b32 v[72:73], v97 offset0:32 offset1:33\nds_read2st64_b32 v[74:75], v97 offset0:34 offset1:35\n"
    "ds_read2st64_b32 v[76:77], v97 offset0:36 offset1:37\nds_read2st64_b32 v[78:79], v97 offset0:38 offset1:39\nv_add_u32 v98, 0, %[ra]\n"
    "ds_read2st64_b32 v[80:81], v98 offset0:64 offset1:65\nds_read2st64_b32 v[82:83], v98 offset0:66 offset1:67\n"
    "ds_read2st64_b32 v[84:85], v98 offset0:68 offset1:69\nds_read2st64_b32 v[86:87], v98 offset0:70 offset1:71\nv_add_u32 v99, 0, %[ra]\n"
    "ds_read2st64_b32 v[88:89], v99 offset0:96 offset1:97\nds_read2st64_b32 v[90:91], v99 offset0:98 offset1:99\n"
    "ds_read2st64_b32 v[92:93], v99 offset0:100 offset1:101\nds_read2st64_b32 v[94:95], v99 offset0:102 offset1:103\ns_waitcnt lgkmcnt(12)\n"
    "v_perm_b32 v48, v65, v64, s85\nv_perm_b32 v49, v67, v66, s85\nv_perm_b32 v50, v69, v68, s85\nv_perm_b32 v51, v71, v70, s85\n"
    "v_perm_b32 v52, v65, v64, s86\nv_perm_b32 v53, v67, v66, s86\nv_perm_b32 v54, v69, v68, s86\nv_perm_b32 v55, v71, v70, s86\n"
    "v_accvgpr_write_b32 a192, v48\nv_accvgpr_write_b32 a193, v49\nv_accvgpr_write_b32 a194, v50\nv_accvgpr_write_b32 a195, v51\n"
    "v_accvgpr_write_b32 a196, v52\nv_accvgpr_write_b32 a197, v53\nv_accvgpr_write_b32 a198, v54\nv_accvgpr_write_b32 a199, v55\ns_waitcnt lgkmcnt(8)\n"
    "v_perm_b32 v48, v73, v72, s85\nv_perm_b32 v49, v75, v74, s85\nv_perm_b32 v50, v77, v76, s85\nv_perm_b32 v51, v79, v78, s85\n"
    "v_perm_b32 v52, v73, v72, s86\nv_perm_b32 v53, v75, v74, s86\nv_perm_b32 v54, v77, v76, s86\nv_perm_b32 v55, v79, v78, s86\n"
    "v_accvgpr_write_b32 a208, v48\nv_accvgpr_write_b32 a209, v49\nv_accvgpr_write_b32 a210, v50\nv_accvgpr_write_b32 a211, v51\n"
    "v_accvgpr_write_b32 a212, v52\nv_accvgpr_write_b32 a213, v53\nv_accvgpr_write_b32 a214, v54\nv_accvgpr_write_b32 a215, v55\ns_waitcnt lgkmcnt(4)\n"
    "v_perm_b32 v48, v81, v80, s85\nv_perm_b32 v49, v83, v82, s85\nv_perm_b32 v50, v85, v84, s85\nv_perm_b32 v51, v87, v86, s85\n"
    "v_perm_b32 v52, v81, v80, s86\nv_perm_b32 v53, v83, v82, s86\nv_perm_b32 v54, v85, v84, s86\nv_perm_b32 v55, v87, v86, s86\n"
    "v_accvgpr_write_b32 a224, v48\nv_accvgpr_write_b32 a225, v49\nv_accvgpr_write_b32 a226, v50\nv_accvgpr_write_b32 a227, v51\n"
    "v_accvgpr_write_b32 a228, v52\nv_accvgpr_write_b32 a229, v53\nv_accvgpr_write_b32 a230, v54\nv_accvgpr_write_b32 a231, v55\ns_waitcnt lgkmcnt(0)\n"
    "v_perm_b32 v48, v89, v88, s85\nv_perm_b32 v49, v91, v90, s85\nv_perm_b32 v50, v93, v92, s85\nv_perm_b32 v51, v95, v94, s85\n"
    "v_perm_b32 v52, v89, v88, s86\nv_perm_b32 v53, v91, v90, s86\nv_perm_b32 v54, v93, v92, s86\nv_perm_b32 v55, v95, v94, s86\n"
    "v_accvgpr_write_b32 a240, v48\nv_accvgpr_write_b32 a241, v49\nv_accvgpr_write_b32 a242, v50\nv_accvgpr_write_b32 a243, v51\n"
    "v_accvgpr_write_b32 a244, v52\nv_accvgpr_write_b32 a245, v53\nv_accvgpr_write_b32 a246, v54\nv_accvgpr_write_b32 a247, v55\ns_waitcnt vmcnt(0)\n"
    "s_barrier\nv_add_u32 v96, 32768, %[ra]\nds_read2st64_b32 v[64:65], v96 offset0:0 offset1:1\nds_read2st64_b32 v[66:67], v96 offset0:2 offset1:3\n"
    "ds_read2st64_b32 v[68:69], v96 offset0:4 offset1:5\nds_read2st64_b32 v[70:71], v96 offset0:6 offset1:7\nv_add_u32 v97, 32768, %[ra]\n"
    "ds_read2st64_b32 v[72:73], v97 offset0:32 offset1:33\nds_read2st64_b32 v[74:75], v97 offset0:34 offset1:35\n"
    "ds_read2st64_b32 v[76:77], v97 offset0:36 offset1:37\nds_read2st64_b32 v[78:79], v97 offset0:38 offset1:39\nv_add_u32 v98, 32768, %[ra]\n"
    "ds_read2st64_b32 v[80:81], v98 offset0:64 offset1:65\nds_read2st64_b32 v[82:83], v98 offset0:66 offset1:67\n"
    "ds_read2st64_b32 v[84:85], v98 offset0:68 offset1:69\nds_read2st64_b32 v[86:87], v98 offset0:70 offset1:71\nv_add_u32 v99, 32768, %[ra]\n"
    "ds_read2st64_b32 v[88:89], v99 offset0:96 offset1:97\nds_read2st64_b32 v[90:91], v99 offset0:98 offset1:99\n"
    "ds_read2st64_b32 v[92:93], v99 offset0:100 offset1:101\nds_read2st64_b32 v[94:95], v99 offset0:102 offset1:103\ns_waitcnt lgkmcnt(12)\n"
    "v_perm_b32 v48, v65, v64, s85\nv_perm_b32 v49, v67, v66, s85\nv_perm_b32 v50, v69, v68, s85\nv_perm_b32 v51, v71, v70, s85\n"
    "v_perm_b32 v52, v65, v64, s86\nv_perm_b32 v53, v67, v66, s86\nv_perm_b32 v54, v69, v68, s86\nv_perm_b32 v55, v71, v70, s86\n"
    "v_accvgpr_write_b32 a200, v48\nv_accvgpr_write_b32 a201, v49\nv_accvgpr_write_b32 a202, v50\nv_accvgpr_write_b32 a203, v51\n"
    "v_accvgpr_write_b32 a204, v52\nv_accvgpr_write_b32 a205, v53\nv_accvgpr_write_b32 a206, v54\nv_accvgpr_write_b32 a207, v55\ns_waitcnt lgkmcnt(8)\n"
    "v_perm_b32 v48, v73, v72, s85\nv_perm_b32 v49, v75, v74, s85\nv_perm_b32 v50, v77, v76, s85\nv_perm_b32 v51, v79, v78, s85\n"
    "v_perm_b32 v52, v73, v72, s86\nv_perm_b32 v53, v75, v74, s86\nv_perm_b32 v54, v77, v76, s86\nv_perm_b32 v55, v79, v78, s86\n"
    "v_accvgpr_write_b32 a216, v48\nv_accvgpr_write_b32 a217, v49\nv_accvgpr_write_b32 a218, v50\nv_accvgpr_write_b32 a219, v51\n"
    "v_accvgpr_write_b32 a220, v52\nv_accvgpr_write_b32 a221, v53\nv_accvgpr_write_b32 a222, v54\nv_accvgpr_write_b32 a223, v55\ns_waitcnt lgkmcnt(4)\n"
    "v_perm_b32 v48, v81, v80, s85\nv_perm_b32 v49, v83, v82, s85\nv_perm_b32 v50, v85, v84, s85\nv_perm_b32 v51, v87, v86, s85\n"
    "v_perm_b32 v52, v81, v80, s86\nv_perm_b32 v53, v83, v82, s86\nv_perm_b32 v54, v85, v84, s86\nv_perm_b32 v55, v87, v86, s86\n"
    "v_accvgpr_write_b32 a232, v48\nv_accvgpr_write_b32 a233, v49\nv_accvgpr_write_b32 a234, v50\nv_accvgpr_write_b32 a235, v51\n"
    "v_accvgpr_write_b32 a236, v52\nv_accvgpr_write_b32 a237, v53\nv_accvgpr_write_b32 a238, v54\nv_accvgpr_write_b32 a239, v55\ns_waitcnt lgkmcnt(0)\n"
    "v_perm_b32 v48, v89, v88, s85\nv_perm_b32 v49, v91, v90, s85\nv_perm_b32 v50, v93, v92, s85\nv_perm_b32 v51, v95, v94, s85\n"
    "v_perm_b32 v52, v89, v88, s86\nv_perm_b32 v53, v91, v90, s86\nv_perm_b32 v54, v93, v92, s86\nv_perm_b32 v55, v95, v94, s86\n"
    "v_accvgpr_write_b32 a248, v48\nv_accvgpr_write_b32 a249, v49\nv_accvgpr_write_b32 a250, v50\nv_accvgpr_write_b32 a251, v51\n"
    "v_accvgpr_write_b32 a252, v52\nv_accvgpr_write_b32 a253, v53\nv_accvgpr_write_b32 a254, v54\nv_accvgpr_write_b32 a255, v55\ns_barrier\n"
    :: [rw] "s"(rw), [ro] "s"(ro), [ld] "s"(ld), [va] "v"(va), [ra] "v"(ra)
    : "memory", "scc", CLS, CLV_48_99, CLA_0_255);
}
DI void load_d(rsrc_t rw, unsigned vo) {
  asm volatile(
    "s_nop 4\ns_mov_b32 s85, 0x05040100\ns_mov_b32 s86, 0x07060302\ns_mov_b32 s84, 0\nbuffer_load_dwordx2 v[64:65], %[vo], %[rw], s84 offen nt\n"
    "s_mov_b32 s84, 1024\nbuffer_load_dwordx2 v[66:67], %[vo], %[rw], s84 offen nt\ns_mov_b32 s84, 2048\n"
    "buffer_load_dwordx2 v[68:69], %[vo], %[rw], s84 offen nt\ns_mov_b32 s84, 3072\nbuffer_load_dwordx2 v[70:71], %[vo], %[rw], s84 offen nt\n"
    "s_mov_b32 s84, 4096\nbuffer_load_dwordx2 v[72:73], %[vo], %[rw], s84 offen nt\ns_mov_b32 s84, 5120\n"
    "buffer_load_dwordx2 v[74:75], %[vo], %[rw], s84 offen nt\ns_mov_b32 s84, 6144\nbuffer_load_dwordx2 v[76:77], %[vo], %[rw], s84 offen nt\n"
    "s_mov_b32 s84, 7168\nbuffer_load_dwordx2 v[78:79], %[vo], %[rw], s84 offen nt\ns_mov_b32 s84, 32768\n"
    "buffer_load_dwordx2 v[80:81], %[vo], %[rw], s84 offen nt\ns_mov_b32 s84, 33792\nbuffer_load_dwordx2 v[82:83], %[vo], %[rw], s84 offen nt\n"
    "s_mov_b32 s84, 34816\nbuffer_load_dwordx2 v[84:85], %[vo], %[rw], s84 offen nt\ns_mov_b32 s84, 35840\n"
    "buffer_load_dwordx2 v[86:87], %[vo], %[rw], s84 offen nt\ns_mov_b32 s84, 36864\nbuffer_load_dwordx2 v[88:89], %[vo], %[rw], s84 offen nt\n"
    "s_mov_b32 s84, 37888\nbuffer_load_dwordx2 v[90:91], %[vo], %[rw], s84 offen nt\ns_mov_b32 s84, 38912\n"
    "buffer_load_dwordx2 v[92:93], %[vo], %[rw], s84 offen nt\ns_mov_b32 s84, 39936\nbuffer_load_dwordx2 v[94:95], %[vo], %[rw], s84 offen nt\n"
    "s_mov_b32 s84, 65536\nbuffer_load_dwordx2 v[96:97], %[vo], %[rw], s84 offen nt\ns_mov_b32 s84, 66560\n"
    "buffer_load_dwordx2 v[98:99], %[vo], %[rw], s84 offen nt\ns_mov_b32 s84, 67584\nbuffer_load_dwordx2 v[100:101], %[vo], %[rw], s84 offen nt\n"
    "s_mov_b32 s84, 68608\nbuffer_load_dwordx2 v[102:103], %[vo], %[rw], s84 offen nt\ns_mov_b32 s84, 69632\n"
    "buffer_load_dwordx2 v[104:105], %[vo], %[rw], s84 offen nt\ns_mov_b32 s84, 70656\nbuffer_load_dwordx2 v[106:107], %[vo], %[rw], s84 offen nt\n"
    "s_mov_b32 s84, 71680\nbuffer_load_dwordx2 v[108:109], %[vo], %[rw], s84 offen nt\ns_mov_b32 s84, 72704\n"
    "buffer_load_dwordx2 v[110:111], %[vo], %[rw], s84 offen nt\ns_mov_b32 s84, 98304\nbuffer_load_dwordx2 v[112:113], %[vo], %[rw], s84 offen nt\n"
    "s_mov_b32 s84, 99328\nbuffer_load_dwordx2 v[114:115], %[vo], %[rw], s84 offen nt\ns_mov_b32 s84, 100352\n"
    "buffer_load_dwordx2 v[116:117], %[vo], %[rw], s84 offen nt\ns_mov_b32 s84, 101376\nbuffer_load_dwordx2 v[118:119], %[vo], %[rw], s84 offen nt\n"
    "s_mov_b32 s84, 102400\nbuffer_load_dwordx2 v[120:121], %[vo], %[rw], s84 offen nt\ns_mov_b32 s84, 103424\n"
    "buffer_load_dwordx2 v[122:123], %[vo], %[rw], s84 offen nt\ns_mov_b32 s84, 104448\nbuffer_load_dwordx2 v[124:125], %[vo], %[rw], s84 offen nt\n"
    "s_mov_b32 s84, 105472\nbuffer_load_dwordx2 v[126:127], %[vo], %[rw], s84 offen nt\ns_mov_b32 s84, 131072\n"
    "buffer_load_dwordx2 v[128:129], %[vo], %[rw], s84 offen nt\ns_mov_b32 s84, 132096\nbuffer_load_dwordx2 v[130:131], %[vo], %[rw], s84 offen nt\n"
    "s_mov_b32 s84, 133120\nbuffer_load_dwordx2 v[132:133], %[vo], %[rw], s84 offen nt\ns_mov_b32 s84, 134144\n"
    "buffer_load_dwordx2 v[134:135], %[vo], %[rw], s84 offen nt\ns_mov_b32 s84, 135168\nbuffer_load_dwordx2 v[136:137], %[vo], %[rw], s84 offen nt\n"
    "s_mov_b32 s84, 136192\nbuffer_load_dwordx2 v[138:139], %[vo], %[rw], s84 offen nt\ns_mov_b32 s84, 137216\n"
    "buffer_load_dwordx2 v[140:141], %[vo], %[rw], s84 offen nt\ns_mov_b32 s84, 138240\nbuffer_load_dwordx2 v[142:143], %[vo], %[rw], s84 offen nt\n"
    "s_mov_b32 s84, 163840\nbuffer_load_dwordx2 v[144:145], %[vo], %[rw], s84 offen nt\ns_mov_b32 s84, 164864\n"
    "buffer_load_dwordx2 v[146:147], %[vo], %[rw], s84 offen nt\ns_mov_b32 s84, 165888\nbuffer_load_dwordx2 v[148:149], %[vo], %[rw], s84 offen nt\n"
    "s_mov_b32 s84, 166912\nbuffer_load_dwordx2 v[150:151], %[vo], %[rw], s84 offen nt\ns_mov_b32 s84, 167936\n"
    "buffer_load_dwordx2 v[152:153], %[vo], %[rw], s84 offen nt\ns_mov_b32 s84, 168960\nbuffer_load_dwordx2 v[154:155], %[vo], %[rw], s84 offen nt\n"
    "s_mov_b32 s84, 169984\nbuffer_load_dwordx2 v[156:157], %[vo], %[rw], s84 offen nt\ns_mov_b32 s84, 171008\n"
    "buffer_load_dwordx2 v[158:159], %[vo], %[rw], s84 offen nt\ns_mov_b32 s84, 196608\nbuffer_load_dwordx2 v[160:161], %[vo], %[rw], s84 offen nt\n"
    "s_mov_b32 s84, 197632\nbuffer_load_dwordx2 v[162:163], %[vo], %[rw], s84 offen nt\ns_mov_b32 s84, 198656\n"
    "buffer_load_dwordx2 v[164:165], %[vo], %[rw], s84 offen nt\ns_mov_b32 s84, 199680\nbuffer_load_dwordx2 v[166:167], %[vo], %[rw], s84 offen nt\n"
    "s_mov_b32 s84, 200704\nbuffer_load_dwordx2 v[168:169], %[vo], %[rw], s84 offen nt\ns_mov_b32 s84, 201728\n"
    "buffer_load_dwordx2 v[170:171], %[vo], %[rw], s84 offen nt\ns_mov_b32 s84, 202752\nbuffer_load_dwordx2 v[172:173], %[vo], %[rw], s84 offen nt\n"
    "s_mov_b32 s84, 203776\nbuffer_load_dwordx2 v[174:175], %[vo], %[rw], s84 offen nt\ns_waitcnt vmcnt(48)\nv_perm_b32 v48, v66, v64, s85\n"
    "v_perm_b32 v49, v70, v68, s85\nv_perm_b32 v50, v74, v72, s85\nv_perm_b32 v51, v78, v76, s85\nv_perm_b32 v52, v66, v64, s86\n"
    "v_perm_b32 v53, v70, v68, s86\nv_perm_b32 v54, v74, v72, s86\nv_perm_b32 v55, v78, v76, s86\nv_perm_b32 v56, v67, v65, s85\n"
    "v_perm_b32 v57, v71, v69, s85\nv_perm_b32 v58, v75, v73, s85\nv_perm_b32 v59, v79, v77, s85\nv_perm_b32 v60, v67, v65, s86\n"
    "v_perm_b32 v61, v71, v69, s86\nv_perm_b32 v62, v75, v73, s86\nv_perm_b32 v63, v79, v77, s86\nv_accvgpr_write_b32 a0, v48\nv_accvgpr_write_b32 a1, v49\n"
    "v_accvgpr_write_b32 a2, v50\nv_accvgpr_write_b32 a3, v51\nv_accvgpr_write_b32 a4, v52\nv_accvgpr_write_b32 a5, v53\nv_accvgpr_write_b32 a6, v54\n"
    "v_accvgpr_write_b32 a7, v55\nv_accvgpr_write_b32 a8, v56\nv_accvgpr_write_b32 a9, v57\nv_accvgpr_write_b32 a10, v58\nv_accvgpr_write_b32 a11, v59\n"
    "v_accvgpr_write_b32 a12, v60\nv_accvgpr_write_b32 a13, v61\nv_accvgpr_write_b32 a14, v62\nv_accvgpr_write_b32 a15, v63\ns_mov_b32 s84, 229376\n"
    "buffer_load_dwordx2 v[64:65], %[vo], %[rw], s84 offen nt\ns_mov_b32 s84, 230400\nbuffer_load_dwordx2 v[66:67], %[vo], %[rw], s84 offen nt\n"
    "s_mov_b32 s84, 231424\nbuffer_load_dwordx2 v[68:69], %[vo], %[rw], s84 offen nt\ns_mov_b32 s84, 232448\n"
    "buffer_load_dwordx2 v[70:71], %[vo], %[rw], s84 offen nt\ns_mov_b32 s84, 233472\nbuffer_load_dwordx2 v[72:73], %[vo], %[rw], s84 offen nt\n"
    "s_mov_b32 s84, 234496\nbuffer_load_dwordx2 v[74:75], %[vo], %[rw], s84 offen nt\ns_mov_b32 s84, 235520\n"
    "buffer_load_dwordx2 v[76:77], %[vo], %[rw], s84 offen nt\ns_mov_b32 s84, 236544\nbuffer_load_dwordx2 v[78:79], %[vo], %[rw], s84 offen nt\n"
    "s_waitcnt vmcnt(48)\nv_perm_b32 v48, v82, v80, s85\nv_perm_b32 v49, v86, v84, s85\nv_perm_b32 v50, v90, v88, s85\nv_perm_b32 v51, v94, v92, s85\n"
    "v_perm_b32 v52, v82, v80, s86\nv_perm_b32 v53, v86, v84, s86\nv_perm_b32 v54, v90, v88, s86\nv_perm_b32 v55, v94, v92, s86\n"
    "v_perm_b32 v56, v83, v81, s85\nv_perm_b32 v57, v87, v85, s85\nv_perm_b32 v58, v91, v89, s85\nv_perm_b32 v59, v95, v93, s85\n"
    "v_perm_b32 v60, v83, v81, s86\nv_perm_b32 v61, v87, v85, s86\nv_perm_b32 v62, v91, v89, s86\nv_perm_b32 v63, v95, v93, s86\n"
    "v_accvgpr_write_b32 a16, v48\nv_accvgpr_write_b32 a17, v49\nv_accvgpr_write_b32 a18, v50\nv_accvgpr_write_b32 a19, v51\nv_accvgpr_write_b32 a20, v52\n"
    "v_accvgpr_write_b32 a21, v53\nv_accvgpr_write_b32 a22, v54\nv_accvgpr_write_b32 a23, v55\nv_accvgpr_write_b32 a24, v56\nv_accvgpr_write_b32 a25, v57\n"
    "v_accvgpr_write_b32 a26, v58\nv_accvgpr_write_b32 a27, v59\nv_accvgpr_write_b32 a28, v60\nv_accvgpr_write_b32 a29, v61\nv_accvgpr_write_b32 a30, v62\n"
    "v_accvgpr_write_b32 a31, v63\ns_mov_b32 s84, 262144\nbuffer_load_dwordx2 v[80:81], %[vo], %[rw], s84 offen nt\ns_mov_b32 s84, 263168\n"
    "buffer_load_dwordx2 v[82:83], %[vo], %[rw], s84 offen nt\ns_mov_b32 s84, 264192\nbuffer_load_dwordx2 v[84:85], %[vo], %[rw], s84 offen nt\n"
    "s_mov_b32 s84, 265216\nbuffer_load_dwordx2 v[86:87], %[vo], %[rw], s84 offen nt\ns_mov_b32 s84, 266240\n"
    "buffer_load_dwordx2 v[88:89], %[vo], %[rw], s84 offen nt\ns_mov_b32 s84, 267264\nbuffer_load_dwordx2 v[90:91], %[vo], %[rw], s84 offen nt\n"
    "s_mov_b32 s84, 268288\nbuffer_load_dwordx2 v[92:93], %[vo], %[rw], s84 offen nt\ns_mov_b32 s84, 269312\n"
    "buffer_load_dwordx2 v[94:95], %[vo], %[rw], s84 offen nt\ns_waitcnt vmcnt(48)\nv_perm_b32 v48, v98, v96, s85\nv_perm_b32 v49, v102, v100, s85\n"
    "v_perm_b32 v50, v106, v104, s85\nv_perm_b32 v51, v110, v108, s85\nv_perm_b32 v52, v98, v96, s86\nv_perm_b32 v53, v102, v100, s86\n"
    "v_perm_b32 v54, v106, v104, s86\nv_perm_b32 v55, v110, v108, s86\nv_perm_b32 v56, v99, v97, s85\nv_perm_b32 v57, v103, v101, s85\n"
    "v_perm_b32 v58, v107, v105, s85\nv_perm_b32 v59, v111, v109, s85\nv_perm_b32 v60, v99, v97, s86\nv_perm_b32 v61, v103, v101, s86\n"
    "v_perm_b32 v62, v107, v105, s86\nv_perm_b32 v63, v111, v109, s86\nv_accvgpr_write_b32 a32, v48\nv_accvgpr_write_b32 a33, v49\n"
    "v_accvgpr_write_b32 a34, v50\nv_accvgpr_write_b32 a35, v51\nv_accvgpr_write_b32 a36, v52\nv_accvgpr_write_b32 a37, v53\nv_accvgpr_write_b32 a38, v54\n"
    "v_accvgpr_write_b32 a39, v55\nv_accvgpr_write_b32 a40, v56\nv_accvgpr_write_b32 a41, v57\nv_accvgpr_write_b32 a42, v58\nv_accvgpr_write_b32 a43, v59\n"
    "v_accvgpr_write_b32 a44, v60\nv_accvgpr_write_b32 a45, v61\nv_accvgpr_write_b32 a46, v62\nv_accvgpr_write_b32 a47, v63\ns_mov_b32 s84, 294912\n"
    "buffer_load_dwordx2 v[96:97], %[vo], %[rw], s84 offen nt\ns_mov_b32 s84, 295936\nbuffer_load_dwordx2 v[98:99], %[vo], %[rw], s84 offen nt\n"
    "s_mov_b32 s84, 296960\nbuffer_load_dwordx2 v[100:101], %[vo], %[rw], s84 offen nt\ns_mov_b32 s84, 297984\n"
    "buffer_load_dwordx2 v[102:103], %[vo], %[rw], s84 offen nt\ns_mov_b32 s84, 299008\nbuffer_load_dwordx2 v[104:105], %[vo], %[rw], s84 offen nt\n"
    "s_mov_b32 s84, 300032\nbuffer_load_dwordx2 v[106:107], %[vo], %[rw], s84 offen nt\ns_mov_b32 s84, 301056\n"
    "buffer_load_dwordx2 v[108:109], %[vo], %[rw], s84 offen nt\ns_mov_b32 s84, 302080\nbuffer_load_dwordx2 v[110:111], %[vo], %[rw], s84 offen nt\n"
    "s_waitcnt vmcnt(48)\nv_perm_b32 v48, v114, v112, s85\nv_perm_b32 v49, v118, v116, s85\nv_perm_b32 v50, v122, v120, s85\n"
    "v_perm_b32 v51, v126, v124, s85\nv_perm_b32 v52, v114, v112, s86\nv_perm_b32 v53, v118, v116, s86\nv_perm_b32 v54, v122, v120, s86\n"
    "v_perm_b32 v55, v126, v124, s86\nv_perm_b32 v56, v115, v113, s85\nv_perm_b32 v57, v119, v117, s85\nv_perm_b32 v58, v123, v121, s85\n"
    "v_perm_b32 v59, v127, v125, s85\nv_perm_b32 v60, v115, v113, s86\nv_perm_b32 v61, v119, v117, s86\nv_perm_b32 v62, v123, v121, s86\n"
    "v_perm_b32 v63, v127, v125, s86\nv_accvgpr_write_b32 a48, v48\nv_accvgpr_write_b32 a49, v49\nv_accvgpr_write_b32 a50, v50\n"
    "v_accvgpr_write_b32 a51, v51\nv_accvgpr_write_b32 a52, v52\nv_accvgpr_write_b32 a53, v53\nv_accvgpr_write_b32 a54, v54\nv_accvgpr_write_b32 a55, v55\n"
    "v_accvgpr_write_b32 a56, v56\nv_accvgpr_write_b32 a57, v57\nv_accvgpr_write_b32 a58, v58\nv_accvgpr_write_b32 a59, v59\nv_accvgpr_write_b32 a60, v60\n"
    "v_accvgpr_write_b32 a61, v61\nv_accvgpr_write_b32 a62, v62\nv_accvgpr_write_b32 a63, v63\ns_mov_b32 s84, 327680\n"
    "buffer_load_dwordx2 v[112:113], %[vo], %[rw], s84 offen nt\ns_mov_b32 s84, 328704\nbuffer_load_dwordx2 v[114:115], %[vo], %[rw], s84 offen nt\n"
    "s_mov_b32 s84, 329728\nbuffer_load_dwordx2 v[116:117], %[vo], %[rw], s84 offen nt\ns_mov_b32 s84, 330752\n"
    "buffer_load_dwordx2 v[118:119], %[vo], %[rw], s84 offen nt\ns_mov_b32 s84, 331776\nbuffer_load_dwordx2 v[120:121], %[vo], %[rw], s84 offen nt\n"
    "s_mov_b32 s84, 332800\nbuffer_load_dwordx2 v[122:123], %[vo], %[rw], s84 offen nt\ns_mov_b32 s84, 333824\n"
    "buffer_load_dwordx2 v[124:125], %[vo], %[rw], s84 offen nt\ns_mov_b32 s84, 334848\nbuffer_load_dwordx2 v[126:127], %[vo], %[rw], s84 offen nt\n"
    "s_waitcnt vmcnt(48)\nv_perm_b32 v48, v130, v128, s85\nv_perm_b32 v49, v134, v132, s85\nv_perm_b32 v50, v138, v136, s85\n"
    "v_perm_b32 v51, v142, v140, s85\nv_perm_b32 v52, v130, v128, s86\nv_perm_b32 v53, v134, v132, s86\nv_perm_b32 v54, v138, v136, s86\n"
    "v_perm_b32 v55, v142, v140, s86\nv_perm_b32 v56, v131, v129, s85\nv_perm_b32 v57, v135, v133, s85\nv_perm_b32 v58, v139, v137, s85\n"
    "v_perm_b32 v59, v143, v141, s85\nv_perm_b32 v60, v131, v129, s86\nv_perm_b32 v61, v135, v133, s86\nv_perm_b32 v62, v139, v137, s86\n"
    "v_perm_b32 v63, v143, v141, s86\nv_accvgpr_write_b32 a64, v48\nv_accvgpr_write_b32 a65, v49\nv_accvgpr_write_b32 a66, v50\n"
    "v_accvgpr_write_b32 a67, v51\nv_accvgpr_write_b32 a68, v52\nv_accvgpr_write_b32 a69, v53\nv_accvgpr_write_b32 a70, v54\nv_accvgpr_write_b32 a71, v55\n"
    "v_accvgpr_write_b32 a72, v56\nv_accvgpr_write_b32 a73, v57\nv_accvgpr_write_b32 a74, v58\nv_accvgpr_write_b32 a75, v59\nv_accvgpr_write_b32 a76, v60\n"
    "v_accvgpr_write_b32 a77, v61\nv_accvgpr_write_b32 a78, v62\nv_accvgpr_write_b32 a79, v63\ns_mov_b32 s84, 360448\n"
    "buffer_load_dwordx2 v[128:129], %[vo], %[rw], s84 offen nt\ns_mov_b32 s84, 361472\nbuffer_load_dwordx2 v[130:131], %[vo], %[rw], s84 offen nt\n"
    "s_mov_b32 s84, 362496\nbuffer_load_dwordx2 v[132:133], %[vo], %[rw], s84 offen nt\ns_mov_b32 s84, 363520\n"
    "buffer_load_dwordx2 v[134:135], %[vo], %[rw], s84 offen nt\ns_mov_b32 s84, 364544\nbuffer_load_dwordx2 v[136:137], %[vo], %[rw], s84 offen nt\n"
    "s_mov_b32 s84, 365568\nbuffer_load_dwordx2 v[138:139], %[vo], %[rw], s84 offen nt\ns_mov_b32 s84, 366592\n"
    "buffer_load_dwordx2 v[140:141], %[vo], %[rw], s84 offen nt\ns_mov_b32 s84, 367616\nbuffer_load_dwordx2 v[142:143], %[vo], %[rw], s84 offen nt\n"
    "s_waitcnt vmcnt(48)\nv_perm_b32 v48, v146, v144, s85\nv_perm_b32 v49, v150, v148, s85\nv_perm_b32 v50, v154, v152, s85\n"
    "v_perm_b32 v51, v158, v156, s85\nv_perm_b32 v52, v146, v144, s86\nv_perm_b32 v53, v150, v148, s86\nv_perm_b32 v54, v154, v152, s86\n"
    "v_perm_b32 v55, v158, v156, s86\nv_perm_b32 v56, v147, v145, s85\nv_perm_b32 v57, v151, v149, s85\nv_perm_b32 v58, v155, v153, s85\n"
    "v_perm_b32 v59, v159, v157, s85\nv_perm_b32 v60, v147, v145, s86\nv_perm_b32 v61, v151, v149, s86\nv_perm_b32 v62, v155, v153, s86\n"
    "v_perm_b32 v63, v159, v157, s86\nv_accvgpr_write_b32 a80, v48\nv_accvgpr_write_b32 a81, v49\nv_accvgpr_write_b32 a82, v50\n"
    "v_accvgpr_write_b32 a83, v51\nv_accvgpr_write_b32 a84, v52\nv_accvgpr_write_b32 a85, v53\nv_accvgpr_write_b32 a86, v54\nv_accvgpr_write_b32 a87, v55\n"
    "v_accvgpr_write_b32 a88, v56\nv_accvgpr_write_b32 a89, v57\nv_accvgpr_write_b32 a90, v58\nv_accvgpr_write_b32 a91, v59\nv_accvgpr_write_b32 a92, v60\n"
    "v_accvgpr_write_b32 a93, v61\nv_accvgpr_write_b32 a94, v62\nv_accvgpr_write_b32 a95, v63\ns_mov_b32 s84, 393216\n"
    "buffer_load_dwordx2 v[144:145], %[vo], %[rw], s84 offen nt\ns_mov_b32 s84, 394240\nbuffer_load_dwordx2 v[146:147], %[vo], %[rw], s84 offen nt\n"
    "s_mov_b32 s84, 395264\nbuffer_load_dwordx2 v[148:149], %[vo], %[rw], s84 offen nt\ns_mov_b32 s84, 396288\n"
    "buffer_load_dwordx2 v[150:151], %[vo], %[rw], s84 offen nt\ns_mov_b32 s84, 397312\nbuffer_load_dwordx2 v[152:153], %[vo], %[rw], s84 offen nt\n"
    "s_mov_b32 s84, 398336\nbuffer_load_dwordx2 v[154:155], %[vo], %[rw], s84 offen nt\ns_mov_b32 s84, 399360\n"
    "buffer_load_dwordx2 v[156:157], %[vo], %[rw], s84 offen nt\ns_mov_b32 s84, 400384\nbuffer_load_dwordx2 v[158:159], %[vo], %[rw], s84 offen nt\n"
    "s_waitcnt vmcnt(48)\nv_perm_b32 v48, v162, v160, s85\nv_perm_b32 v49, v166, v164, s85\nv_perm_b32 v50, v170, v168, s85\n"
    "v_perm_b32 v51, v174, v172, s85\nv_perm_b32 v52, v162, v160, s86\nv_perm_b32 v53, v166, v164, s86\nv_perm_b32 v54, v170, v168, s86\n"
    "v_perm_b32 v55, v174, v172, s86\nv_perm_b32 v56, v163, v161, s85\nv_perm_b32 v57, v167, v165, s85\nv_perm_b32 v58, v171, v169, s85\n"
    "v_perm_b32 v59, v175, v173, s85\nv_perm_b32 v60, v163, v161, s86\nv_perm_b32 v61, v167, v165, s86\nv_perm_b32 v62, v171, v169, s86\n"
    "v_perm_b32 v63, v175, v173, s86\nv_accvgpr_write_b32 a96, v48\nv_accvgpr_write_b32 a97, v49\nv_accvgpr_write_b32 a98, v50\n"
    "v_accvgpr_write_b32 a99, v51\nv_accvgpr_write_b32 a100, v52\nv_accvgpr_write_b32 a101, v53\nv_accvgpr_write_b32 a102, v54\n"
    "v_accvgpr_write_b32 a103, v55\nv_accvgpr_write_b32 a104, v56\nv_accvgpr_write_b32 a105, v57\nv_accvgpr_write_b32 a106, v58\n"
    "v_accvgpr_write_b32 a107, v59\nv_accvgpr_write_b32 a108, v60\nv_accvgpr_write_b32 a109, v61\nv_accvgpr_write_b32 a110, v62\n"
    "v_accvgpr_write_b32 a111, v63\ns_mov_b32 s84, 425984\nbuffer_load_dwordx2 v[160:161], %[vo], %[rw], s84 offen nt\ns_mov_b32 s84, 427008\n"
    "buffer_load_dwordx2 v[162:163], %[vo], %[rw], s84 offen nt\ns_mov_b32 s84, 428032\nbuffer_load_dwordx2 v[164:165], %[vo], %[rw], s84 offen nt\n"
    "s_mov_b32 s84, 429056\nbuffer_load_dwordx2 v[166:167], %[vo], %[rw], s84 offen nt\ns_mov_b32 s84, 430080\n"
    "buffer_load_dwordx2 v[168:169], %[vo], %[rw], s84 offen nt\ns_mov_b32 s84, 431104\nbuffer_load_dwordx2 v[170:171], %[vo], %[rw], s84 offen nt\n"
    "s_mov_b32 s84, 432128\nbuffer_load_dwordx2 v[172:173], %[vo], %[rw], s84 offen nt\ns_mov_b32 s84, 433152\n"
    "buffer_load_dwordx2 v[174:175], %[vo], %[rw], s84 offen nt\ns_waitcnt vmcnt(48)\nv_perm_b32 v48, v66, v64, s85\nv_perm_b32 v49, v70, v68, s85\n"
    "v_perm_b32 v50, v74, v72, s85\nv_perm_b32 v51, v78, v76, s85\nv_perm_b32 v52, v66, v64, s86\nv_perm_b32 v53, v70, v68, s86\n"
    "v_perm_b32 v54, v74, v72, s86\nv_perm_b32 v55, v78, v76, s86\nv_perm_b32 v56, v67, v65, s85\nv_perm_b32 v57, v71, v69, s85\n"
    "v_perm_b32 v58, v75, v73, s85\nv_perm_b32 v59, v79, v77, s85\nv_perm_b32 v60, v67, v65, s86\nv_perm_b32 v61, v71, v69, s86\n"
    "v_perm_b32 v62, v75, v73, s86\nv_perm_b32 v63, v79, v77, s86\nv_accvgpr_write_b32 a112, v48\nv_accvgpr_write_b32 a113, v49\n"
    "v_accvgpr_write_b32 a114, v50\nv_accvgpr_write_b32 a115, v51\nv_accvgpr_write_b32 a116, v52\nv_accvgpr_write_b32 a117, v53\n"
    "v_accvgpr_write_b32 a118, v54\nv_accvgpr_write_b32 a119, v55\nv_accvgpr_write_b32 a120, v56\nv_accvgpr_write_b32 a121, v57\n"
    "v_accvgpr_write_b32 a122, v58\nv_accvgpr_write_b32 a123, v59\nv_accvgpr_write_b32 a124, v60\nv_accvgpr_write_b32 a125, v61\n"
    "v_accvgpr_write_b32 a126, v62\nv_accvgpr_write_b32 a127, v63\ns_waitcnt vmcnt(40)\nv_perm_b32 v48, v82, v80, s85\nv_perm_b32 v49, v86, v84, s85\n"
    "v_perm_b32 v50, v90, v88, s85\nv_perm_b32 v51, v94, v92, s85\nv_perm_b32 v52, v82, v80, s86\nv_perm_b32 v53, v86, v84, s86\n"
    "v_perm_b32 v54, v90, v88, s86\nv_perm_b32 v55, v94, v92, s86\nv_perm_b32 v56, v83, v81, s85\nv_perm_b32 v57, v87, v85, s85\n"
    "v_perm_b32 v58, v91, v89, s85\nv_perm_b32 v59, v95, v93, s85\nv_perm_b32 v60, v83, v81, s86\nv_perm_b32 v61, v87, v85, s86\n"
    "v_perm_b32 v62, v91, v89, s86\nv_perm_b32 v63, v95, v93, s86\nv_accvgpr_write_b32 a128, v48\nv_accvgpr_write_b32 a129, v49\n"
    "v_accvgpr_write_b32 a130, v50\nv_accvgpr_write_b32 a131, v51\nv_accvgpr_write_b32 a132, v52\nv_accvgpr_write_b32 a133, v53\n"
    "v_accvgpr_write_b32 a134, v54\nv_accvgpr_write_b32 a135, v55\nv_accvgpr_write_b32 a136, v56\nv_accvgpr_write_b32 a137, v57\n"
    "v_accvgpr_write_b32 a138, v58\nv_accvgpr_write_b32 a139, v59\nv_accvgpr_write_b32 a140, v60\nv_accvgpr_write_b32 a141, v61\n"
    "v_accvgpr_write_b32 a142, v62\nv_accvgpr_write_b32 a143, v63\ns_waitcnt vmcnt(32)\nv_perm_b32 v48, v98, v96, s85\nv_perm_b32 v49, v102, v100, s85\n"
    "v_perm_b32 v50, v106, v104, s85\nv_perm_b32 v51, v110, v108, s85\nv_perm_b32 v52, v98, v96, s86\nv_perm_b32 v53, v102, v100, s86\n"
    "v_perm_b32 v54, v106, v104, s86\nv_perm_b32 v55, v110, v108, s86\nv_perm_b32 v56, v99, v97, s85\nv_perm_b32 v57, v103, v101, s85\n"
    "v_perm_b32 v58, v107, v105, s85\nv_perm_b32 v59, v111, v109, s85\nv_perm_b32 v60, v99, v97, s86\nv_perm_b32 v61, v103, v101, s86\n"
    "v_perm_b32 v62, v107, v105, s86\nv_perm_b32 v63, v111, v109, s86\nv_accvgpr_write_b32 a144, v48\nv_accvgpr_write_b32 a145, v49\n"
    "v_accvgpr_write_b32 a146, v50\nv_accvgpr_write_b32 a147, v51\nv_accvgpr_write_b32 a148, v52\nv_accvgpr_write_b32 a149, v53\n"
    "v_accvgpr_write_b32 a150, v54\nv_accvgpr_write_b32 a151, v55\nv_accvgpr_write_b32 a152, v56\nv_accvgpr_write_b32 a153, v57\n"
    "v_accvgpr_write_b32 a154, v58\nv_accvgpr_write_b32 a155, v59\nv_accvgpr_write_b32 a156, v60\nv_accvgpr_write_b32 a157, v61\n"
    "v_accvgpr_write_b32 a158, v62\nv_accvgpr_write_b32 a159, v63\ns_waitcnt vmcnt(24)\nv_perm_b32 v48, v114, v112, s85\nv_perm_b32 v49, v118, v116, s85\n"
    "v_perm_b32 v50, v122, v120, s85\nv_perm_b32 v51, v126, v124, s85\nv_perm_b32 v52, v114, v112, s86\nv_perm_b32 v53, v118, v116, s86\n"
    "v_perm_b32 v54, v122, v120, s86\nv_perm_b32 v55, v126, v124, s86\nv_perm_b32 v56, v115, v113, s85\nv_perm_b32 v57, v119, v117, s85\n"
    "v_perm_b32 v58, v123, v121, s85\nv_perm_b32 v59, v127, v125, s85\nv_perm_b32 v60, v115, v113, s86\nv_perm_b32 v61, v119, v117, s86\n"
    "v_perm_b32 v62, v123, v121, s86\nv_perm_b32 v63, v127, v125, s86\nv_accvgpr_write_b32 a160, v48\nv_accvgpr_write_b32 a161, v49\n"
    "v_accvgpr_write_b32 a162, v50\nv_accvgpr_write_b32 a163, v51\nv_accvgpr_write_b32 a164, v52\nv_accvgpr_write_b32 a165, v53\n"
    "v_accvgpr_write_b32 a166, v54\nv_accvgpr_write_b32 a167, v55\nv_accvgpr_write_b32 a168, v56\nv_accvgpr_write_b32 a169, v57\n"
    "v_accvgpr_write_b32 a170, v58\nv_accvgpr_write_b32 a171, v59\nv_accvgpr_write_b32 a172, v60\nv_accvgpr_write_b32 a173, v61\n"
    "v_accvgpr_write_b32 a174, v62\nv_accvgpr_write_b32 a175, v63\ns_waitcnt vmcnt(16)\nv_perm_b32 v48, v130, v128, s85\nv_perm_b32 v49, v134, v132, s85\n"
    "v_perm_b32 v50, v138, v136, s85\nv_perm_b32 v51, v142, v140, s85\nv_perm_b32 v52, v130, v128, s86\nv_perm_b32 v53, v134, v132, s86\n"
    "v_perm_b32 v54, v138, v136, s86\nv_perm_b32 v55, v142, v140, s86\nv_perm_b32 v56, v131, v129, s85\nv_perm_b32 v57, v135, v133, s85\n"
    "v_perm_b32 v58, v139, v137, s85\nv_perm_b32 v59, v143, v141, s85\nv_perm_b32 v60, v131, v129, s86\nv_perm_b32 v61, v135, v133, s86\n"
    "v_perm_b32 v62, v139, v137, s86\nv_perm_b32 v63, v143, v141, s86\nv_accvgpr_write_b32 a176, v48\nv_accvgpr_write_b32 a177, v49\n"
    "v_accvgpr_write_b32 a178, v50\nv_accvgpr_write_b32 a179, v51\nv_accvgpr_write_b32 a180, v52\nv_accvgpr_write_b32 a181, v53\n"
    "v_accvgpr_write_b32 a182, v54\nv_accvgpr_write_b32 a183, v55\nv_accvgpr_write_b32 a184, v56\nv_accvgpr_write_b32 a185, v57\n"
    "v_accvgpr_write_b32 a186, v58\nv_accvgpr_write_b32 a187, v59\nv_accvgpr_write_b32 a188, v60\nv_accvgpr_write_b32 a189, v61\n"
    "v_accvgpr_write_b32 a190, v62\nv_accvgpr_write_b32 a191, v63\ns_waitcnt vmcnt(8)\nv_perm_b32 v48, v146, v144, s85\nv_perm_b32 v49, v150, v148, s85\n"
    "v_perm_b32 v50, v154, v152, s85\nv_perm_b32 v51, v158, v156, s85\nv_perm_b32 v52, v146, v144, s86\nv_perm_b32 v53, v150, v148, s86\n"
    "v_perm_b32 v54, v154, v152, s86\nv_perm_b32 v55, v158, v156, s86\nv_perm_b32 v56, v147, v145, s85\nv_perm_b32 v57, v151, v149, s85\n"
    "v_perm_b32 v58, v155, v153, s85\nv_perm_b32 v59, v159, v157, s85\nv_perm_b32 v60, v147, v145, s86\nv_perm_b32 v61, v151, v149, s86\n"
    "v_perm_b32 v62, v155, v153, s86\nv_perm_b32 v63, v159, v157, s86\nv_accvgpr_write_b32 a192, v48\nv_accvgpr_write_b32 a193, v49\n"
    "v_accvgpr_write_b32 a194, v50\nv_accvgpr_write_b32 a195, v51\nv_accvgpr_write_b32 a196, v52\nv_accvgpr_write_b32 a197, v53\n"
    "v_accvgpr_write_b32 a198, v54\nv_accvgpr_write_b32 a199, v55\nv_accvgpr_write_b32 a200, v56\nv_accvgpr_write_b32 a201, v57\n"
    "v_accvgpr_write_b32 a202, v58\nv_accvgpr_write_b32 a203, v59\nv_accvgpr_write_b32 a204, v60\nv_accvgpr_write_b32 a205, v61\n"
    "v_accvgpr_write_b32 a206, v62\nv_accvgpr_write_b32 a207, v63\ns_waitcnt vmcnt(0)\nv_perm_b32 v48, v162, v160, s85\nv_perm_b32 v49, v166, v164, s85\n"
    "v_perm_b32 v50, v170, v168, s85\nv_perm_b32 v51, v174, v172, s85\nv_perm_b32 v52, v162, v160, s86\nv_perm_b32 v53, v166, v164, s86\n"
    "v_perm_b32 v54, v170, v168, s86\nv_perm_b32 v55, v174, v172, s86\nv_perm_b32 v56, v163, v161, s85\nv_perm_b32 v57, v167, v165, s85\n"
    "v_perm_b32 v58, v171, v169, s85\nv_perm_b32 v59, v175, v173, s85\nv_perm_b32 v60, v163, v161, s86\nv_perm_b32 v61, v167, v165, s86\n"
    "v_perm_b32 v62, v171, v169, s86\nv_perm_b32 v63, v175, v173, s86\nv_accvgpr_write_b32 a208, v48\nv_accvgpr_write_b32 a209, v49\n"
    "v_accvgpr_write_b32 a210, v50\nv_accvgpr_write_b32 a211, v51\nv_accvgpr_write_b32 a212, v52\nv_accvgpr_write_b32 a213, v53\n"
    "v_accvgpr_write_b32 a214, v54\nv_accvgpr_write_b32 a215, v55\nv_accvgpr_write_b32 a216, v56\nv_accvgpr_write_b32 a217, v57\n"
    "v_accvgpr_write_b32 a218, v58\nv_accvgpr_write_b32 a219, v59\nv_accvgpr_write_b32 a220, v60\nv_accvgpr_write_b32 a221, v61\n"
    "v_accvgpr_write_b32 a222, v62\nv_accvgpr_write_b32 a223, v63\n"
    :: [rw] "s"(rw), [vo] "v"(vo)
    : "memory", "scc", CLS, CLV_48_175, CLA_0_255);
}

constexpr unsigned MB = 1u << 20;
constexpr unsigned OFF_MOD = 128u << 10, OFF_H = 2 * MB, OFF_S = 4 * MB, OFF_Y = 12 * MB, OFF_KQ = 28 * MB, OFF_B = 60 * MB, WS_USED = 124 * MB;
constexpr unsigned OFF_G = 4096;
constexpr unsigned PG_H = 0, PG_POI = PG_H + 64 * 128, PG_F1 = PG_POI + 32 * 128, PG_F2 = PG_F1 + 32 * 128, PG_LN = PG_F2 + 32 * 128,
                   PG_X4 = PG_F2 + 64 * 128, PG_VX = PG_X4 + 64 * 128,
                   PG_X2 = PG_VX + 256 * 128,
                   PG_X3 = PG_X2 + 128 * 128,
                   PG_END = PG_X3 + 128 * 128;
constexpr unsigned OOB = 0x80000000u;
constexpr int BUD = 200000;
constexpr int KST = 272;
constexpr unsigned CODE_BYTES = 98 * 1024;
constexpr int LDS_BYTES = 128 * 1024;

struct Prm {
  const u16 *x, *mem0, *Wq, *Wk, *Wv, *Wo, *wg, *wu, *wd, *c, *wm;
  u16* out; unsigned char* ws; unsigned* log;
  int wM, wV, wP, wW1, wWV, wS, map, wC, ring, pad;
};

DI float b2f(unsigned x) { return __uint_as_float(x << 16); }
DI float lo2f(unsigned x) { return __uint_as_float(x << 16); }
DI float hi2f(unsigned x) { return __uint_as_float(x & 0xffff0000u); }
DI unsigned cvtpk(float lo, float hi) { f32x2 v = {lo, hi}; return __builtin_bit_cast(unsigned, __builtin_convertvector(v, bf16x2)); }
DI float rbf(float x) { return lo2f(cvtpk(x, x)); }
DI rsrc_t mkrs(const void* p, unsigned n) { return __builtin_amdgcn_make_buffer_rsrc((void*)p, 0, n, 0x00020000); }
DI v4u ld16(rsrc_t r, unsigned v, unsigned s) { return __builtin_amdgcn_raw_buffer_load_b128(r, v, s, 16); }
DI v2u ld8(rsrc_t r, unsigned v, unsigned s) { return __builtin_amdgcn_raw_buffer_load_b64(r, v, s, 16); }
DI unsigned ld4(rsrc_t r, unsigned v, unsigned s) { return __builtin_amdgcn_raw_buffer_load_b32(r, v, s, 16); }
DI f32x4 ldmod(rsrc_t r, int i) { return __builtin_bit_cast(f32x4, ld16(r, OFF_MOD + i * 4, 0)); }
DI void st16(v4u d, rsrc_t r, unsigned v, unsigned s) { __builtin_amdgcn_raw_buffer_store_b128(d, r, v, s, 16); }
DI void st4(unsigned d, rsrc_t r, unsigned v, unsigned s) { __builtin_amdgcn_raw_buffer_store_b32(d, r, v, s, 16); }
DI void drain() { asm volatile("s_waitcnt vmcnt(0)" ::: "memory"); }
DI void bar() { asm volatile("s_waitcnt lgkmcnt(0)\n\ts_barrier" ::: "memory"); }
DI void cfence() { asm volatile("" ::: "memory"); }
DI f32x4 mfma(v4u a, v4u b, f32x4 c) { return __builtin_amdgcn_mfma_f32_16x16x32_bf16(__builtin_bit_cast(bf16x8, a), __builtin_bit_cast(bf16x8, b), c, 0, 0, 0); }
DI unsigned min4(v4u v) { return min(min(v.x, v.y), min(v.z, v.w)); }
DI bool all_ge(unsigned v, unsigned need) { return __builtin_amdgcn_ballot_w64(v < need) == 0; }
DI void publish(rsrc_t rc, unsigned off, unsigned val, int lane) { drain(); st4(val, rc, lane == 0 ? off : OOB, 0); }
template <int NW, int SL = 2> DI void wait_ge(rsrc_t rc, unsigned voff, unsigned need, int& bud) {
  for (;;) {
    cfence();
    unsigned m;
    if (NW == 4) m = min4(ld16(rc, voff, 0)); else if (NW == 2) { v2u v = ld8(rc, voff, 0); m = min(v.x, v.y); } else m = ld4(rc, voff, 0);
    if (all_ge(m, need) || --bud <= 0) break;
    __builtin_amdgcn_s_sleep(SL);
  }
  cfence();
}
template <int CTRL> DI float dpp(float x) { return __builtin_bit_cast(float, __builtin_amdgcn_mov_dpp(__builtin_bit_cast(int, x), CTRL, 0xf, 0xf, true)); }
template <int CTRL> DI unsigned dppu(unsigned x) { return (unsigned)__builtin_amdgcn_mov_dpp((int)x, CTRL, 0xf, 0xf, true); }
DI float sum16(float x) { x += dpp<0x128>(x); x += dpp<0x124>(x); x += dpp<0x4E>(x); x += dpp<0xB1>(x); return x; }
DI float xsum16(float x) { auto r = __builtin_amdgcn_permlane16_swap(__float_as_uint(x), __float_as_uint(x), false, false); return __uint_as_float(r[0]) + __uint_as_float(r[1]); }
DI float xsum32(float x) { auto r = __builtin_amdgcn_permlane32_swap(__float_as_uint(x), __float_as_uint(x), false, false); return __uint_as_float(r[0]) + __uint_as_float(r[1]); }
DI float xmax16(float x) { auto r = __builtin_amdgcn_permlane16_swap(__float_as_uint(x), __float_as_uint(x), false, false); return fmaxf(__uint_as_float(r[0]), __uint_as_float(r[1])); }
DI float xmax32(float x) { auto r = __builtin_amdgcn_permlane32_swap(__float_as_uint(x), __float_as_uint(x), false, false); return fmaxf(__uint_as_float(r[0]), __uint_as_float(r[1])); }

struct Par { unsigned b; };
DI unsigned nearh(unsigned o, const Par& q) { return o + (((o >> 8) ^ q.b) & 0x2000u); }
DI unsigned offS(int s, const Par& q) { return nearh(OFF_S + (s & 31) * 262144u, q); }
DI unsigned offY(int s, const Par& q) { return nearh(OFF_Y + (s & 31) * 524288u, q); }
DI unsigned offK(int s, const Par& q) { return nearh(OFF_KQ + (s & 31) * MB, q); }
DI unsigned offB(int s, const Par& q) { return nearh(OFF_B + (s & 31) * 2 * MB, q); }

constexpr unsigned FC_PX = 0, FC_X1 = 10 * 16384, FC_ST = 14 * 16384, FC_HH = 18 * 16384;
DI unsigned offF(int s, const Par& q) { return offB(s, q) ^ 8192u; }
DI unsigned pgw(unsigned o, const Par& q) { return OFF_S + (o >> 15) * 262144u + 0x30000u + ((o >> 13) & 3) * 16384u + (q.b ^ 0x2000u) + (o & 8191u); }

#define TSA
#define LOGDECL
#define LOGSTEP(N)
#define LOGT(i)
#define LOGC(i)
#define LOGS(i)
#define LOGOUT(slot)

#define LOGS2(i)
template <int NP> DI void ln1_load(const Prm& p, int row0, int lane, v4u (&xr)[NP][2]) {
#pragma unroll
  for (int i = 0; i < NP; ++i) {
    const GAS v4u* src = (const GAS v4u*)(p.x + (size_t)(row0 + 2 * i + (lane >> 5)) * 512 + 16 * (lane & 31));
    xr[i][0] = src[0]; xr[i][1] = src[1];
  }
}
DI void ln1_norm(v4u (&xr)[2]) {
  float v[16], s = 0.f;
#pragma unroll
  for (int k = 0; k < 8; ++k) { const unsigned u = xr[k >> 2][k & 3]; v[2 * k] = lo2f(u); v[2 * k + 1] = hi2f(u); s += v[2 * k] + v[2 * k + 1]; }
  const float mean = xsum16(sum16(s)) * (1.f / 512.f);
  float q = 0.f;
#pragma unroll
  for (int i = 0; i < 16; ++i) { const float d = v[i] - mean; q = __builtin_fmaf(d, d, q); }
  q = xsum16(sum16(q));
  const float m = rbf(mean), sd = rbf(__builtin_sqrtf(rbf(rbf(q * (1.f / 512.f)) + 1e-5f))), rs = __builtin_amdgcn_rcpf(sd);
#pragma unroll
  for (int k = 0; k < 8; ++k) { const unsigned t = cvtpk(v[2 * k] - m, v[2 * k + 1] - m); xr[k >> 2][k & 3] = cvtpk(lo2f(t) * rs, hi2f(t) * rs); }
  asm volatile("" : "+v"(xr[0]), "+v"(xr[1]));
}
template <int NP> DI void ln1_rows(rsrc_t rc, int row0, int lane, const v4u (&xr)[NP][2]) {
  const int m = lane & 31;
  float c1[16], sh[16];
#pragma unroll
  for (int k = 0; k < 4; ++k) {
    const f32x4 a = ldmod(rc, 16 * m + 4 * k) + ldmod(rc, 7168 + 16 * m + 4 * k), b = ldmod(rc, 512 + 16 * m + 4 * k) + ldmod(rc, 7680 + 16 * m + 4 * k);
#pragma unroll
    for (int j = 0; j < 4; ++j) { const unsigned t = cvtpk(a[j], b[j]); sh[4 * k + j] = lo2f(t); c1[4 * k + j] = rbf(1.f + hi2f(t)); }
  }
#pragma unroll
  for (int i = 0; i < NP; ++i) {
    float v[16];
#pragma unroll
    for (int k = 0; k < 8; ++k) {
      const unsigned u = xr[i][k >> 2][k & 3], t = cvtpk(lo2f(u) * c1[2 * k], hi2f(u) * c1[2 * k + 1]);
      v[2 * k] = lo2f(t) + sh[2 * k]; v[2 * k + 1] = hi2f(t) + sh[2 * k + 1];
    }
    v4u e, o;
#pragma unroll
    for (int k = 0; k < 4; ++k) { e[k] = cvtpk(v[4 * k], v[4 * k + 2]); o[k] = cvtpk(v[4 * k + 1], v[4 * k + 3]); }
    v4u a, b;
#pragma unroll
    for (int k = 0; k < 4; ++k) {
      const unsigned e0 = dppu<0x50>(e[k]), o0 = dppu<0x50>(o[k]), e1 = dppu<0xFA>(e[k]), o1 = dppu<0xFA>(o[k]);
      a[k] = (m & 1) ? o0 : e0; b[k] = (m & 1) ? o1 : e1;
    }
    const unsigned off = OFF_H + (row0 + 2 * i + (lane >> 5)) * 1024 + 128 * (m >> 2) + 16 * (m & 3);
    st16(a, rc, off, 0); st16(b, rc, off + 64, 0);
  }
}

DI void g_put(rsrc_t rc, int e, u64 v, bool on) { __builtin_amdgcn_raw_buffer_store_b64((v2u){(unsigned)v, (unsigned)(v >> 32)}, rc, on ? OFF_G + e * 64 : OOB, 0, 16); }
DI u64 g_wait(rsrc_t rc, int e, int e2, u64 tag, int& bud) {
  v2u v;
  for (;;) {
    cfence();
    v = ld8(rc, OFF_G + e * 64, 0);
    const v2u w = ld8(rc, OFF_G + e2 * 64, 0);
    const unsigned lo = (unsigned)tag, hi = (unsigned)(tag >> 32);
    if (__builtin_amdgcn_ballot_w64((v.x & ~1u) != lo || v.y != hi || (w.x & ~1u) != lo || w.y != hi) == 0 || --bud <= 0) break;
    __builtin_amdgcn_s_sleep(3);
  }
  cfence();
  return __builtin_amdgcn_ballot_w64(v.x & 1);
}
DI Par get_par(rsrc_t rc, int x, int e2, u64 tag, bool all, LAS unsigned char* lds, int& bud) {
  bool m = false;
  if (__builtin_amdgcn_readfirstlane(threadIdx.x) < 64) { m = g_wait(rc, 128 + 2 * x, e2, tag, bud) != 0; *(LAS unsigned*)(lds + 126976) = m; }
  if (all) { bar(); m = __builtin_amdgcn_readfirstlane(*(LAS unsigned*)(lds + 126976)); }
  Par q; q.b = m ? 0x2000u : 0u;
  return q;
}
template <int NR, bool HALF> DI void mod_load(const Prm& p, int cb, int k0, v4u (&w)[NR]) {
#pragma unroll
  for (int i = 0; i < NR; ++i) w[i] = *(const GAS v4u*)(p.wm + (size_t)(k0 + (threadIdx.x >> 3) + 32 * i) * 3072 + cb * 64 + (threadIdx.x & 7) * 8);
}
template <int NR, bool HALF> DI void mod_tile(const Prm& p, rsrc_t rc, int cb, int k0, const v4u (&w)[NR], unsigned cu, LAS unsigned char* lds) {
  LAS float* red = (LAS float*)lds; LAS float* sc = red + 32 * 65;
  const int t = threadIdx.x, rg = t >> 3, ch = t & 7;
  { const float v0 = lo2f(cu), v1 = hi2f(cu);
    sc[2 * t] = rbf(v0 / (1.f + __expf(-v0))); sc[2 * t + 1] = rbf(v1 / (1.f + __expf(-v1))); }
  bar();
  LOGS2(5)
  float a[8] = {0.f, 0.f, 0.f, 0.f, 0.f, 0.f, 0.f, 0.f};
#pragma unroll
  for (int i = 0; i < NR; ++i) { const float s = sc[k0 + rg + 32 * i];
#pragma unroll
    for (int k = 0; k < 4; ++k) { a[2 * k] = __builtin_fmaf(s, lo2f(w[i][k]), a[2 * k]); a[2 * k + 1] = __builtin_fmaf(s, hi2f(w[i][k]), a[2 * k + 1]); } }
#pragma unroll
  for (int k = 0; k < 8; ++k) red[rg * 65 + ch * 8 + k] = a[k];
  bar();
  LOGS2(6)
  const int cl = t >> 2, part = t & 3;
  float s = 0.f;
#pragma unroll
  for (int i = 0; i < 8; ++i) s += red[(part * 8 + i) * 65 + cl];
  s += dpp<0xB1>(s); s += dpp<0x4E>(s);
  const int i = cb * 64 + cl;
  if (HALF) st4(__float_as_uint(s), rc, part == 0 ? OFF_MOD + (k0 ? 28672 : 0) + i * 4 : OOB, 0);
  else {
    const float v = rbf(s);
    st4(__float_as_uint(part ? rbf(1.f + v) : v), rc, part == 0 ? OFF_MOD + i * 4 : (part == 1 && (i >> 9) == 4) ? OFF_MOD + 24576 + (i & 511) * 4 : OOB, 0);
    __builtin_amdgcn_raw_buffer_store_b16((u16)(__float_as_uint(v) >> 16), rc, part == 0 ? OFF_MOD + 16384 + i * 2 : OOB, 0, 16);
  }
  drain(); bar();
}
DI void probe(const Prm& p, rsrc_t rc, int i, u64 tag, int lane) {
  unsigned best[2] = {~0u, ~0u};
  const u64 tend = __builtin_amdgcn_s_memrealtime() + 350;
#pragma clang loop unroll(disable)
  for (int it = 0; it < 64; ++it) {
    GAS unsigned* a = (GAS unsigned*)(p.ws + OFF_S + (i & 7) * 262144 + 0x2C000 + (it & 1) * 8192 + (i >> 3) * 1024 + ((it >> 1) & 1) * 256) + (lane & 31);
    unsigned v; u64 t0, t1;
    asm volatile("global_store_dword %3, %4, off sc1\n\ts_waitcnt vmcnt(0)\n\ts_memtime %1\n\ts_waitcnt lgkmcnt(0)\n\tglobal_load_dword %0, %3, off sc1\n\ts_waitcnt vmcnt(0)\n\ts_memtime %2\n\ts_waitcnt lgkmcnt(0)"
                 : "=&v"(v), "=&s"(t0), "=&s"(t1) : "v"(a), "v"(it) : "memory");
    const unsigned d = (unsigned)(t1 - t0);
    if (it & 1) best[1] = min(best[1], d); else best[0] = min(best[0], d);
    if (it >= 3 && (it & 1) && __builtin_amdgcn_s_memrealtime() > tend) break;
  }
  g_put(rc, i, tag | (best[1] > best[0]), lane == 0);
}

template <int W> DI void role_s1(const Prm& p, rsrc_t rc, const Par& q, int h, LAS unsigned char* lds) {
  const int lane = threadIdx.x & 63, n = lane & 15, gb = lane >> 4;
  const unsigned lb = (unsigned)(size_t)lds;
  const unsigned pgf = pgw(PG_F1 + h * 128, q);
  v16u cv = (v16u)(0u);
  cv[0] = lane * 16 + W * 1024; cv[1] = lb + 8192 + gb * 256 + (n & 1) * 128 + (n >> 1) * 16; cv[2] = (lane & 2) ? 0xEEEE : 0x4444;
  cv[3] = W < 2 ? lb + 1088 * (W & 1) + 272 * (2 * ((n >> 1) & 1) + (n >> 3)) + 64 * gb + 4 * (2 * ((n >> 2) & 1) + (n & 1)) : lb + 4096 + 2 * gb * KST + 128 * (W & 1) + 8 * n;
  cv[4] = lane * 16; cv[5] = lb + 272 * gb + 16 * n; cv[6] = lb + 4096 + (n & 7) * KST + 32 * gb;
  cv[7] = 0x4000 * (1 + W) + h * 256 + lane * 4;
  cv[8] = lane < 16 ? 0xC000 + W * 4096 + h * 128 + ((n & 1) + 2 * (n >> 3)) * 16 + ((n >> 1) & 1) * 8 + ((n >> 2) & 1) * 4 : OOB;
  if (W >= 2) { cv[7] = (h >> 3) * 16384 + (h & 7) * 1024 + lane * 16; cv[8] = lane < 16 ? 65536u + h * 256 + lane * 16 : OOB; }
  cv[9] = W == 0 ? 0x20000u + (lane & 1) * 4096u : ((h < 2 && lane < 16) ? 0x14000u + h * 4096u + lane * 256u : OOB);
  cv[10] = cv[0] + 4096; cv[13] = lb + 8192 + W * 1024 + lane * 16; cv[14] = W < 2 ? gb * 256 + ((n & 8) + 2 * (n & 3) + ((n >> 2) & 1)) * 16 : n - 4 * gb;
  cv[11] = W >= 2 ? pgf + (lane & 3) * 4 : OOB; cv[12] = lane == 0 ? pgw(PG_VX + (32 + h) * 128, q) : OOB;
  const unsigned src = offK(0, q) + ((W & 1) * 32 + h) * 16384;
  unsigned nret = 0; int bud = BUD;
  LOGDECL
  LOGS(3)
  v8u pt = (v8u)(0u); unsigned known = 1;
  const unsigned c7 = cv[7], c8 = cv[8], c9 = cv[9], c12 = cv[12], lm = lb + 8192 + W * 1024;
  if (W >= 2) *(LAS unsigned*)(lds + 6656 + (W & 1) * 256 + lane * 4) = 0;
  int zero = 0; asm volatile("" : "+s"(zero));
  cv[7] = cv[8] = cv[12] = OOB; if (W) cv[9] = OOB;
  bud = 1;
#pragma clang loop unroll(disable)
  for (int s = zero - 1; s < 64; ++s) {
    const unsigned sM = s < 0 ? offS(1, q) ^ 8192u : offS(s, q), kq = 16384 + (s & 1) * 16384;
    const unsigned sK = offK(s > 0 ? min(s + 1, 63) : 31, q) + h * 16384 + (W >> 1) * 524288 + (W & 1) * 4096, ln = lb + (s > 0 ? kq ^ 49152 : 65536) + W * 4096;
    if (W == 0) s1_step0(rc, sM, p.wM, lb + kq, lm, ln, sK, cv, bud, nret TSA, s > 0 ? offS(s - 1, q) : s ? sM : ~0u, p.wW1);
    if (W == 1 && s == 63) cv[12] = lane < 2 ? pgw(PG_VX + (32 + h) * 128, q) + lane * 16384 : OOB;
    if (W == 1) s1_step1(rc, sM, p.wM, lb + kq, lm, ln, sK, cv, bud, nret TSA, s == 63 ? 63 : max(s - 1, 0), ((s + 2) & 3) * 16384u);
    if (W >= 2) {
      const unsigned lpg = 6656 + (W & 1) * 256, sX = offF(s == 63 ? 63 : max(s - 1, 0), q) + FC_PX + (W & 1) * 81920u;
      if (W == 2) s1_step2(rc, sM, p.wM, lb + kq, lm, ln, sK, cv, bud, nret TSA, lb + lpg, sX, pt); else s1_step3(rc, sM, p.wM, lb + kq, lm, ln, sK, cv, bud, nret TSA, lb + lpg, sX, pt);
      if (s == 62) { const unsigned sZ = offF(62, q) + FC_PX + (W & 1) * 81920u; st16(pt.lo, rc, cv[7], sZ); st16(pt.hi, rc, cv[8], sZ); }
      const unsigned need = min(s + 3, 64);
      if (s >= 0 && known < need + 2) {
        const unsigned pg = *(volatile LAS unsigned*)(lds + lpg + lane * 4);
        if (all_ge(pg, min(need + 6, 64u))) known = need + 6; else if (all_ge(pg, need + 2)) known = need + 2;
        else if (known < need) { if (!all_ge(pg, need)) wait_ge<4>(rc, pgf, need, bud); known = need; }
      }
      cv[11] = (known < 64 && known < need + 4) ? pgf + (lane & 3) * 4 : OOB;
    }
    if (s <= 0) {
      if (s < 0) { cv[7] = c7; cv[8] = c8; cv[9] = c9; cv[12] = c12; bud = BUD; nret = 0; bar(); }
      if (W < 2) {
        wait_ge<4>(rc, pgf, s + 2, bud);
#pragma unroll
        for (int i = 0; i < 8; ++i) *(LAS v4u*)(lds + (kq ^ 49152) + W * 8192 + i * 1024 + lane * 16) = ld16(rc, lane * 16 + i * 1024, src + (s + 1) * MB);
        asm volatile("s_waitcnt lgkmcnt(0)" ::: "memory");
      }
      if (s < 0) continue;
    }
    LOGSTEP(4);
  }
  if (W >= 2) { const unsigned sX = offF(63, q) + FC_PX + (W & 1) * 81920u; st16(pt.lo, rc, cv[7], sX); st16(pt.hi, rc, cv[8], sX); }
  drain(); bar();
  if (W == 2) st4(64u, rc, cv[12], 3 * 16384u);
  LOGOUT(h)
}

DI void role_c(const Prm& p, rsrc_t rc, const Par& q, int j, int rh, int W, LAS unsigned char* lds) {
  const int lane = threadIdx.x & 63, n = lane & 15, gb = lane >> 4;
  if (W) return;
  v16u cv = (v16u)(0u);
  const int rot = (j ^ rh) & 1;
  for (int k = 0; k < 2; ++k) { cv[k] = (gb >> 1) * 1024 + (gb & 1) * 256 + n * 16 + (k ^ rot) * 4096; cv[2 + k] = 0x4000 * (1 + rh) + lane * 16 + (k ^ rot) * 1024; }
  if (p.map & 32) cv[0] = OOB;
  if (p.map & 128) cv[1] = OOB;
  cv[4] = (p.map & 256) ? OOB : lane * 16; cv[5] = (lane < 16 && (j & 15) == 0) ? 0x20000u + rh * 16384u + (j >> 4) * 4096u + lane * 256u : OOB;
  cv[6] = 0xC000 + rh * 4096 + (8 * ((n >> 1) & 1) + 2 * gb) * 128 + (n >> 2) * 16 + (n & 1) * 8;
  const unsigned mo = j * 256 + rh * 128 + ((gb & 2) + (n & 1)) * 32 + ((n >> 1) & 1) * 16 + 4 * (n >> 2);
  cv[7] = mo + (gb & 1) * 0x10000; cv[8] = mo + (gb & 1) * 0x4000;
  cv[9] = (lane & 1) ? 0xEEEE : 0x4444; cv[10] = 0x14000u + (j >> 4) * 4096u;
  const unsigned vpg = pgw(PG_F2 + (lane & 31) * 128, q);
  unsigned nret = 0; int bud = BUD;
  LOGDECL
  LOGS(3)
  unsigned known = 1;
  const unsigned c0 = cv[0], c1 = cv[1], c5 = cv[5], c7 = cv[7], c8 = cv[8];
  int zero = 0; asm volatile("" : "+s"(zero));
  cv[0] = cv[1] = cv[5] = cv[7] = cv[8] = OOB; bud = 1; nret = 19999;
#pragma clang loop unroll(disable)
  for (int s = zero - 1; s < 63; ++s) {
    unsigned prog;
    const bool smp = s >= 0 && known < (unsigned)s + 5;
    const unsigned fl = s < 0 ? 8192u : 0u;
    c_step(rc, offS(s < 0 ? 1 : s, q) ^ fl, offS(s + 1, q), (offY(max(s, 0), q) ^ fl) + j * 16384, offB(s, q) + j * 65536, p.wP, p.wS, p.wC, cv, smp ? vpg : OOB, prog, bud, nret TSA);
    if (s < 0) { cv[0] = c0; cv[1] = c1; cv[5] = c5; cv[7] = c7; cv[8] = c8; bud = BUD; nret = 0; wait_ge<4, 6>(rc, vpg, 1u, bud); continue; }
    if (known < (unsigned)s + 4) {
      if (all_ge(prog, s + 12)) known = s + 12; else if (all_ge(prog, s + 8)) known = s + 8; else if (all_ge(prog, s + 5)) known = s + 5; else if (all_ge(prog, 64)) known = 99;
      else if (known < (unsigned)s + 2 && !all_ge(prog, s + 2)) wait_ge<4, 6>(rc, vpg, s + 2, bud);
    }
    LOGSTEP(7);
  }
  drain();
  if (rh == 0) { LOGOUT(32 + j) }
}

DI void role_v(const Prm& p, rsrc_t rc, const Par& q, int h, int W, LAS unsigned char* lds) {
  const int lane = threadIdx.x & 63, n = lane & 15, gb = lane >> 4;
  const unsigned lb = (unsigned)(size_t)lds;
  v16u cv = (v16u)(0u);
  cv[0] = lane * 16 + W * 1024; cv[1] = lb + 8192 + gb * 256 + (n & 1) * 128 + (n >> 1) * 16; cv[2] = (lane & 2) ? 0xEEEE : 0x4444;
  cv[3] = lb + 1088 * (W >> 1) + 272 * (2 * (W & 1) + (n >> 3)) + 64 * gb + 32 * (n & 1) + 4 * ((n >> 1) & 3);
  cv[4] = lb + 272 * gb + 16 * n; cv[5] = 0x24000u + (lane & 1) * 4096u;
  cv[6] = (8 * W + gb) * 16384 + h * 256 + n * 16; cv[7] = cv[0] + 4096;
  cv[8] = (lane == 0 && W == 3) ? pgw(PG_VX + h * 128, q) : OOB; cv[9] = lb + 8192 + W * 1024 + lane * 16;
  { const unsigned o = (h * 4 + W) * 640 + lane * 16; cv[10] = lane < 40 ? (o >> 13) * 16384 + (o & 8191) : OOB; }
  unsigned nret = 0; int bud = BUD;
  LOGDECL
  LOGS(3)
  int zero = 0; asm volatile("" : "+s"(zero));
  const unsigned c6 = cv[6], c8 = cv[8], c10 = cv[10];
  cv[6] = cv[8] = cv[10] = OOB; bud = 1;
#pragma clang loop unroll(disable)
  for (int s = zero - 1; s < 64; ++s) {
    const unsigned sM = s < 0 ? offS(1, q) ^ 8192u : offS(s, q);
    v_step(rc, sM, (s && W == 0) ? (s > 0 ? offS(s - 1, q) : sM) : ~0u, offY(s, q), s < 62 ? offY(s + 2, q) : offY(s, q) ^ 8192u, s < 61 ? offS(s + 3, q) : offS(63, q) ^ 8192u, lb + 8192 + W * 1024, p.wV, p.wWV, s, ((s + 3) & 3) * 16384u, cv, bud, nret TSA);
    if (s < 0) { cv[6] = c6; cv[8] = c8; cv[10] = c10; bud = BUD; nret = 0; continue; }
    LOGSTEP(4);
  }
  drain(); bar();
  st4(64u, rc, cv[8], 3 * 16384u);
  LOGOUT(64 + h)
}

template <int F> DI void role_f(const Prm& p, rsrc_t rc, const Par& q, int h, int W, LAS unsigned char* lds) {
  const int lane = threadIdx.x & 63, n = lane & 15, gb = lane >> 4;
  const unsigned lb = (unsigned)(size_t)lds, pgo = pgw((F == 1 ? PG_F1 : PG_F2) + h * 128 + W * 4, q);
  v16u cv = (v16u)(0u);
  for (int k = 0; k < 4; ++k) cv[k] = n * 1024 + ((4 * k + 2 * (gb & 1) + (gb >> 1)) ^ n) * 16;
  cv[4] = (lane ^ (8 * (W & 1))) * 16;
  cv[5] = F == 1 ? OFF_KQ + ((W < 2 ? 32 : 0) + h) * 16384 + (W & 1) * 2048 + lane * 16 : OFF_B + W * 8 * 65536 + (h >> 3) * 16384 + (h & 7) * 1024 + lane * 16;
  cv[6] = lane == 0 ? pgo : OOB; cv[11] = (W == 0 && lane < 4) ? pgo + lane * 4 : OOB;
  cv[7] = lb + 98304 + W * 1024 + lane * 16; cv[8] = lb + 98304 + lane * 16;
  int bud = BUD;
  LOGDECL
  LOGS(3)
  cv[9] = lb + 120 * 1024 + (W ? 4096 : 0); cv[10] = pgw(lane < 32 ? PG_X4 + (lane & 15) * 128 : PG_H + lane * 128, q);
  for (int i = 0; i <= F; ++i) dma_rows(rc, OFF_H + i * 32768 + W * 8192, lb + i * 32768 + W * 8192, cv[4]);
  if (F == 1) f1_loop(rc, lb, lb + 98304, OFF_H + 2 * 32768 + W * 8192, OFF_H + 63 * 32768 + W * 8192, lb + 65536 + W * 8192, (q.b ? 0x55555555u : 0xAAAAAAAAu), W, cv);
  else f2_loop(rc, lb, lb + 98304, OFF_H + 3 * 32768 + W * 8192, OFF_H + 63 * 32768 + W * 8192, lb + W * 8192, (q.b ? 0x55555555u : 0xAAAAAAAAu), W, cv);
  publish(rc, pgo, 64u, lane);
  LOGOUT(64 + 32 * F + h)
}

DI void role_x2(const Prm& p, rsrc_t rc, const Par& q, int set, int c, int W, LAS unsigned char* lds) {
  const int lane = threadIdx.x & 63, id = set * 8 + c;
  const int tok = 8 * W + (lane >> 3), col = 64 * c + 8 * (lane & 7);
  const f32x4 ga0 = ldmod(rc, 1024 + col), ga1 = ldmod(rc, 1028 + col);
  const float ga[8] = {ga0[0], ga0[1], ga0[2], ga0[3], ga1[0], ga1[1], ga1[2], ga1[3]};
  int bud = BUD;
  LOGDECL
#pragma clang loop unroll(disable)
  for (int s = (p.map & 16) ? 64 : set; s < 64; s += 4) {
    wait_ge<4, 40>(rc, pgw(PG_F2 + (8 * W + (lane & 7)) * 128, q), s + 1, bud);
    const v4u xv = *(const GAS v4u*)(p.x + (size_t)(s * 32 + tok) * 512 + col);
    x2_loadb(rc, offB(s, q) + c * 4 * 65536 + W * 16384, lane * 16 + c * 1024);
    if (W == 0) wait_ge<1, 3>(rc, pgw(PG_VX + set * 8192 + lane * 128, q), s + 1, bud);
    bar();
    LOGT(0);
    const unsigned sF = offF(s, q), sP = sF + FC_PX;
    x2_main(rc, sP + W * 16384, sP + 65536 + W * 2048, offY(s, q) + c * 4 * 16384 + W * 2048, lane * 16, lane * 16 + c * 1024, (unsigned)(size_t)lds + W * 8192 + lane * 16);
    bar();
    LOGT(3);
    f32x4 r0 = (f32x4)(0.f), r1 = (f32x4)(0.f);
#pragma unroll
    for (int w = 0; w < 4; ++w) {
      const unsigned o = ((w * 2 + (W >> 1)) * 4 + 2 * ((lane >> 2) & 1)) * 1024 + (16 * (lane & 3) + (tok & 15)) * 16;
      r0 += *(LAS f32x4*)(lds + o); r1 += *(LAS f32x4*)(lds + o + 1024);
    }
    v4u x1; float sm = 0.f;
#pragma unroll
    for (int r = 0; r < 4; ++r) {
      unsigned t = cvtpk(r0[r], r1[r]);
      t = cvtpk(lo2f(t) * ga[2 * r], hi2f(t) * ga[2 * r + 1]);
      x1[r] = cvtpk(lo2f(xv[r]) + lo2f(t), hi2f(xv[r]) + hi2f(t));
      sm += lo2f(x1[r]) + hi2f(x1[r]);
    }
    st16(x1, rc, FC_X1 + W * 16384 + (lane >> 3) * 1024 + col * 2, sF);
    sm += dpp<0xB1>(sm); sm += dpp<0x4E>(sm); sm += dpp<0x141>(sm);
    const float mean = sm * (1.f / 64.f);
    float m2 = 0.f;
#pragma unroll
    for (int r = 0; r < 4; ++r) { const float a = lo2f(x1[r]) - mean, b = hi2f(x1[r]) - mean; m2 = __builtin_fmaf(a, a, __builtin_fmaf(b, b, m2)); }
    m2 += dpp<0xB1>(m2); m2 += dpp<0x4E>(m2); m2 += dpp<0x141>(m2);
    __builtin_amdgcn_raw_buffer_store_b64((v2u){__float_as_uint(mean), __float_as_uint(m2)}, rc, (lane & 7) == 0 ? FC_ST + c * 256 + tok * 8 : OOB, sF, 16);
    drain(); bar();
    if (W == 0) st4(s + 1, rc, lane < 4 ? pgw(PG_X2 + (lane * 32 + id) * 128, q) : OOB, 0);
    LOGT(1);
  }
  LOGOUT(160 + id)
}

DI void role_x3(const Prm& p, rsrc_t rc, const Par& q, int f, int y, int crew, int W, LAS unsigned char* lds) {
  const int lane = threadIdx.x & 63, n = lane & 15, gb = lane >> 4;
  v16u cs, bi;
  x3_prep(rc, OFF_MOD + 16384 + (1536 + 8 * gb) * 2, OFF_MOD + 24576 + 8 * gb * 4, cs, bi);
  const unsigned lb = (unsigned)(size_t)lds, pl = pgw(PG_X2 + (y * 32 + (lane & 7)) * 128, q);
  v16u cv = (v16u)(0u);
  for (int k = 0; k < 4; ++k) cv[k] = n * 1024 + ((4 * k + gb) ^ n) * 16;
  cv[4] = (lane ^ (8 * (W & 1))) * 16;
  { const int kb = 4 * f + W, w4 = kb / 14, k = kb - w4 * 14; cv[5] = FC_HH + (w4 * 2 + (k >> 3)) * 16384 + (k & 7) * 1024 + lane * 16; }
  cv[6] = pl; cv[7] = lane < 4 ? pgw(PG_X3 + (lane * 32 + crew * 16 + f) * 128 + W * 4, q) : OOB;
  cv[8] = (gb & 1) * 1024 + (16 * (gb >> 1) + n) * 8; cv[9] = W < 2 ? W * 1024 + lane * 16 : OOB;
  int bud = BUD; unsigned pre = 0, done = 0, b0 = 0;
  LOGDECL
#pragma clang loop unroll(disable)
  for (int s = (p.map & 16) ? 64 : crew * 59; s < 64;) {
    const int sn = s + 1 + (s >= 58);
    const unsigned sF = offF(s, q), sN = offF(sn, q), st0 = 65536 + (b0 >> 3);
    if (!pre) {
      drain(); st4(done, rc, cv[7], 0);
      wait_ge<1, 2>(rc, pl + (s & 3) * 1024, s + 1, bud);
      dma_rows(rc, sF + FC_X1 + W * 16384, lb + b0 + W * 8192, cv[4]);
      if (W < 2) *(LAS v4u*)(lds + st0 + W * 1024 + lane * 16) = ld16(rc, cv[9], sF + FC_ST);
      asm volatile("s_waitcnt vmcnt(0) lgkmcnt(0)" ::: "memory");
    }
    LOGT(0);
    x3_step(rc, lb + b0, lb + st0, sF, (sn & 3) * 1024, sn + 1, done, sN + FC_X1 + W * 16384, lb + (b0 ^ 32768) + W * 8192, sN + FC_ST, lb + (st0 ^ 4096) + W * 1024, cv, cs, bi, pre);
    LOGT(1);
    done = s + 1; s = sn; b0 ^= 32768;
  }
  drain(); st4(64u, rc, cv[7], 0);
  LOGOUT(192 + crew * 16 + f)
}

DI void role_x4(const Prm& p, rsrc_t rc, const Par& q, int id, int y, int crew, int W, LAS unsigned char* lds) {
  const int lane = threadIdx.x & 63, n = lane & 15, gb = lane >> 4, mt = id >> 3, J = id & 7;
  const int tok = 4 * W + gb, col = 64 * J + 4 * n, r16 = mt * 16 + tok;
  const f32x4 gbv = ldmod(rc, 2560 + col);
  const int kb = 14 * W + min(lane, 13);
  const unsigned lb = (unsigned)(size_t)lds;
  v16u cv = (v16u)(0u);
  for (int k = 0; k < 4; ++k) { cv[k] = OFF_B + FC_HH + (mt * 4 + W) * 32768 + (k >> 1) * 16384 + (k & 1) * 4096 + lane * 16; cv[10 + k] = __float_as_uint(gbv[k]); }
  cv[4] = OFF_B + FC_X1 + (r16 >> 3) * 16384 + (r16 & 7) * 1024 + col * 2;
  cv[5] = pgw(PG_X3 + (y * 32 + crew * 16 + (kb >> 2)) * 128 + (kb & 3) * 4, q);
  cv[6] = lb + W * 4096 + lane * 16; cv[7] = lb + (16 * (n >> 2) + tok) * 16 + (n & 3) * 4;
  cv[8] = r16 * 1024 + col * 2; cv[9] = (lane == 0 && W == 0) ? pgw(PG_X4 + id * 128, q) : OOB;
  cv[14] = lb + 120 * 1024 + (W ? 4096 : 0);
  LOGDECL
  if (!(p.map & 16)) x4_loop(rc, mkrs(p.out, 2048 * 1024), (q.b ? 0x55555555u : 0xAAAAAAAAu), crew * 59, cv);
  if (p.map & 16) st4(64, rc, lane == 0 ? pgw(PG_X4 + id * 128, q) : OOB, 0);
  LOGOUT(224 + crew * 16 + id)
}

__global__ void __launch_bounds__(256, 1) k_main(Prm p) {
  extern __shared__ __attribute__((aligned(16))) unsigned char lds_[];
  LAS unsigned char* lds = (LAS unsigned char*)lds_;
  const int lane = threadIdx.x & 63, W = __builtin_amdgcn_readfirstlane(threadIdx.x >> 6), n = lane & 15, gb = lane >> 4;
  const int x = blockIdx.x & 7, r = blockIdx.x >> 3, y = x - 4, k = r & 7;
  LOGS(0)
  {
    u64 pc; asm volatile("s_getpc_b64 %0" : "=s"(pc));
    const rsrc_t rp = mkrs((const void*)(pc & ~(u64)255), (p.map & 64) ? 0 : CODE_BYTES);
    asm volatile("s_mov_b32 m0, %0\n\ts_nop 4\n\tbuffer_load_dwordx4 %1, %2, 0 offen lds" :: "s"((unsigned)(size_t)lds + 114688 + W * 1024), "v"((r * 4 + W) * 1024 + lane * 16), "s"(rp) : "memory");
  }
  const rsrc_t rc = mkrs(p.ws, WS_USED);
  asm volatile("s_mov_b32 m0, %0\n\ts_nop 4\n\tbuffer_load_dword %1, %2, 0 offen sc1 lds" :: "s"((unsigned)(size_t)lds + 118784 + W * 256), "v"((W == 0 && lane < 2) ? (r * 2 + lane) * 2 * MB + 0x1F000u + x * 128 : OOB), "s"(rc) : "memory");
  u64 z0; asm volatile("s_load_dwordx2 %0, %1, 0x0" : "={s[96:97]}"(z0) : "s"(p.ws));
#define GETTAG u64 z; asm volatile("s_waitcnt lgkmcnt(0)\n\ts_mov_b64 %0, %1" : "=s"(z) : "{s[96:97]}"(z0)); const u64 tag = (z * 0x9E3779B97F4A7C15ull + 0x632BE59BD9B4E019ull) & ~1ull;
  int bud = BUD;
  if (x == 0 || x == 3) for (int i = p.ring & 255; i > 0; --i) __builtin_amdgcn_s_sleep(8);
  if (x > 3 && r >= 24) {
    if (W == 0) { GETTAG probe(p, rc, y * 8 + k, tag, lane); LOGS2(5) for (int i = p.ring >> 8; i > 0; --i) __builtin_amdgcn_s_sleep(8); }
    bar();
  }
  const unsigned wrow = (16 * (gb & 1) + (gb >> 1)) * 8192;
  const bool ff = x > 3 && r < 16;
  const int hd = x > 3 ? y * 8 + k : r;
  const unsigned lb = (unsigned)(size_t)lds;
  if (x == 0 || (ff && r < 8)) load_qk(mkrs(W < 2 ? p.Wq : p.Wk, 512 * 4096 * 2), lb + W * 8192, (32 * (W & 1) + gb) * 8192 + hd * 256 + n * 16, lb + (W >> 1) * 16384 + (wrow >> 5) + 128 * (W & 1) + 8 * n);
  else if (x == 3 || ff)
    load_vo(mkrs(p.Wv, 512 * 4096 * 2), mkrs(p.Wo, 512 * 4096 * 2), lb + W * 8192, (32 * W + gb) * 8192 + hd * 256 + n * 16, (hd * 128 + 8 * W) * 1024 + lane * 16,
            lb + (wrow >> 5) + 64 * W + 4 * n, lb + gb * 8192 + 256 * W + 4 * n);
  if (x == 0) {
    LOGS(1)
    GETTAG
    const Par q = get_par(rc, x, 128 + 2 * x, tag, true, lds, bud);
    if (W == 0) role_s1<0>(p, rc, q, r, lds); else if (W == 1) role_s1<1>(p, rc, q, r, lds); else if (W == 2) role_s1<2>(p, rc, q, r, lds); else role_s1<3>(p, rc, q, r, lds);
  } else if (x == 3) {
    LOGS(1)
    GETTAG
    role_v(p, rc, get_par(rc, x, 128 + 2 * x, tag, true, lds, bud), r, W, lds);
  } else if (x < 3) {
    const int j = (x - 1) * 16 + (r & 15), rh = r >> 4, hs = (x - 1) * 32 + r;
    v4u xr[2][2];
    GETTAG
    if (rh == 0) {
      v4u w[8];
      const unsigned cu = ((const GAS unsigned*)p.c)[threadIdx.x];
      mod_load<8, true>(p, j >> 1, (j & 1) * 256, w);
      ln1_load<2>(p, hs * 16 + W * 4, lane, xr);
      mod_tile<8, true>(p, rc, j >> 1, (j & 1) * 256, w, cu, lds);
      g_put(rc, 64 + j, tag, threadIdx.x == 0);
      LOGS(1)
    } else ln1_load<2>(p, hs * 16 + W * 4, lane, xr);
    ln1_norm(xr[0]); ln1_norm(xr[1]);
    if (W == 0) g_wait(rc, 64 + (lane & 31), 64, tag, bud);
    bar();
    LOGS2(7)
    ln1_rows<2>(rc, hs * 16 + W * 4, lane, xr);
    drain(); bar();
    g_put(rc, 192 + hs, tag, threadIdx.x == 0);
    LOGS(4)
    if (hs == 0 && W == 1) {
      const bool m = __builtin_popcount((unsigned)g_wait(rc, lane, 192 + lane, tag, bud)) > 16;
      g_put(rc, 128 + 2 * lane, tag | m, lane < 8);
    }
    if (W) return;
    const Par q = get_par(rc, x, 128 + 2 * x, tag, false, lds, bud);
    role_c(p, rc, q, j, rh, W, lds);
  } else if (r < 16) {
    const int h = y * 8 + k;
    LOGS(1)
    GETTAG
    const Par q = get_par(rc, x, 128 + 2 * x, tag, true, lds, bud);
    if (r < 8) role_f<1>(p, rc, q, h, W, lds); else role_f<2>(p, rc, q, h, W, lds);
  } else if (r < 24) {
    const int id = y * 8 + k;
    unsigned m0v = 0;
    if (id < 8) {
      const int i = id * 256 + threadIdx.x, pos = (i >> 2) & 15, row = (pos & 6) + (pos >> 3), jj = i >> 6, d = 32 * (jj >> 1) + (jj & 1) + 8 * (i & 3) + 4 * (pos & 1);
      m0v = (unsigned)p.mem0[row * 512 + d] | ((unsigned)p.mem0[row * 512 + d + 2] << 16);
    }
#pragma clang loop unroll(disable)
    for (int c = id; c < 105; c += 32) {
      const int d = c - 63, s3 = d / 10;
      const unsigned o = c < 64 ? OFF_Y + c * 16384u : c < 93 ? OFF_S + s3 * 262144u + (d - s3 * 10) * 16384u : OFF_S + ((c - 93) >> 2) * 262144u + 0x30000u + ((c - 93) & 3) * 16384u;
#pragma unroll
      for (int i = 0; i < 4; ++i) st16((v4u)(c < 93 ? 0xffffffffu : 0u), rc, threadIdx.x * 16 + i * 4096, o);
    }
    if (id < 8) {
      if (m0v == 0xffffffffu) m0v = 0xffc0ffc0u;
      st4(m0v, rc, (id * 256 + threadIdx.x) * 4, OFF_S); st4(m0v, rc, (id * 256 + threadIdx.x) * 4, OFF_S + 8192);
    }
    GETTAG
    drain(); bar(); g_put(rc, 32 + id, tag, threadIdx.x == 0);
    LOGS2(3)
    v4u xr[4][2];
    {
      v4u w[16];
      const unsigned cu = ((const GAS unsigned*)p.c)[threadIdx.x];
      mod_load<16, false>(p, 16 + id, 0, w);
      ln1_load<4>(p, (32 + id) * 32 + W * 8, lane, xr);
      mod_tile<16, false>(p, rc, 16 + id, 0, w, cu, lds);
      g_put(rc, 96 + id, tag, threadIdx.x == 0);
    }
    LOGS(1)
#pragma unroll
    for (int i = 0; i < 4; ++i) ln1_norm(xr[i]);
    if (W == 0) g_wait(rc, 64 + lane, 64, tag, bud);
    bar();
    ln1_rows<4>(rc, (32 + id) * 32 + W * 8, lane, xr);
    const Par q = get_par(rc, x, 128 + 2 * x, tag, true, lds, bud);
    drain(); bar();
    if (W == 0) st4(64u, rc, lane == 0 ? pgw(PG_H + (32 + id) * 128, q) : OOB, 0);
    LOGS(4)
    role_x2(p, rc, q, y, k, W, lds);
  }
  if (x > 3 && (r >= 24 || (r >= 8 && r < 16))) {
    const int crew = r < 16;
    if (crew) bar();
    if (k < 4) {
      load_d(mkrs(p.wd, 512 * 1792 * 2), (W * 448 + 8 * gb) * 1024 + (64 * ((y * 4 + k) & 7) + 4 * n) * 2);
      LOGS(1)
      GETTAG
      role_x4(p, rc, get_par(rc, x, 96 + (lane & 31), tag, true, lds, bud), y * 4 + k, y, crew, W, lds);
      if (blockIdx.x == 24 * 8 + 4 && threadIdx.x == 0) __hip_atomic_store((GAS u64*)p.ws, z + 1, __ATOMIC_RELAXED, __HIP_MEMORY_SCOPE_AGENT);
    } else if (4 * (k - 4) + y < 14) {
      const int f = 4 * (k - 4) + y;
      load_gu(mkrs(p.wg, 512 * 1792 * 2), mkrs(p.wu, 512 * 1792 * 2), lb + W * 8192, (32 * W + gb) * 3584 + f * 256 + n * 16, lb + gb * 2048 + 64 * W + 4 * n);
      LOGS(1)
      GETTAG
      role_x3(p, rc, get_par(rc, x, 96 + (lane & 31), tag, true, lds, bud), f, y, crew, W, lds);
    }
  }
  drain();
}

#define P_WM 5
#define P_WV 6
#define P_WP 3
#define P_WW1 2
#define P_WWV 0
#define P_WS 12
#define P_MAP 0
#define P_WC 28
#define P_RING 2566
extern "C" void kernel_launch(void* const* d_in, const int* in_sizes, int n_in, void* d_out, int out_size, void* d_ws, size_t ws_size, hipStream_t stream) {
  Prm p;
  p.x = (const u16*)d_in[0]; p.mem0 = (const u16*)d_in[1]; p.Wq = (const u16*)d_in[2]; p.Wk = (const u16*)d_in[3]; p.Wv = (const u16*)d_in[4]; p.Wo = (const u16*)d_in[5];
  p.wg = (const u16*)d_in[8]; p.wu = (const u16*)d_in[9]; p.wd = (const u16*)d_in[10]; p.c = (const u16*)d_in[11]; p.wm = (const u16*)d_in[12];
  p.out = (u16*)d_out;
  p.ws = (unsigned char*)(((size_t)d_ws + 2 * MB - 1) & ~(size_t)(2 * MB - 1));
  p.log = (unsigned*)p.ws; p.wM = P_WM; p.wV = P_WV; p.wP = P_WP; p.wW1 = P_WW1; p.wWV = P_WWV; p.wS = P_WS; p.map = P_MAP; p.wC = P_WC; p.ring = P_RING;
  p.pad = 0;
  (void)hipFuncSetAttribute((const void*)k_main, hipFuncAttributeMaxDynamicSharedMemorySize, LDS_BYTES);
  k_main<<<256, 256, LDS_BYTES, stream>>>(p);
}
```
